```python
import torch

def _softmax(x, axis=-1):
    return torch.softmax(x, dim=axis)
def _gelu(x):
    return torch.nn.functional.gelu(x, approximate="tanh")
def _silu(x):
    return torch.nn.functional.silu(x)
def _sigmoid(x):
    return torch.sigmoid(x)
def _layernorm(x, axis=-1, eps=1e-5):
    m = x.mean(dim=axis, keepdim=True)
    v = x.var(dim=axis, keepdim=True, unbiased=False)
    return (x - m) / torch.sqrt(v + eps)
def _scatter_add(tgt, idx, src, axis=0):
    return tgt.clone().index_add_(axis, idx, src)
def _segment_sum(src, seg, n):
    out = torch.zeros((n,) + src.shape[1:], dtype=src.dtype)
    return out.index_add_(0, seg, src)
def _pad(x, pw):
    flat = tuple(v for pair in reversed(pw) for v in pair)
    return torch.nn.functional.pad(x, flat)

kernel_name = 'gated_mix__grouped_kv_mqa__mha_block__prompt'

def setup_inputs():

    _w = (lambda a: (lambda t: t.to(torch.bfloat16) if t.is_floating_point() else t)(torch.as_tensor(a)))
    def _A_setup():
        B, S, D, H = 1, 2048, 512, 32
        Dh = D // H
        return {
            "x": _w((lambda *s: torch.randn(*s, dtype=torch.bfloat16) * 0.05)(B, S, D)),
            "wq": _w((lambda *s: torch.randn(*s, dtype=torch.bfloat16) * 1.6)(D, H * Dh)),
            "wk": _w((lambda *s: torch.randn(*s, dtype=torch.bfloat16) * 1.6)(D, Dh)),
            "wv": _w((lambda *s: torch.randn(*s, dtype=torch.bfloat16) * 0.8)(D, Dh)),
            "wo": _w((lambda *s: torch.randn(*s, dtype=torch.bfloat16) * 0.8)(H * Dh, D)),
        }
    _A_in = _A_setup()

    def _B_setup():
        B, S, D, H = 1, 2048, 512, 32
        return {
            "x": _w((lambda *s: torch.randn(*s, dtype=torch.bfloat16) * 0.05)(B, S, D)),
            "wqkv": _w((lambda *s: torch.randn(*s, dtype=torch.bfloat16) * 3.2)(D, 3 * D)),
            "wo": _w((lambda *s: torch.randn(*s, dtype=torch.bfloat16) * 0.05)(D, D)),
        }
    _B_in = _B_setup()

    return {
        "A_x": _A_in["x"],
        "A_wq": _A_in["wq"],
        "A_wk": _A_in["wk"],
        "A_wv": _A_in["wv"],
        "A_wo": _A_in["wo"],
        "B_x": _B_in["x"],
        "B_wqkv": _B_in["wqkv"],
        "B_wo": _B_in["wo"],
        "gate_w": _w((lambda *s: torch.randn(*s, dtype=torch.bfloat16) * 0.1)(512) * 16.0),
    }

def reference(A_x, A_wq, A_wk, A_wv, A_wo, B_x, B_wqkv, B_wo, gate_w):
    def _A_ref(x, wq, wk, wv, wo):
        B, S, D = x.shape
        H = 32
        Dh = D // H
        q = (lambda x, *d: x.permute(*d))((x @ wq).reshape(B, S, H, Dh), 0, 2, 1, 3)
        k = x @ wk
        v = x @ wv
        scores = torch.einsum("bhqd,bkd->bhqk", q, k) * (Dh ** -0.5)
        p = _softmax(scores, axis=-1)
        o = torch.einsum("bhqk,bkd->bhqd", p, v)
        return (lambda x, *d: x.permute(*d))(o, 0, 2, 1, 3).reshape(B, S, H * Dh) @ wo

    def _B_ref(x, wqkv, wo):
        B, S, D = x.shape
        H = 32
        Dh = D // H
        qkv = (lambda x, *d: x.permute(*d))((x @ wqkv).reshape(B, S, 3, H, Dh), 2, 0, 3, 1, 4)
        q, k, v = qkv[0], qkv[1], qkv[2]
        scores = torch.einsum("bhqd,bhkd->bhqk", q, k) * (Dh ** -0.5)
        p = _softmax(scores, axis=-1)
        o = torch.einsum("bhqk,bhkd->bhqd", p, v)
        return (lambda x, *d: x.permute(*d))(o, 0, 2, 1, 3).reshape(B, S, D) @ wo

    x = A_x
    g = _sigmoid((x * gate_w).sum(-1, keepdims=True))
    a = _A_ref(x, A_wq, A_wk, A_wv, A_wo)
    b = _B_ref(x, B_wqkv, B_wo)
    return g * a + (1 - g) * b
```

```cpp
#include <hip/hip_runtime.h>
#include <hip/hip_bf16.h>
#include <stdint.h>

typedef short    bf16x8 __attribute__((ext_vector_type(8)));
typedef short    s16x4  __attribute__((ext_vector_type(4)));
typedef float    f32x16 __attribute__((ext_vector_type(16)));
typedef float    f32x4  __attribute__((ext_vector_type(4)));
typedef float    f32x2  __attribute__((ext_vector_type(2)));
typedef unsigned u32x4  __attribute__((ext_vector_type(4)));
typedef __bf16   bf16x2v __attribute__((ext_vector_type(2)));

#define DI __device__ __forceinline__
#define MFMA32(a, b, c) __builtin_amdgcn_mfma_f32_32x32x16_bf16((a), (b), (c), 0, 0, 0)
#define LDS_AS __attribute__((address_space(3)))

constexpr size_t WS_Q  = 0;
constexpr size_t WS_KV = WS_Q + (size_t)64 * 65536;
constexpr size_t WS_O  = WS_KV + (size_t)34 * 131072;
constexpr size_t WS_WO = WS_O + (size_t)64 * 65536;
constexpr size_t WS_G  = WS_WO + (size_t)1024 * 1024;

constexpr float QSCALE = 0.25f * 1.4426950408889634f;

DI unsigned pk_bf16(float lo, float hi) {
    f32x2 v = {lo, hi};
    bf16x2v r = __builtin_convertvector(v, bf16x2v);
    return __builtin_bit_cast(unsigned, r);
}
DI float bf2f(unsigned short u) { return __uint_as_float(((unsigned)u) << 16); }

DI void glds16(const void* g, unsigned lds_base) {
    unsigned sv;
    asm volatile("s_mov_b32 %0, m0\n\ts_mov_b32 m0, %2\n\ts_nop 0\n\tglobal_load_lds_dwordx4 %1, off\n\ts_mov_b32 m0, %0"
                 : "=&s"(sv) : "v"(g), "s"(lds_base) : "memory");
}
#define WAIT_BAR(N) asm volatile("s_waitcnt vmcnt(" #N ") lgkmcnt(0)\n\ts_barrier" ::: "memory")

DI bf16x8 lds_b128(unsigned addr) { return *(const LDS_AS bf16x8*)(uintptr_t)addr; }
DI s16x4 lds_tr(unsigned addr) {
    return __builtin_bit_cast(s16x4, __builtin_amdgcn_ds_read_tr16_b64_v4i16((LDS_AS s16x4*)(uintptr_t)addr));
}
DI bf16x8 cat8(s16x4 a, s16x4 b) { return __builtin_shufflevector(a, b, 0, 1, 2, 3, 4, 5, 6, 7); }

DI u32x4 pack8(const f32x16& a, int base, float sc) {
    u32x4 r;
    r[0] = pk_bf16(a[base + 0] * sc, a[base + 1] * sc);
    r[1] = pk_bf16(a[base + 2] * sc, a[base + 3] * sc);
    r[2] = pk_bf16(a[base + 4] * sc, a[base + 5] * sc);
    r[3] = pk_bf16(a[base + 6] * sc, a[base + 7] * sc);
    return r;
}

constexpr int P_XS = 0, P_WS = 16384, P_WX = 32768, P_STAGE = 36864, P_LDS = 4 * P_STAGE;

template <bool ISV, bool EXTRA>
DI void proj_body(const unsigned short* __restrict__ x, const unsigned short* __restrict__ wsrc, int ldw,
                  const unsigned short* __restrict__ wk, const unsigned short* __restrict__ wv,
                  char* __restrict__ ws, int mt, int nt, int xtt, unsigned lds0, char* smem) {
    const int tid = threadIdx.x, lane = tid & 63;
    const int w = __builtin_amdgcn_readfirstlane(tid >> 6);
    const int wm = w >> 1, wn = w & 1;
    const int r = lane & 31, h = lane >> 5;

    const unsigned short* xsrc[2];
#pragma unroll
    for (int par = 0; par < 2; ++par) {
        const int row = 32 * w + 8 * par + (lane >> 3);
        const int ch = (lane & 7) ^ ((row >> 1) & 7);
        xsrc[par] = x + (size_t)(mt * 128 + row) * 512 + ch * 8;
    }
    const unsigned short* wsr;
    {
        const int krow = 16 * w + (lane >> 4);
        const int ch = (lane & 15) ^ (((lane >> 4) & 3) << 2);
        wsr = wsrc + (size_t)krow * ldw + ch * 8;
    }
    const unsigned short* esr = nullptr;
    if (EXTRA) {
        const int krow = 16 * w + (lane >> 2);
        esr = ((lane & 2) ? wv : wk) + krow * 16 + (lane & 1) * 8;
    }
    auto issue = [&](int c) {
        const unsigned st = lds0 + (c & 3) * P_STAGE;
        glds16(xsrc[0] + c * 64, st + P_XS + w * 4096);
        glds16(xsrc[1] + c * 64, st + P_XS + w * 4096 + 1024);
        glds16(xsrc[0] + c * 64 + 16 * 512, st + P_XS + w * 4096 + 2048);
        glds16(xsrc[1] + c * 64 + 16 * 512, st + P_XS + w * 4096 + 3072);
#pragma unroll
        for (int i = 0; i < 4; ++i) glds16(wsr + (size_t)(c * 64 + 4 * i) * ldw, st + P_WS + w * 4096 + i * 1024);
        if (EXTRA) glds16(esr + c * 64 * 16, st + P_WX + w * 1024);
    };
    issue(0); issue(1); issue(2); issue(3);

    unsigned xa[2][4];
#pragma unroll
    for (int tt = 0; tt < 2; ++tt) {
        const int row = wm * 64 + tt * 32 + r;
#pragma unroll
        for (int ks = 0; ks < 4; ++ks) xa[tt][ks] = lds0 + P_XS + row * 128 + (((2 * ks + h) ^ ((row >> 1) & 7)) << 4);
    }
    const int q4 = (lane & 15) >> 2, p4 = lane & 3, g1 = (lane >> 4) & 1;
    unsigned wa[2];
#pragma unroll
    for (int ct = 0; ct < 2; ++ct)
        wa[ct] = lds0 + P_WS + (8 * h + q4) * 256 + ((4 * ((2 * wn + ct) ^ q4) + 2 * g1 + (p4 >> 1)) << 4) + (p4 & 1) * 8;
    unsigned ea = 0, exa = 0;
    if (EXTRA) {
        ea = lds0 + P_WX + (16 * w + 8 * h + q4) * 64 + g1 * 32 + p4 * 8;
        const int row = xtt * 32 + r;
        exa = lds0 + P_XS + row * 128 + (((2 * w + h) ^ ((row >> 1) & 7)) << 4);
    }

    f32x16 acc[2][2], acce;
#pragma unroll
    for (int i = 0; i < 16; ++i) { acc[0][0][i] = 0.f; acc[0][1][i] = 0.f; acc[1][0][i] = 0.f; acc[1][1][i] = 0.f; acce[i] = 0.f; }

#pragma unroll
    for (int c = 0; c < 8; ++c) {
        if (EXTRA) {
            if (c == 0) WAIT_BAR(27); else if (c <= 5) WAIT_BAR(18); else if (c == 6) WAIT_BAR(9); else WAIT_BAR(0);
        } else {
            if (c == 0) WAIT_BAR(24); else if (c <= 5) WAIT_BAR(16); else if (c == 6) WAIT_BAR(8); else WAIT_BAR(0);
        }
        if (c >= 1 && c <= 4) issue(c + 3);
        const unsigned so = (c & 3) * P_STAGE;
#pragma unroll
        for (int ks = 0; ks < 4; ++ks) {
            bf16x8 xf[2], wf[2];
#pragma unroll
            for (int tt = 0; tt < 2; ++tt) xf[tt] = lds_b128(xa[tt][ks] + so);
#pragma unroll
            for (int ct = 0; ct < 2; ++ct)
                wf[ct] = cat8(lds_tr(wa[ct] + so + ks * 4096), lds_tr(wa[ct] + so + ks * 4096 + 1024));
#pragma unroll
            for (int ct = 0; ct < 2; ++ct)
#pragma unroll
                for (int tt = 0; tt < 2; ++tt)
                    acc[ct][tt] = ISV ? MFMA32(xf[tt], wf[ct], acc[ct][tt]) : MFMA32(wf[ct], xf[tt], acc[ct][tt]);
        }
        if (EXTRA) {
            bf16x8 xe = lds_b128(exa + so);
            bf16x8 we = cat8(lds_tr(ea + so), lds_tr(ea + so + 256));
            acce = MFMA32(xe, we, acce);
        }
    }

#pragma unroll
    for (int ct = 0; ct < 2; ++ct)
#pragma unroll
        for (int tt = 0; tt < 2; ++tt) {
            const int tile = mt * 4 + wm * 2 + tt;
            const int hd0 = (nt & 3) * 8 + wn * 4 + ct * 2;
            if (ISV) {
                char* dst = ws + WS_KV + (size_t)(hd0 + (r >> 4)) * 131072 + tile * 2048 + 1024 + h * 256 + (r & 15) * 16;
                *(u32x4*)(dst) = pack8(acc[ct][tt], 0, 1.f);
                *(u32x4*)(dst + 512) = pack8(acc[ct][tt], 8, 1.f);
            } else if (nt < 8) {
                char* dst = ws + WS_Q + (size_t)((nt >> 2) * 32 + hd0) * 65536 + tile * 1024 + lane * 16;
                *(u32x4*)(dst) = pack8(acc[ct][tt], 0, QSCALE);
                *(u32x4*)(dst + 65536) = pack8(acc[ct][tt], 8, QSCALE);
            } else {
                char* dst = ws + WS_KV + (size_t)hd0 * 131072 + tile * 2048 + lane * 16;
                *(u32x4*)(dst) = pack8(acc[ct][tt], 0, 1.f);
                *(u32x4*)(dst + 131072) = pack8(acc[ct][tt], 8, 1.f);
            }
        }
    if (EXTRA) {
        float* red = (float*)smem;
#pragma unroll
        for (int i = 0; i < 16; ++i) red[w * 1056 + ((i & 3) + 8 * (i >> 2) + 4 * h) * 33 + r] = acce[i];
        __syncthreads();
        if (tid < 128) {
            const int tile = mt * 4 + xtt;
            float v[8];
            char* dst;
            if (tid < 64) {
#pragma unroll
                for (int j = 0; j < 8; ++j) {
                    const int idx = r * 33 + 8 * (j >> 2) + 4 * h + (j & 3);
                    v[j] = red[idx] + red[1056 + idx] + red[2112 + idx] + red[3168 + idx];
                }
                dst = ws + WS_KV + (size_t)32 * 131072 + tile * 2048 + lane * 16;
            } else {
                const int s = lane >> 5, hh = (lane >> 4) & 1, d = lane & 15;
#pragma unroll
                for (int j = 0; j < 8; ++j) {
                    const int idx = (16 * s + 8 * (j >> 2) + 4 * hh + (j & 3)) * 33 + 16 + d;
                    v[j] = red[idx] + red[1056 + idx] + red[2112 + idx] + red[3168 + idx];
                }
                dst = ws + WS_KV + (size_t)32 * 131072 + tile * 2048 + 1024 + lane * 16;
            }
            u32x4 o;
            o[0] = pk_bf16(v[0], v[1]); o[1] = pk_bf16(v[2], v[3]); o[2] = pk_bf16(v[4], v[5]); o[3] = pk_bf16(v[6], v[7]);
            *(u32x4*)dst = o;
        }
    }
}

__global__ __launch_bounds__(256, 1) void proj_kernel(const unsigned short* __restrict__ x, const unsigned short* __restrict__ wq,
                                                      const unsigned short* __restrict__ wk, const unsigned short* __restrict__ wv,
                                                      const unsigned short* __restrict__ wqkv, const unsigned short* __restrict__ gw,
                                                      char* __restrict__ ws) {
    extern __shared__ __attribute__((aligned(16))) char smem[];
    const unsigned lds0 = (unsigned)(uintptr_t)smem;
    const int b = blockIdx.x, xcd = b & 7, slot = b >> 3;
    const int mt = (xcd & 3) * 4 + (slot & 3), nt = (xcd >> 2) * 8 + (slot >> 2);
    const unsigned short* wsrc = (nt < 4) ? (wq + nt * 128) : (wqkv + (nt - 4) * 128);
    const int ldw = (nt < 4) ? 512 : 1536;
    const bool extra = (nt & 3) == 1;
    const int xtt = nt >> 2;

    const int tid = threadIdx.x;
    const int gtok = mt * 128 + nt * 8 + (tid >> 5);
    u32x4 gx0, gx1, gw0, gw1;
    {
        const unsigned short* px = x + (size_t)gtok * 512 + (tid & 31) * 16;
        const unsigned short* pw = gw + (tid & 31) * 16;
        asm volatile("global_load_dwordx4 %0, %4, off\n\tglobal_load_dwordx4 %1, %4, off offset:16\n\t"
                     "global_load_dwordx4 %2, %5, off\n\tglobal_load_dwordx4 %3, %5, off offset:16"
                     : "=&v"(gx0), "=&v"(gx1), "=&v"(gw0), "=&v"(gw1) : "v"(px), "v"(pw) : "memory");
    }
    if (nt >= 12) {
        if (extra) proj_body<true, true>(x, wsrc, ldw, wk, wv, ws, mt, nt, xtt, lds0, smem);
        else       proj_body<true, false>(x, wsrc, ldw, wk, wv, ws, mt, nt, xtt, lds0, smem);
    } else {
        if (extra) proj_body<false, true>(x, wsrc, ldw, wk, wv, ws, mt, nt, xtt, lds0, smem);
        else       proj_body<false, false>(x, wsrc, ldw, wk, wv, ws, mt, nt, xtt, lds0, smem);
    }
    asm volatile("" : "+v"(gx0), "+v"(gx1), "+v"(gw0), "+v"(gw1));
    float s = 0.f;
#pragma unroll
    for (int i = 0; i < 4; ++i) {
        s += __uint_as_float(gx0[i] << 16) * __uint_as_float(gw0[i] << 16);
        s += __uint_as_float(gx0[i] & 0xffff0000u) * __uint_as_float(gw0[i] & 0xffff0000u);
        s += __uint_as_float(gx1[i] << 16) * __uint_as_float(gw1[i] << 16);
        s += __uint_as_float(gx1[i] & 0xffff0000u) * __uint_as_float(gw1[i] & 0xffff0000u);
    }
#pragma unroll
    for (int o = 16; o >= 1; o >>= 1) s += __shfl_xor(s, o);
    if ((tid & 31) == 0) ((float*)(ws + WS_G))[gtok] = 1.f / (1.f + __expf(-s));
}

constexpr int A_KV = 0, A_CONST = 131072, A_Q = A_CONST + 8192, A_LDS = A_Q + 16384;

DI float max16(const f32x16& s) {
    float a = fmaxf(fmaxf(s[0], s[1]), s[2]), b = fmaxf(fmaxf(s[3], s[4]), s[5]);
    a = fmaxf(fmaxf(a, s[6]), s[7]);   b = fmaxf(fmaxf(b, s[8]), s[9]);
    a = fmaxf(fmaxf(a, s[10]), s[11]); b = fmaxf(fmaxf(b, s[12]), s[13]);
    a = fmaxf(fmaxf(a, s[14]), s[15]);
    return fmaxf(a, b);
}
DI float half_max(float m) {
    auto rr = __builtin_amdgcn_permlane32_swap(__float_as_uint(m), __float_as_uint(m), false, false);
    return fmaxf(__uint_as_float(rr[0]), __uint_as_float(rr[1]));
}

DI void attn_tiles(int t0, int ngrp, unsigned kaddr, unsigned vaddr, unsigned vstep, const bf16x8& qf, const f32x16& negm, f32x16& o) {
    unsigned ka = kaddr + t0 * 2048, va = vaddr + (t0 >> 2) * vstep;
#pragma unroll 1
    for (int g = 0; g < ngrp; ++g) {
#pragma unroll
        for (int u = 0; u < 4; ++u) {
            const bf16x8 kf = lds_b128(ka + u * 2048);
            const bf16x8 v0 = lds_b128(va + u * 2048);
            const bf16x8 v1 = lds_b128(va + u * 2048 + 512);
            const f32x16 s = MFMA32(kf, qf, negm);
            u32x4 p0, p1;
#pragma unroll
            for (int i = 0; i < 4; ++i) {
                p0[i] = pk_bf16(__builtin_amdgcn_exp2f(s[2 * i]), __builtin_amdgcn_exp2f(s[2 * i + 1]));
            }
#pragma unroll
            for (int i = 0; i < 4; ++i) {
                p1[i] = pk_bf16(__builtin_amdgcn_exp2f(s[8 + 2 * i]), __builtin_amdgcn_exp2f(s[8 + 2 * i + 1]));
            }
            o = MFMA32(v0, __builtin_bit_cast(bf16x8, p0), o);
            o = MFMA32(v1, __builtin_bit_cast(bf16x8, p1), o);
        }
        ka += 8192; va += vstep;
    }
}

__global__ __launch_bounds__(1024) void attn_kernel(const unsigned short* __restrict__ woA, const unsigned short* __restrict__ woB,
                                                    char* __restrict__ ws) {
    extern __shared__ __attribute__((aligned(16))) char smem[];
    const unsigned lds0 = (unsigned)(uintptr_t)smem;
    const int tid = threadIdx.x, lane = tid & 63;
    const int w = __builtin_amdgcn_readfirstlane(tid >> 6);
    const int r = lane & 31, h = lane >> 5;
    const int b = blockIdx.x, xcd = b & 7, slot = b >> 3;
    const int isB = (slot < 16);
    const int hd = xcd * 4 + ((slot & 15) >> 2);
    const int hh = isB ? 32 + hd : hd;
    const int kvh = isB ? hd : 32;
    const int tile = (slot & 3) * 16 + w;

    glds16(ws + WS_Q + (size_t)hh * 65536 + tile * 1024 + lane * 16, lds0 + A_Q + w * 1024);
    {
        const char* src = ws + WS_KV + (size_t)kvh * 131072 + w * 1024 + lane * 16;
#pragma unroll
        for (int i = 0; i < 8; ++i) glds16(src + i * 16384, lds0 + A_KV + i * 16384 + w * 1024);
    }
    if (tid < 512) {
        const unsigned one = ((tid & 15) == 0 || (tid & 15) == 4) ? 0x3f803f80u : 0u;
        u32x4 c = {one, one, one, one};
        *(u32x4*)(smem + A_CONST + tid * 16) = c;
    }
    const unsigned kaddr = lds0 + A_KV + lane * 16;
    const unsigned vaddr = (r < 16) ? (lds0 + A_KV + 1024 + h * 256 + r * 16) : (lds0 + A_CONST + (r - 16) * 16);
    const unsigned vstep = (r < 16) ? 8192u : 0u;

    WAIT_BAR(7);
    const bf16x8 qf = lds_b128(lds0 + A_Q + w * 1024 + lane * 16);
    f32x16 zero;
#pragma unroll
    for (int i = 0; i < 16; ++i) zero[i] = 0.f;
    f32x16 negm, o = zero;
    {
        const f32x16 s = MFMA32(lds_b128(kaddr), qf, zero);
        const float m = -half_max(max16(s));
#pragma unroll
        for (int i = 0; i < 16; ++i) negm[i] = m;
    }
    attn_tiles(0, 2, kaddr, vaddr, vstep, qf, negm, o);
    WAIT_BAR(0);

    unsigned short wv8[8];
    const int fr = b * 4 + w;
    if (w < 4) {
        const int kk = fr >> 4, ntile = fr & 15;
        const unsigned short* wsrc = ((kk < 32) ? woA : woB) + (size_t)(kk & 31) * 16 * 512 + ntile * 32 + r;
#pragma unroll
        for (int j = 0; j < 8; ++j) wv8[j] = wsrc[(8 * (j >> 2) + 4 * h + (j & 3)) * 512];
    }
    const float gate = ((const float*)(ws + WS_G))[tile * 32 + r];

    attn_tiles(8, 14, kaddr, vaddr, vstep, qf, negm, o);

    if (w < 4) {
        u32x4 pk;
#pragma unroll
        for (int j = 0; j < 4; ++j) pk[j] = (unsigned)wv8[2 * j] | ((unsigned)wv8[2 * j + 1] << 16);
        *(u32x4*)(ws + WS_WO + (size_t)fr * 1024 + lane * 16) = pk;
    }

    {
        bool bad = !(o[8] < 1.8446744073709552e19f);
#pragma unroll
        for (int i = 0; i < 8; ++i) bad = bad || !(fabsf(o[i]) <= 3.0e38f);
        if (__any(bad)) {
            float m = -INFINITY;
#pragma unroll 1
            for (int t = 0; t < 64; ++t) {
                const f32x16 s = MFMA32(lds_b128(kaddr + t * 2048), qf, zero);
                m = fmaxf(m, max16(s));
            }
            m = -half_max(m);
#pragma unroll
            for (int i = 0; i < 16; ++i) negm[i] = m;
            o = zero;
            attn_tiles(0, 16, kaddr, vaddr, vstep, qf, negm, o);
        }
    }
    const float sc = (isB ? 1.f - gate : gate) / o[8];
    *(u32x4*)(ws + WS_O + (size_t)tile * 65536 + hh * 1024 + lane * 16) = pack8(o, 0, sc);
}

constexpr int O_ROW = 272, O_PART = 64 * O_ROW, O_LDS = 8 * O_PART;

__global__ __launch_bounds__(512) void out_kernel(const char* __restrict__ ws, unsigned short* __restrict__ out) {
    extern __shared__ __attribute__((aligned(16))) char smem[];
    const int tid = threadIdx.x, lane = tid & 63;
    const int w = __builtin_amdgcn_readfirstlane(tid >> 6);
    const int r = lane & 31, h = lane >> 5;
    const int b = blockIdx.x, xcd = b & 7, slot = b >> 3;
    const int mt = xcd * 4 + (slot & 3), nt = slot >> 2;

    const char* osrc = ws + WS_O + (size_t)(mt * 2) * 65536 + (w * 8) * 1024 + lane * 16;
    const char* wsrc = ws + WS_WO + (size_t)(w * 8) * 16384 + (nt * 2) * 1024 + lane * 16;
    bf16x8 of[8][2], wf[8][2];
#pragma unroll
    for (int k = 0; k < 8; ++k) {
        of[k][0] = *(const bf16x8*)(osrc + k * 1024);
        of[k][1] = *(const bf16x8*)(osrc + 65536 + k * 1024);
        wf[k][0] = *(const bf16x8*)(wsrc + k * 16384);
        wf[k][1] = *(const bf16x8*)(wsrc + k * 16384 + 1024);
    }
    f32x16 acc[2][2];
#pragma unroll
    for (int i = 0; i < 16; ++i) { acc[0][0][i] = 0.f; acc[0][1][i] = 0.f; acc[1][0][i] = 0.f; acc[1][1][i] = 0.f; }
#pragma unroll
    for (int k = 0; k < 8; ++k)
#pragma unroll
        for (int ct = 0; ct < 2; ++ct)
#pragma unroll
            for (int tt = 0; tt < 2; ++tt) acc[ct][tt] = MFMA32(wf[k][ct], of[k][tt], acc[ct][tt]);

#pragma unroll
    for (int ct = 0; ct < 2; ++ct)
#pragma unroll
        for (int tt = 0; tt < 2; ++tt)
#pragma unroll
            for (int g = 0; g < 4; ++g) {
                f32x4 v = {acc[ct][tt][4 * g], acc[ct][tt][4 * g + 1], acc[ct][tt][4 * g + 2], acc[ct][tt][4 * g + 3]};
                *(f32x4*)(smem + w * O_PART + (tt * 32 + r) * O_ROW + (ct * 32 + 8 * g + 4 * h) * 4) = v;
            }
    __syncthreads();
    const int tok = tid >> 3, n8 = tid & 7;
    f32x4 s0 = {0.f, 0.f, 0.f, 0.f}, s1 = {0.f, 0.f, 0.f, 0.f};
#pragma unroll
    for (int p = 0; p < 8; ++p) {
        s0 += *(const f32x4*)(smem + p * O_PART + tok * O_ROW + n8 * 32);
        s1 += *(const f32x4*)(smem + p * O_PART + tok * O_ROW + n8 * 32 + 16);
    }
    u32x4 res;
    res[0] = pk_bf16(s0[0], s0[1]); res[1] = pk_bf16(s0[2], s0[3]); res[2] = pk_bf16(s1[0], s1[1]); res[3] = pk_bf16(s1[2], s1[3]);
    *(u32x4*)(out + (size_t)(mt * 64 + tok) * 512 + nt * 64 + n8 * 8) = res;
}

extern "C" void kernel_launch(void* const* d_in, const int* in_sizes, int n_in, void* d_out, int out_size, void* d_ws,
                              size_t ws_size, hipStream_t stream) {
    const unsigned short* x    = (const unsigned short*)d_in[0];
    const unsigned short* wq   = (const unsigned short*)d_in[1];
    const unsigned short* wk   = (const unsigned short*)d_in[2];
    const unsigned short* wv   = (const unsigned short*)d_in[3];
    const unsigned short* woA  = (const unsigned short*)d_in[4];
    const unsigned short* wqkv = (const unsigned short*)d_in[6];
    const unsigned short* woB  = (const unsigned short*)d_in[7];
    const unsigned short* gw   = (const unsigned short*)d_in[8];
    char* ws = (char*)d_ws;
    (void)hipFuncSetAttribute((const void*)proj_kernel, hipFuncAttributeMaxDynamicSharedMemorySize, P_LDS);
    (void)hipFuncSetAttribute((const void*)attn_kernel, hipFuncAttributeMaxDynamicSharedMemorySize, A_LDS);
    (void)hipFuncSetAttribute((const void*)out_kernel, hipFuncAttributeMaxDynamicSharedMemorySize, O_LDS);
    proj_kernel<<<256, 256, P_LDS, stream>>>(x, wq, wk, wv, wqkv, gw, ws);
    attn_kernel<<<256, 1024, A_LDS, stream>>>(woA, woB, ws);
    out_kernel<<<256, 512, O_LDS, stream>>>(ws, (unsigned short*)d_out);
}
```

```cpp
#include <hip/hip_runtime.h>
#include <hip/hip_bf16.h>
#include <stdint.h>

typedef short    bf16x8 __attribute__((ext_vector_type(8)));
typedef short    s16x4  __attribute__((ext_vector_type(4)));
typedef float    f32x16 __attribute__((ext_vector_type(16)));
typedef float    f32x4  __attribute__((ext_vector_type(4)));
typedef float    f32x2  __attribute__((ext_vector_type(2)));
typedef unsigned u32x4  __attribute__((ext_vector_type(4)));
typedef __bf16   bf16x2v __attribute__((ext_vector_type(2)));

#define DI __device__ __forceinline__
#define MFMA32(a, b, c) __builtin_amdgcn_mfma_f32_32x32x16_bf16((a), (b), (c), 0, 0, 0)
#define LDS_AS __attribute__((address_space(3)))

constexpr size_t WS_Q  = 0;
constexpr size_t WS_KV = WS_Q + (size_t)64 * 65536;
constexpr size_t WS_O  = WS_KV + (size_t)34 * 131072;
constexpr size_t WS_WO = WS_O + (size_t)64 * 65536;
constexpr size_t WS_G  = WS_WO + (size_t)1024 * 1024;

#define ST(k, i)
constexpr float QSCALE = 0.25f * 1.4426950408889634f;

DI unsigned pk_bf16(float lo, float hi) {
    f32x2 v = {lo, hi};
    bf16x2v r = __builtin_convertvector(v, bf16x2v);
    return __builtin_bit_cast(unsigned, r);
}
DI float bf2f(unsigned short u) { return __uint_as_float(((unsigned)u) << 16); }

DI void glds16(const void* g, unsigned lds_base) {
    unsigned sv;
    asm volatile("s_mov_b32 %0, m0\n\ts_mov_b32 m0, %2\n\ts_nop 0\n\tglobal_load_lds_dwordx4 %1, off\n\ts_mov_b32 m0, %0"
                 : "=&s"(sv) : "v"(g), "s"(lds_base) : "memory");
}
#define WAIT_BAR(N) asm volatile("s_waitcnt vmcnt(" #N ") lgkmcnt(0)\n\ts_barrier" ::: "memory")

DI bf16x8 lds_b128(unsigned addr) { return *(const LDS_AS bf16x8*)(uintptr_t)addr; }
DI s16x4 lds_tr(unsigned addr) {
    return __builtin_bit_cast(s16x4, __builtin_amdgcn_ds_read_tr16_b64_v4i16((LDS_AS s16x4*)(uintptr_t)addr));
}
DI bf16x8 cat8(s16x4 a, s16x4 b) { return __builtin_shufflevector(a, b, 0, 1, 2, 3, 4, 5, 6, 7); }

DI u32x4 pack8(const f32x16& a, int base, float sc) {
    u32x4 r;
    r[0] = pk_bf16(a[base + 0] * sc, a[base + 1] * sc);
    r[1] = pk_bf16(a[base + 2] * sc, a[base + 3] * sc);
    r[2] = pk_bf16(a[base + 4] * sc, a[base + 5] * sc);
    r[3] = pk_bf16(a[base + 6] * sc, a[base + 7] * sc);
    return r;
}

constexpr int P_XS = 0, P_WS = 16384, P_WX = 32768, P_STAGE = 36864, P_LDS = 4 * P_STAGE;

template <bool ISV, bool EXTRA>
DI void proj_body(const unsigned short* __restrict__ x, const unsigned short* __restrict__ wsrc, int ldw,
                  const unsigned short* __restrict__ wk, const unsigned short* __restrict__ wv,
                  char* __restrict__ ws, int mt, int nt, int xtt, unsigned lds0, char* smem) {
    const int tid = threadIdx.x, lane = tid & 63;
    const int w = __builtin_amdgcn_readfirstlane(tid >> 6);
    const int wm = w >> 1, wn = w & 1;
    const int r = lane & 31, h = lane >> 5;

    const unsigned short* xsrc[2];
#pragma unroll
    for (int par = 0; par < 2; ++par) {
        const int row = 32 * w + 8 * par + (lane >> 3);
        const int ch = (lane & 7) ^ ((row >> 1) & 7);
        xsrc[par] = x + (size_t)(mt * 128 + row) * 512 + ch * 8;
    }
    const unsigned short* wsr;
    {
        const int krow = 16 * w + (lane >> 4);
        const int ch = (lane & 15) ^ (((lane >> 4) & 3) << 2);
        wsr = wsrc + (size_t)krow * ldw + ch * 8;
    }
    const unsigned short* esr = nullptr;
    if (EXTRA) {
        const int krow = 16 * w + (lane >> 2);
        esr = ((lane & 2) ? wv : wk) + krow * 16 + (lane & 1) * 8;
    }
    auto issue = [&](int c) {
        const unsigned st = lds0 + (c & 3) * P_STAGE;
        glds16(xsrc[0] + c * 64, st + P_XS + w * 4096);
        glds16(xsrc[1] + c * 64, st + P_XS + w * 4096 + 1024);
        glds16(xsrc[0] + c * 64 + 16 * 512, st + P_XS + w * 4096 + 2048);
        glds16(xsrc[1] + c * 64 + 16 * 512, st + P_XS + w * 4096 + 3072);
#pragma unroll
        for (int i = 0; i < 4; ++i) glds16(wsr + (size_t)(c * 64 + 4 * i) * ldw, st + P_WS + w * 4096 + i * 1024);
        if (EXTRA) glds16(esr + c * 64 * 16, st + P_WX + w * 1024);
    };
    issue(0); issue(1); issue(2); issue(3);

    unsigned xa[2][4];
#pragma unroll
    for (int tt = 0; tt < 2; ++tt) {
        const int row = wm * 64 + tt * 32 + r;
#pragma unroll
        for (int ks = 0; ks < 4; ++ks) xa[tt][ks] = lds0 + P_XS + row * 128 + (((2 * ks + h) ^ ((row >> 1) & 7)) << 4);
    }
    const int q4 = (lane & 15) >> 2, p4 = lane & 3, g1 = (lane >> 4) & 1;
    unsigned wa[2];
#pragma unroll
    for (int ct = 0; ct < 2; ++ct)
        wa[ct] = lds0 + P_WS + (8 * h + q4) * 256 + ((4 * ((2 * wn + ct) ^ q4) + 2 * g1 + (p4 >> 1)) << 4) + (p4 & 1) * 8;
    unsigned ea = 0, exa = 0;
    if (EXTRA) {
        ea = lds0 + P_WX + (16 * w + 8 * h + q4) * 64 + g1 * 32 + p4 * 8;
        const int row = xtt * 32 + r;
        exa = lds0 + P_XS + row * 128 + (((2 * w + h) ^ ((row >> 1) & 7)) << 4);
    }

    f32x16 acc[2][2], acce;
#pragma unroll
    for (int i = 0; i < 16; ++i) { acc[0][0][i] = 0.f; acc[0][1][i] = 0.f; acc[1][0][i] = 0.f; acc[1][1][i] = 0.f; acce[i] = 0.f; }

#pragma unroll
    for (int c = 0; c < 8; ++c) {
        if (EXTRA) {
            if (c == 0) WAIT_BAR(27); else if (c <= 5) WAIT_BAR(18); else if (c == 6) WAIT_BAR(9); else WAIT_BAR(0);
        } else {
            if (c == 0) WAIT_BAR(24); else if (c <= 5) WAIT_BAR(16); else if (c == 6) WAIT_BAR(8); else WAIT_BAR(0);
        }
        if (c == 0) ST(0, 1);
        if (c == 1) ST(0, 2);
        if (c == 4) ST(0, 3);
        if (c == 7) ST(0, 4);
        if (c >= 1 && c <= 4) issue(c + 3);
        const unsigned so = (c & 3) * P_STAGE;
#pragma unroll
        for (int ks = 0; ks < 4; ++ks) {
            bf16x8 xf[2], wf[2];
#pragma unroll
            for (int tt = 0; tt < 2; ++tt) xf[tt] = lds_b128(xa[tt][ks] + so);
#pragma unroll
            for (int ct = 0; ct < 2; ++ct)
                wf[ct] = cat8(lds_tr(wa[ct] + so + ks * 4096), lds_tr(wa[ct] + so + ks * 4096 + 1024));
#pragma unroll
            for (int ct = 0; ct < 2; ++ct)
#pragma unroll
                for (int tt = 0; tt < 2; ++tt)
                    acc[ct][tt] = ISV ? MFMA32(xf[tt], wf[ct], acc[ct][tt]) : MFMA32(wf[ct], xf[tt], acc[ct][tt]);
        }
        if (EXTRA) {
            bf16x8 xe = lds_b128(exa + so);
            bf16x8 we = cat8(lds_tr(ea + so), lds_tr(ea + so + 256));
            acce = MFMA32(xe, we, acce);
        }
    }

    ST(0, 5);
#pragma unroll
    for (int ct = 0; ct < 2; ++ct)
#pragma unroll
        for (int tt = 0; tt < 2; ++tt) {
            const int tile = mt * 4 + wm * 2 + tt;
            const int hd0 = (nt & 3) * 8 + wn * 4 + ct * 2;
            if (ISV) {
                char* dst = ws + WS_KV + (size_t)(hd0 + (r >> 4)) * 131072 + tile * 2048 + 1024 + h * 256 + (r & 15) * 16;
                *(u32x4*)(dst) = pack8(acc[ct][tt], 0, 1.f);
                *(u32x4*)(dst + 512) = pack8(acc[ct][tt], 8, 1.f);
            } else if (nt < 8) {
                char* dst = ws + WS_Q + (size_t)((nt >> 2) * 32 + hd0) * 65536 + tile * 1024 + lane * 16;
                *(u32x4*)(dst) = pack8(acc[ct][tt], 0, QSCALE);
                *(u32x4*)(dst + 65536) = pack8(acc[ct][tt], 8, QSCALE);
            } else {
                char* dst = ws + WS_KV + (size_t)hd0 * 131072 + tile * 2048 + lane * 16;
                *(u32x4*)(dst) = pack8(acc[ct][tt], 0, 1.f);
                *(u32x4*)(dst + 131072) = pack8(acc[ct][tt], 8, 1.f);
            }
        }
    if (EXTRA) {
        float* red = (float*)smem;
#pragma unroll
        for (int i = 0; i < 16; ++i) red[w * 1056 + ((i & 3) + 8 * (i >> 2) + 4 * h) * 33 + r] = acce[i];
        __syncthreads();
        if (tid < 128) {
            const int tile = mt * 4 + xtt;
            float v[8];
            char* dst;
            if (tid < 64) {
#pragma unroll
                for (int j = 0; j < 8; ++j) {
                    const int idx = r * 33 + 8 * (j >> 2) + 4 * h + (j & 3);
                    v[j] = red[idx] + red[1056 + idx] + red[2112 + idx] + red[3168 + idx];
                }
                dst = ws + WS_KV + (size_t)32 * 131072 + tile * 2048 + lane * 16;
            } else {
                const int s = lane >> 5, hh = (lane >> 4) & 1, d = lane & 15;
#pragma unroll
                for (int j = 0; j < 8; ++j) {
                    const int idx = (16 * s + 8 * (j >> 2) + 4 * hh + (j & 3)) * 33 + 16 + d;
                    v[j] = red[idx] + red[1056 + idx] + red[2112 + idx] + red[3168 + idx];
                }
                dst = ws + WS_KV + (size_t)32 * 131072 + tile * 2048 + 1024 + lane * 16;
            }
            u32x4 o;
            o[0] = pk_bf16(v[0], v[1]); o[1] = pk_bf16(v[2], v[3]); o[2] = pk_bf16(v[4], v[5]); o[3] = pk_bf16(v[6], v[7]);
            *(u32x4*)dst = o;
        }
    }
}

__global__ __launch_bounds__(256, 1) void proj_kernel(const unsigned short* __restrict__ x, const unsigned short* __restrict__ wq,
                                                      const unsigned short* __restrict__ wk, const unsigned short* __restrict__ wv,
                                                      const unsigned short* __restrict__ wqkv, const unsigned short* __restrict__ gw,
                                                      char* __restrict__ ws) {
    extern __shared__ __attribute__((aligned(16))) char smem[];
    const unsigned lds0 = (unsigned)(uintptr_t)smem;
    const int b = blockIdx.x, xcd = b & 7, slot = b >> 3;
    const int mt = (xcd & 3) * 4 + (slot & 3), nt = (xcd >> 2) * 8 + (slot >> 2);
    const unsigned short* wsrc = (nt < 4) ? (wq + nt * 128) : (wqkv + (nt - 4) * 128);
    const int ldw = (nt < 4) ? 512 : 1536;
    const bool extra = (nt & 3) == 1;
    const int xtt = nt >> 2;

    ST(0, 0);
    const int tid = threadIdx.x;
    const int gtok = mt * 128 + nt * 8 + (tid >> 5);
    u32x4 gx0, gx1, gw0, gw1;
    {
        const unsigned short* px = x + (size_t)gtok * 512 + (tid & 31) * 16;
        const unsigned short* pw = gw + (tid & 31) * 16;
        asm volatile("global_load_dwordx4 %0, %4, off\n\tglobal_load_dwordx4 %1, %4, off offset:16\n\t"
                     "global_load_dwordx4 %2, %5, off\n\tglobal_load_dwordx4 %3, %5, off offset:16"
                     : "=&v"(gx0), "=&v"(gx1), "=&v"(gw0), "=&v"(gw1) : "v"(px), "v"(pw) : "memory");
    }
    if (nt >= 12) {
        if (extra) proj_body<true, true>(x, wsrc, ldw, wk, wv, ws, mt, nt, xtt, lds0, smem);
        else       proj_body<true, false>(x, wsrc, ldw, wk, wv, ws, mt, nt, xtt, lds0, smem);
    } else {
        if (extra) proj_body<false, true>(x, wsrc, ldw, wk, wv, ws, mt, nt, xtt, lds0, smem);
        else       proj_body<false, false>(x, wsrc, ldw, wk, wv, ws, mt, nt, xtt, lds0, smem);
    }
    asm volatile("" : "+v"(gx0), "+v"(gx1), "+v"(gw0), "+v"(gw1));
    float s = 0.f;
#pragma unroll
    for (int i = 0; i < 4; ++i) {
        s += __uint_as_float(gx0[i] << 16) * __uint_as_float(gw0[i] << 16);
        s += __uint_as_float(gx0[i] & 0xffff0000u) * __uint_as_float(gw0[i] & 0xffff0000u);
        s += __uint_as_float(gx1[i] << 16) * __uint_as_float(gw1[i] << 16);
        s += __uint_as_float(gx1[i] & 0xffff0000u) * __uint_as_float(gw1[i] & 0xffff0000u);
    }
#pragma unroll
    for (int o = 16; o >= 1; o >>= 1) s += __shfl_xor(s, o);
    if ((tid & 31) == 0) ((float*)(ws + WS_G))[gtok] = 1.f / (1.f + __expf(-s));
    ST(0, 6);
}

constexpr int A_KV = 0, A_CONST = 131072, A_LDS = A_CONST + 8192;

DI float max16(const f32x16& s) {
    float a = fmaxf(fmaxf(s[0], s[1]), s[2]), b = fmaxf(fmaxf(s[3], s[4]), s[5]);
    a = fmaxf(fmaxf(a, s[6]), s[7]);   b = fmaxf(fmaxf(b, s[8]), s[9]);
    a = fmaxf(fmaxf(a, s[10]), s[11]); b = fmaxf(fmaxf(b, s[12]), s[13]);
    a = fmaxf(fmaxf(a, s[14]), s[15]);
    return fmaxf(a, b);
}
DI float half_max(float m) {
    auto rr = __builtin_amdgcn_permlane32_swap(__float_as_uint(m), __float_as_uint(m), false, false);
    return fmaxf(__uint_as_float(rr[0]), __uint_as_float(rr[1]));
}

#define R_SA "192"
#define R_SB "208"
#define EX(X, i) "v_exp_f32 v[" X "+" #i "], v[" X "+" #i "]\n\t"
#define CV(X, j, a, b) "v_cvt_pk_bf16_f32 v[224+" #j "], v[" X "+" #a "], v[" X "+" #b "]\n\t"
#define HALF(X, Y, OX, OY, NY, QY, WA, RA, WB, RB, WC, RC)                                        \
    EX(X, 0) EX(X, 1) WA                                                                          \
    "v_mfma_f32_32x32x16_bf16 " OY ", %[v1], v[228:231], " OY "\n\t" RA                           \
    EX(X, 2) EX(X, 3) CV(X, 0, 0, 1) EX(X, 4) EX(X, 5) CV(X, 1, 2, 3) WB                          \
    "v_mfma_f32_32x32x16_bf16 v[" Y ":" Y "+15], %[kf], " QY ", " NY "\n\t" RB                    \
    EX(X, 6) EX(X, 7) CV(X, 2, 4, 5) EX(X, 8) EX(X, 9) CV(X, 3, 6, 7) EX(X, 10) EX(X, 11) WC      \
    "v_mfma_f32_32x32x16_bf16 " OX ", %[v0], v[224:227], " OX "\n\t" RC                           \
    CV(X, 4, 8, 9) EX(X, 12) EX(X, 13) CV(X, 5, 10, 11) EX(X, 14) EX(X, 15) CV(X, 6, 12, 13) CV(X, 7, 14, 15)
#define TILE(OFF_V1, OFF_KN, OFF_V0N)                                                             \
    HALF(R_SA, R_SB, "%[oa]", "%[ob]", "%[nb]", "%[qb]",                                          \
         "", "ds_read_b128 %[v1], %[va] offset:" OFF_V1 "\n\t",                                   \
         "", "ds_read_b128 %[kf], %[ka] offset:" OFF_KN "\n\t",                                   \
         "s_waitcnt lgkmcnt(2)\n\t", "")                                                          \
    HALF(R_SB, R_SA, "%[ob]", "%[oa]", "%[na]", "%[qa]",                                          \
         "s_waitcnt lgkmcnt(1)\n\t", "",                                                          \
         "s_waitcnt lgkmcnt(0)\n\t", "",                                                          \
         "", "ds_read_b128 %[v0], %[va] offset:" OFF_V0N "\n\t")

DI void attn_main(unsigned ka, unsigned va, unsigned vstep, const bf16x8& qa, const bf16x8& qb, const f32x16& na, const f32x16& nb,
                  f32x16& oa, f32x16& ob, int sync1, int sync2) {
    bf16x8 kf, v0, v1;
    int cnt = 0;
    asm volatile(
        "ds_read_b128 %[kf], %[ka]\n\t"
        "ds_read_b128 %[v1], %[va]\n\t"
        "ds_read_b128 %[v0], %[va]\n\t"
        "v_mov_b32 v228, 0\n\tv_mov_b32 v229, 0\n\tv_mov_b32 v230, 0\n\tv_mov_b32 v231, 0\n\t"
        "s_waitcnt lgkmcnt(0)\n\t"
        "v_mfma_f32_32x32x16_bf16 v[192:207], %[kf], %[qa], %[na]\n\t"
        "s_nop 7\n\ts_nop 7\n\t"
        "L_loop_%=:\n\t"
        "s_cmp_lg_u32 %[cnt], %[s1]\n\t"
        "s_cbranch_scc1 L_n1_%=\n\t"
        "s_waitcnt vmcnt(12)\n\t"
        "s_barrier\n\t"
        "L_n1_%=:\n\t"
        "s_cmp_lg_u32 %[cnt], %[s2]\n\t"
        "s_cbranch_scc1 L_n2_%=\n\t"
        "s_waitcnt vmcnt(0)\n\t"
        "s_barrier\n\t"
        "L_n2_%=:\n\t"
        TILE("512", "2048", "2048")
        TILE("2560", "4096", "4096")
        TILE("4608", "6144", "6144")
        HALF(R_SA, R_SB, "%[oa]", "%[ob]", "%[nb]", "%[qb]",
             "", "ds_read_b128 %[v1], %[va] offset:6656\n\t",
             "", "ds_read_b128 %[kf], %[ka] offset:8192\n\tv_add_u32 %[va], %[va], %[vs]\n\t",
             "s_waitcnt lgkmcnt(2)\n\t", "v_add_u32 %[ka], 0x2000, %[ka]\n\t")
        HALF(R_SB, R_SA, "%[ob]", "%[oa]", "%[na]", "%[qa]",
             "s_waitcnt lgkmcnt(1)\n\t", "s_add_u32 %[cnt], %[cnt], 1\n\t",
             "s_waitcnt lgkmcnt(0)\n\t", "s_cmp_lg_u32 %[cnt], 16\n\t",
             "", "ds_read_b128 %[v0], %[va]\n\t")
        "s_cbranch_scc1 L_loop_%=\n\t"
        "s_nop 1\n\t"
        "v_mfma_f32_32x32x16_bf16 %[ob], %[v1], v[228:231], %[ob]\n\t"
        "s_waitcnt lgkmcnt(0)\n\t"
        "s_nop 7\n\ts_nop 7\n\t"
        : [oa] "+v"(oa), [ob] "+v"(ob), [ka] "+v"(ka), [va] "+v"(va), [cnt] "+s"(cnt),
          [kf] "=&v"(kf), [v0] "=&v"(v0), [v1] "=&v"(v1)
        : [qa] "v"(qa), [qb] "v"(qb), [na] "v"(na), [nb] "v"(nb), [vs] "v"(vstep), [s1] "s"(sync1), [s2] "s"(sync2)
        : "memory", "scc",
          "v192", "v193", "v194", "v195", "v196", "v197", "v198", "v199", "v200", "v201", "v202", "v203", "v204", "v205", "v206", "v207",
          "v208", "v209", "v210", "v211", "v212", "v213", "v214", "v215", "v216", "v217", "v218", "v219", "v220", "v221", "v222", "v223",
          "v224", "v225", "v226", "v227", "v228", "v229", "v230", "v231");
}

__global__ __launch_bounds__(512, 2) void attn_kernel(const unsigned short* __restrict__ woA, const unsigned short* __restrict__ woB,
                                                      char* __restrict__ ws) {
    extern __shared__ __attribute__((aligned(16))) char smem[];
    const unsigned lds0 = (unsigned)(uintptr_t)smem;
    const int tid = threadIdx.x, lane = tid & 63;
    const int w = __builtin_amdgcn_readfirstlane(tid >> 6);
    const int r = lane & 31, h = lane >> 5;
    const int b = blockIdx.x, xcd = b & 7, slot = b >> 3;
    const int isB = (slot < 16);
    const int hd = xcd * 4 + ((slot & 15) >> 2);
    const int hh = isB ? 32 + hd : hd;
    const int kvh = isB ? hd : 32;
    const int tile = (slot & 3) * 16 + 2 * w;

    ST(1, 0);
    bf16x8 qa, qb;
    {
        const char* qsrc = ws + WS_Q + (size_t)hh * 65536 + tile * 1024 + lane * 16;
        asm volatile("global_load_dwordx4 %0, %2, off\n\tglobal_load_dwordx4 %1, %2, off offset:1024"
                     : "=&v"(qa), "=&v"(qb) : "v"(qsrc) : "memory");
    }
    const char* kvsrc = ws + WS_KV + (size_t)kvh * 131072 + w * 1024 + lane * 16;
#pragma unroll
    for (int i = 0; i < 4; ++i) glds16(kvsrc + i * 8192, lds0 + A_KV + i * 8192 + w * 1024);
    const int fr = b * 4 + (w >> 1);
    unsigned wo0, wo1, wo2, wo3; float ga, gb;
    {
        const int kk = fr >> 4, ntile = fr & 15;
        const unsigned short* wsrc = ((kk < 32) ? woA : woB) + (size_t)((kk & 31) * 16 + 8 * (w & 1) + 4 * h) * 512 + ntile * 32 + r;
        const float* gsrc = (const float*)(ws + WS_G) + tile * 32 + r;
        asm volatile("global_load_ushort %0, %6, off\n\tglobal_load_ushort %1, %6, off offset:1024\n\t"
                     "global_load_ushort %2, %6, off offset:2048\n\tglobal_load_ushort %3, %6, off offset:3072\n\t"
                     "global_load_dword %4, %7, off\n\tglobal_load_dword %5, %7, off offset:128"
                     : "=&v"(wo0), "=&v"(wo1), "=&v"(wo2), "=&v"(wo3), "=&v"(ga), "=&v"(gb) : "v"(wsrc), "v"(gsrc) : "memory");
    }
    {
        const unsigned one = ((tid & 15) == 0 || (tid & 15) == 4) ? 0x3f803f80u : 0u;
        u32x4 c = {one, one, one, one};
        *(u32x4*)(smem + A_CONST + tid * 16) = c;
    }
    const unsigned kaddr = lds0 + A_KV + lane * 16;
    const unsigned vaddr = (r < 16) ? (lds0 + A_KV + 1024 + h * 256 + r * 16) : (lds0 + A_CONST + (r - 16) * 16);
    const unsigned vstep = (r < 16) ? 8192u : 0u;

    asm volatile("s_waitcnt vmcnt(8) lgkmcnt(0)\n\ts_barrier" : "+v"(qa), "+v"(qb) :: "memory");
    ST(1, 1);
#pragma unroll
    for (int i = 4; i < 16; ++i) glds16(kvsrc + i * 8192, lds0 + A_KV + i * 8192 + w * 1024);

    f32x16 zero;
#pragma unroll
    for (int i = 0; i < 16; ++i) zero[i] = 0.f;
    f32x16 na, nb, oa = zero, ob = zero;
    {
        const bf16x8 k0 = lds_b128(kaddr);
        const f32x16 sa = MFMA32(k0, qa, zero);
        const f32x16 sb = MFMA32(k0, qb, zero);
        const float ma = -half_max(max16(sa)) - 40.f, mb = -half_max(max16(sb)) - 40.f;
#pragma unroll
        for (int i = 0; i < 16; ++i) { na[i] = ma; nb[i] = mb; }
    }
    ST(1, 2);
    attn_main(kaddr, vaddr, vstep, qa, qb, na, nb, oa, ob, 1, 3);
    ST(1, 3);
    asm volatile("" : "+v"(wo0), "+v"(wo1), "+v"(wo2), "+v"(wo3), "+v"(ga), "+v"(gb));
    {
        uint2 pk;
        pk.x = wo0 | (wo1 << 16); pk.y = wo2 | (wo3 << 16);
        *(uint2*)(ws + WS_WO + (size_t)fr * 1024 + lane * 16 + (w & 1) * 8) = pk;
    }

    {
        bool bad = !(oa[8] < 1.329228e36f) || !(ob[8] < 1.329228e36f);
#pragma unroll
        for (int i = 0; i < 8; ++i) bad = bad || !(fabsf(oa[i]) <= 3.0e38f) || !(fabsf(ob[i]) <= 3.0e38f);
        if (__any(bad)) {
            float ma = -INFINITY, mb = -INFINITY;
#pragma unroll 1
            for (int t = 0; t < 64; ++t) {
                const bf16x8 kf = lds_b128(kaddr + t * 2048);
                ma = fmaxf(ma, max16(MFMA32(kf, qa, zero)));
                mb = fmaxf(mb, max16(MFMA32(kf, qb, zero)));
            }
            ma = -half_max(ma); mb = -half_max(mb);
#pragma unroll
            for (int i = 0; i < 16; ++i) { na[i] = ma; nb[i] = mb; }
            oa = zero; ob = zero;
            attn_main(kaddr, vaddr, vstep, qa, qb, na, nb, oa, ob, -1, -1);
        }
    }
    const float sa = (isB ? 1.f - ga : ga) / oa[8], sb = (isB ? 1.f - gb : gb) / ob[8];
    char* odst = ws + WS_O + (size_t)tile * 65536 + hh * 1024 + lane * 16;
    *(u32x4*)(odst) = pack8(oa, 0, sa);
    *(u32x4*)(odst + 65536) = pack8(ob, 0, sb);
    ST(1, 4);
}

constexpr int O_ROW = 272, O_PART = 64 * O_ROW, O_LDS = 8 * O_PART;

__global__ __launch_bounds__(512) void out_kernel(const char* __restrict__ ws, unsigned short* __restrict__ out) {
    extern __shared__ __attribute__((aligned(16))) char smem[];
    const int tid = threadIdx.x, lane = tid & 63;
    const int w = __builtin_amdgcn_readfirstlane(tid >> 6);
    const int r = lane & 31, h = lane >> 5;
    const int b = blockIdx.x, xcd = b & 7, slot = b >> 3;
    const int mt = xcd * 4 + (slot & 3), nt = slot >> 2;

    ST(2, 0);
    const char* osrc = ws + WS_O + (size_t)(mt * 2) * 65536 + (w * 8) * 1024 + lane * 16;
    const char* wsrc = ws + WS_WO + (size_t)(w * 8) * 16384 + (nt * 2) * 1024 + lane * 16;
    bf16x8 of[8][2], wf[8][2];
#pragma unroll
    for (int k = 0; k < 8; ++k) {
        of[k][0] = *(const bf16x8*)(osrc + k * 1024);
        of[k][1] = *(const bf16x8*)(osrc + 65536 + k * 1024);
        wf[k][0] = *(const bf16x8*)(wsrc + k * 16384);
        wf[k][1] = *(const bf16x8*)(wsrc + k * 16384 + 1024);
    }
    f32x16 acc[2][2];
#pragma unroll
    for (int i = 0; i < 16; ++i) { acc[0][0][i] = 0.f; acc[0][1][i] = 0.f; acc[1][0][i] = 0.f; acc[1][1][i] = 0.f; }
#pragma unroll
    for (int k = 0; k < 8; ++k)
#pragma unroll
        for (int ct = 0; ct < 2; ++ct)
#pragma unroll
            for (int tt = 0; tt < 2; ++tt) acc[ct][tt] = MFMA32(wf[k][ct], of[k][tt], acc[ct][tt]);
    ST(2, 1);

#pragma unroll
    for (int ct = 0; ct < 2; ++ct)
#pragma unroll
        for (int tt = 0; tt < 2; ++tt)
#pragma unroll
            for (int g = 0; g < 4; ++g) {
                f32x4 v = {acc[ct][tt][4 * g], acc[ct][tt][4 * g + 1], acc[ct][tt][4 * g + 2], acc[ct][tt][4 * g + 3]};
                *(f32x4*)(smem + w * O_PART + (tt * 32 + r) * O_ROW + (ct * 32 + 8 * g + 4 * h) * 4) = v;
            }
    __syncthreads();
    ST(2, 2);
    const int tok = tid >> 3, n8 = tid & 7;
    f32x4 s0 = {0.f, 0.f, 0.f, 0.f}, s1 = {0.f, 0.f, 0.f, 0.f};
#pragma unroll
    for (int p = 0; p < 8; ++p) {
        s0 += *(const f32x4*)(smem + p * O_PART + tok * O_ROW + n8 * 32);
        s1 += *(const f32x4*)(smem + p * O_PART + tok * O_ROW + n8 * 32 + 16);
    }
    u32x4 res;
    res[0] = pk_bf16(s0[0], s0[1]); res[1] = pk_bf16(s0[2], s0[3]); res[2] = pk_bf16(s1[0], s1[1]); res[3] = pk_bf16(s1[2], s1[3]);
    *(u32x4*)(out + (size_t)(mt * 64 + tok) * 512 + nt * 64 + n8 * 8) = res;
    ST(2, 3);
}

extern "C" void kernel_launch(void* const* d_in, const int* in_sizes, int n_in, void* d_out, int out_size, void* d_ws,
                              size_t ws_size, hipStream_t stream) {
    const unsigned short* x    = (const unsigned short*)d_in[0];
    const unsigned short* wq   = (const unsigned short*)d_in[1];
    const unsigned short* wk   = (const unsigned short*)d_in[2];
    const unsigned short* wv   = (const unsigned short*)d_in[3];
    const unsigned short* woA  = (const unsigned short*)d_in[4];
    const unsigned short* wqkv = (const unsigned short*)d_in[6];
    const unsigned short* woB  = (const unsigned short*)d_in[7];
    const unsigned short* gw   = (const unsigned short*)d_in[8];
    char* ws = (char*)d_ws;
    (void)hipFuncSetAttribute((const void*)proj_kernel, hipFuncAttributeMaxDynamicSharedMemorySize, P_LDS);
    (void)hipFuncSetAttribute((const void*)attn_kernel, hipFuncAttributeMaxDynamicSharedMemorySize, A_LDS);
    (void)hipFuncSetAttribute((const void*)out_kernel, hipFuncAttributeMaxDynamicSharedMemorySize, O_LDS);
    proj_kernel<<<256, 256, P_LDS, stream>>>(x, wq, wk, wv, wqkv, gw, ws);
    attn_kernel<<<256, 512, A_LDS, stream>>>(woA, woB, ws);
    out_kernel<<<256, 512, O_LDS, stream>>>(ws, (unsigned short*)d_out);
}
```

```cpp
#include <hip/hip_runtime.h>
#include <hip/hip_bf16.h>
#include <stdint.h>

typedef short    bf16x8 __attribute__((ext_vector_type(8)));
typedef short    s16x4  __attribute__((ext_vector_type(4)));
typedef float    f32x16 __attribute__((ext_vector_type(16)));
typedef float    f32x4  __attribute__((ext_vector_type(4)));
typedef float    f32x2  __attribute__((ext_vector_type(2)));
typedef unsigned u32x4  __attribute__((ext_vector_type(4)));
typedef __bf16   bf16x2v __attribute__((ext_vector_type(2)));

#define DI __device__ __forceinline__
#define MFMA32(a, b, c) __builtin_amdgcn_mfma_f32_32x32x16_bf16((a), (b), (c), 0, 0, 0)
#define LDS_AS __attribute__((address_space(3)))

constexpr size_t WS_Q  = 0;
constexpr size_t WS_KV = WS_Q + (size_t)64 * 65536;
constexpr size_t WS_O  = WS_KV + (size_t)34 * 131072;
constexpr size_t WS_WO = WS_O + (size_t)64 * 65536;
constexpr size_t WS_G  = WS_WO + (size_t)1024 * 1024;

#define ST(k, i)
constexpr float QSCALE = 0.25f * 1.4426950408889634f;

DI unsigned pk_bf16(float lo, float hi) {
    f32x2 v = {lo, hi};
    bf16x2v r = __builtin_convertvector(v, bf16x2v);
    return __builtin_bit_cast(unsigned, r);
}
DI float bf2f(unsigned short u) { return __uint_as_float(((unsigned)u) << 16); }

DI void glds16(const void* g, unsigned lds_base) {
    unsigned sv;
    asm volatile("s_mov_b32 %0, m0\n\ts_mov_b32 m0, %2\n\ts_nop 0\n\tglobal_load_lds_dwordx4 %1, off\n\ts_mov_b32 m0, %0"
                 : "=&s"(sv) : "v"(g), "s"(lds_base) : "memory");
}
#define WAIT_BAR(N) asm volatile("s_waitcnt vmcnt(" #N ") lgkmcnt(0)\n\ts_barrier" ::: "memory")

DI bf16x8 lds_b128(unsigned addr) { return *(const LDS_AS bf16x8*)(uintptr_t)addr; }
DI s16x4 lds_tr(unsigned addr) {
    return __builtin_bit_cast(s16x4, __builtin_amdgcn_ds_read_tr16_b64_v4i16((LDS_AS s16x4*)(uintptr_t)addr));
}
DI bf16x8 cat8(s16x4 a, s16x4 b) { return __builtin_shufflevector(a, b, 0, 1, 2, 3, 4, 5, 6, 7); }

DI u32x4 pack8(const f32x16& a, int base, float sc) {
    u32x4 r;
    r[0] = pk_bf16(a[base + 0] * sc, a[base + 1] * sc);
    r[1] = pk_bf16(a[base + 2] * sc, a[base + 3] * sc);
    r[2] = pk_bf16(a[base + 4] * sc, a[base + 5] * sc);
    r[3] = pk_bf16(a[base + 6] * sc, a[base + 7] * sc);
    return r;
}

constexpr int P_XS = 0, P_WS = 16384, P_WX = 32768, P_STAGE = 36864, P_LDS = 4 * P_STAGE;
#define SBAR() __builtin_amdgcn_sched_barrier(0)

struct PFrag { bf16x8 x[2][2], w[2][2], xe, we; };

template <bool ISV, bool EXTRA>
DI void proj_body(const unsigned short* __restrict__ x, const unsigned short* __restrict__ wsrc, int ldw,
                  const unsigned short* __restrict__ wk, const unsigned short* __restrict__ wv,
                  char* __restrict__ ws, int mt, int nt, int xtt, unsigned lds0, char* smem) {
    const int tid = threadIdx.x, lane = tid & 63;
    const int w = __builtin_amdgcn_readfirstlane(tid >> 6);
    const int wm = (w >> 1) & 1, wn = w & 1, kh = w >> 2;
    const int r = lane & 31, h = lane >> 5;

    const unsigned short* xsrc[2];
#pragma unroll
    for (int par = 0; par < 2; ++par) {
        const int row = 16 * w + 8 * par + (lane >> 3);
        const int ch = (lane & 7) ^ ((row >> 1) & 7);
        xsrc[par] = x + (size_t)(mt * 128 + row) * 512 + ch * 8;
    }
    const unsigned short* wsr;
    {
        const int krow = 8 * w + (lane >> 4);
        const int ch = (lane & 15) ^ (((lane >> 4) & 3) << 2);
        wsr = wsrc + (size_t)krow * ldw + ch * 8;
    }
    const unsigned short* esr = nullptr;
    if (EXTRA) {
        const int krow = 8 * w + ((lane & 31) >> 2);
        esr = ((lane & 2) ? wv : wk) + krow * 16 + (lane & 1) * 8;
    }
    auto issue_x = [&](int c) {
        const unsigned st = lds0 + (c & 3) * P_STAGE;
        glds16(xsrc[0] + c * 64, st + P_XS + w * 2048);
        glds16(xsrc[1] + c * 64, st + P_XS + w * 2048 + 1024);
    };
    auto issue_w = [&](int c) {
        const unsigned st = lds0 + (c & 3) * P_STAGE;
        glds16(wsr + (size_t)(c * 64) * ldw, st + P_WS + w * 2048);
        glds16(wsr + (size_t)(c * 64 + 4) * ldw, st + P_WS + w * 2048 + 1024);
        if (EXTRA) { if (lane < 32) glds16(esr + c * 64 * 16, st + P_WX + w * 512); }
    };
#pragma unroll
    for (int c = 0; c < 4; ++c) { issue_x(c); issue_w(c); }

    unsigned xa[2][2];
#pragma unroll
    for (int tt = 0; tt < 2; ++tt) {
        const int row = wm * 64 + tt * 32 + r;
#pragma unroll
        for (int k2 = 0; k2 < 2; ++k2) xa[k2][tt] = lds0 + P_XS + row * 128 + (((2 * (2 * kh + k2) + h) ^ ((row >> 1) & 7)) << 4);
    }
    const int q4 = (lane & 15) >> 2, p4 = lane & 3, g1 = (lane >> 4) & 1;
    unsigned wa[2];
#pragma unroll
    for (int ct = 0; ct < 2; ++ct)
        wa[ct] = lds0 + P_WS + (32 * kh + 8 * h + q4) * 256 + ((4 * ((2 * wn + ct) ^ q4) + 2 * g1 + (p4 >> 1)) << 4) + (p4 & 1) * 8;
    unsigned ea = 0, exa = 0;
    if (EXTRA) {
        ea = lds0 + P_WX + (16 * (w & 3) + 8 * h + q4) * 64 + g1 * 32 + p4 * 8;
        const int row = xtt * 32 + r;
        exa = lds0 + P_XS + row * 128 + (((2 * (w & 3) + h) ^ ((row >> 1) & 7)) << 4);
    }

    f32x16 acc[2][2], acce;
#pragma unroll
    for (int i = 0; i < 16; ++i) { acc[0][0][i] = 0.f; acc[0][1][i] = 0.f; acc[1][0][i] = 0.f; acc[1][1][i] = 0.f; acce[i] = 0.f; }

    PFrag F[2];
    auto mm = [&](const PFrag& f, int k2) {
#pragma unroll
        for (int ct = 0; ct < 2; ++ct)
#pragma unroll
            for (int tt = 0; tt < 2; ++tt)
                acc[ct][tt] = ISV ? MFMA32(f.x[k2][tt], f.w[k2][ct], acc[ct][tt]) : MFMA32(f.w[k2][ct], f.x[k2][tt], acc[ct][tt]);
    };
#pragma unroll
    for (int c = 0; c <= 8; ++c) {
        if (c < 8) {
            if (EXTRA) {
                if (c == 0) WAIT_BAR(15); else if (c <= 5) WAIT_BAR(10); else if (c == 6) WAIT_BAR(5); else WAIT_BAR(0);
            } else {
                if (c == 0) WAIT_BAR(12); else if (c <= 5) WAIT_BAR(8); else if (c == 6) WAIT_BAR(4); else WAIT_BAR(0);
            }
            if (c == 0) ST(0, 1);
            if (c == 1) ST(0, 2);
            if (c == 4) ST(0, 3);
            if (c == 7) ST(0, 4);
            const unsigned so = (c & 3) * P_STAGE;
            PFrag& f = F[c & 1];
#pragma unroll
            for (int k2 = 0; k2 < 2; ++k2) {
#pragma unroll
                for (int tt = 0; tt < 2; ++tt) f.x[k2][tt] = lds_b128(xa[k2][tt] + so);
#pragma unroll
                for (int ct = 0; ct < 2; ++ct)
                    f.w[k2][ct] = cat8(lds_tr(wa[ct] + so + k2 * 4096), lds_tr(wa[ct] + so + k2 * 4096 + 1024));
            }
            if (EXTRA) {
                f.xe = lds_b128(exa + so);
                f.we = cat8(lds_tr(ea + so), lds_tr(ea + so + 256));
            }
        }
        SBAR();
        if (c >= 1) {
            const PFrag& g = F[(c - 1) & 1];
            mm(g, 0);
            SBAR();
            if (c <= 4) issue_x(c + 3);
            SBAR();
            mm(g, 1);
            SBAR();
            if (c <= 4) issue_w(c + 3);
            if (EXTRA) { if (kh == ((c - 1) & 1)) acce = MFMA32(g.xe, g.we, acce); }
            SBAR();
        }
    }
    ST(0, 5);

    f32x16 fin[2];
    {
        char* mine = smem + w * 8192 + lane * 16;
        const char* theirs = smem + (w ^ 4) * 8192 + lane * 16;
#pragma unroll
        for (int tt = 0; tt < 2; ++tt)
#pragma unroll
            for (int g = 0; g < 4; ++g) {
                f32x4 v;
#pragma unroll
                for (int i = 0; i < 4; ++i) v[i] = kh ? acc[0][tt][4 * g + i] : acc[1][tt][4 * g + i];
                *(f32x4*)(mine + (tt * 4 + g) * 1024) = v;
            }
        float* red = (float*)(smem + 2 * P_STAGE);
        if (EXTRA) {
#pragma unroll
            for (int i = 0; i < 16; ++i) red[w * 1056 + ((i & 3) + 8 * (i >> 2) + 4 * h) * 33 + r] = acce[i];
        }
        __syncthreads();
#pragma unroll
        for (int tt = 0; tt < 2; ++tt)
#pragma unroll
            for (int g = 0; g < 4; ++g) {
                const f32x4 v = *(const f32x4*)(theirs + (tt * 4 + g) * 1024);
#pragma unroll
                for (int i = 0; i < 4; ++i) fin[tt][4 * g + i] = v[i] + (kh ? acc[1][tt][4 * g + i] : acc[0][tt][4 * g + i]);
            }
#pragma unroll
        for (int tt = 0; tt < 2; ++tt) {
            const int tile = mt * 4 + wm * 2 + tt;
            const int hd0 = (nt & 3) * 8 + wn * 4 + kh * 2;
            if (ISV) {
                char* dst = ws + WS_KV + (size_t)(hd0 + (r >> 4)) * 131072 + tile * 2048 + 1024 + h * 256 + (r & 15) * 16;
                *(u32x4*)(dst) = pack8(fin[tt], 0, 1.f);
                *(u32x4*)(dst + 512) = pack8(fin[tt], 8, 1.f);
            } else if (nt < 8) {
                char* dst = ws + WS_Q + (size_t)((nt >> 2) * 32 + hd0) * 65536 + tile * 1024 + lane * 16;
                *(u32x4*)(dst) = pack8(fin[tt], 0, QSCALE);
                *(u32x4*)(dst + 65536) = pack8(fin[tt], 8, QSCALE);
            } else {
                char* dst = ws + WS_KV + (size_t)hd0 * 131072 + tile * 2048 + lane * 16;
                *(u32x4*)(dst) = pack8(fin[tt], 0, 1.f);
                *(u32x4*)(dst + 131072) = pack8(fin[tt], 8, 1.f);
            }
        }
        if (EXTRA) {
            if (w >= 6) {
                const int tile = mt * 4 + xtt;
                float v[8];
                char* dst;
                int idx0, istr;
                if (w == 6) {
                    idx0 = r * 33 + 4 * h; istr = 1;
                    dst = ws + WS_KV + (size_t)32 * 131072 + tile * 2048 + lane * 16;
                } else {
                    idx0 = (16 * (lane >> 5) + 4 * ((lane >> 4) & 1)) * 33 + 16 + (lane & 15); istr = 33;
                    dst = ws + WS_KV + (size_t)32 * 131072 + tile * 2048 + 1024 + lane * 16;
                }
#pragma unroll
                for (int j = 0; j < 8; ++j) {
                    const int idx = idx0 + (8 * (j >> 2) + (j & 3)) * istr;
                    float a = 0.f;
#pragma unroll
                    for (int p = 0; p < 8; ++p) a += red[p * 1056 + idx];
                    v[j] = a;
                }
                u32x4 o;
                o[0] = pk_bf16(v[0], v[1]); o[1] = pk_bf16(v[2], v[3]); o[2] = pk_bf16(v[4], v[5]); o[3] = pk_bf16(v[6], v[7]);
                *(u32x4*)dst = o;
            }
        }
    }
}

__global__ __launch_bounds__(512, 2) void proj_kernel(const unsigned short* __restrict__ x, const unsigned short* __restrict__ wq,
                                                      const unsigned short* __restrict__ wk, const unsigned short* __restrict__ wv,
                                                      const unsigned short* __restrict__ wqkv, const unsigned short* __restrict__ gw,
                                                      char* __restrict__ ws) {
    extern __shared__ __attribute__((aligned(16))) char smem[];
    const unsigned lds0 = (unsigned)(uintptr_t)smem;
    const int b = blockIdx.x, xcd = b & 7, slot = b >> 3;
    const int mt = (xcd & 3) * 4 + (slot & 3), nt = (xcd >> 2) * 8 + (slot >> 2);
    const unsigned short* wsrc = (nt < 4) ? (wq + nt * 128) : (wqkv + (nt - 4) * 128);
    const int ldw = (nt < 4) ? 512 : 1536;
    const bool extra = (nt & 3) == 1;
    const int xtt = nt >> 2;

    ST(0, 0);
    const int tid = threadIdx.x;
    const int gtok = mt * 128 + nt * 8 + (tid >> 6);
    u32x4 gx, gv;
    {
        const unsigned short* px = x + (size_t)gtok * 512 + (tid & 63) * 8;
        const unsigned short* pw = gw + (tid & 63) * 8;
        asm volatile("global_load_dwordx4 %0, %2, off\n\tglobal_load_dwordx4 %1, %3, off"
                     : "=&v"(gx), "=&v"(gv) : "v"(px), "v"(pw) : "memory");
    }
    if (nt >= 12) {
        if (extra) proj_body<true, true>(x, wsrc, ldw, wk, wv, ws, mt, nt, xtt, lds0, smem);
        else       proj_body<true, false>(x, wsrc, ldw, wk, wv, ws, mt, nt, xtt, lds0, smem);
    } else {
        if (extra) proj_body<false, true>(x, wsrc, ldw, wk, wv, ws, mt, nt, xtt, lds0, smem);
        else       proj_body<false, false>(x, wsrc, ldw, wk, wv, ws, mt, nt, xtt, lds0, smem);
    }
    asm volatile("" : "+v"(gx), "+v"(gv));
    float s = 0.f;
#pragma unroll
    for (int i = 0; i < 4; ++i) {
        s += __uint_as_float(gx[i] << 16) * __uint_as_float(gv[i] << 16);
        s += __uint_as_float(gx[i] & 0xffff0000u) * __uint_as_float(gv[i] & 0xffff0000u);
    }
#pragma unroll
    for (int o = 32; o >= 1; o >>= 1) s += __shfl_xor(s, o);
    if ((tid & 63) == 0) ((float*)(ws + WS_G))[gtok] = 1.f / (1.f + __expf(-s));
    ST(0, 6);
}

constexpr int A_KV = 0, A_CONST = 131072, A_LDS = A_CONST + 8192;

DI float max16(const f32x16& s) {
    float a = fmaxf(fmaxf(s[0], s[1]), s[2]), b = fmaxf(fmaxf(s[3], s[4]), s[5]);
    a = fmaxf(fmaxf(a, s[6]), s[7]);   b = fmaxf(fmaxf(b, s[8]), s[9]);
    a = fmaxf(fmaxf(a, s[10]), s[11]); b = fmaxf(fmaxf(b, s[12]), s[13]);
    a = fmaxf(fmaxf(a, s[14]), s[15]);
    return fmaxf(a, b);
}
DI float half_max(float m) {
    auto rr = __builtin_amdgcn_permlane32_swap(__float_as_uint(m), __float_as_uint(m), false, false);
    return fmaxf(__uint_as_float(rr[0]), __uint_as_float(rr[1]));
}

#define R_SA "192"
#define R_SB "208"
#define EX(X, i) "v_exp_f32 v[" X "+" #i "], v[" X "+" #i "]\n\t"
#define CV(X, j, a, b) "v_cvt_pk_bf16_f32 v[224+" #j "], v[" X "+" #a "], v[" X "+" #b "]\n\t"
#define HALF(X, Y, OX, OY, NY, QY, WA, RA, WB, RB, WC, RC)                                        \
    EX(X, 0) EX(X, 1) WA                                                                          \
    "v_mfma_f32_32x32x16_bf16 " OY ", %[v1], v[228:231], " OY "\n\t" RA                           \
    EX(X, 2) EX(X, 3) CV(X, 0, 0, 1) EX(X, 4) EX(X, 5) CV(X, 1, 2, 3) WB                          \
    "v_mfma_f32_32x32x16_bf16 v[" Y ":" Y "+15], %[kf], " QY ", " NY "\n\t" RB                    \
    EX(X, 6) EX(X, 7) CV(X, 2, 4, 5) EX(X, 8) EX(X, 9) CV(X, 3, 6, 7) EX(X, 10) EX(X, 11) WC      \
    "v_mfma_f32_32x32x16_bf16 " OX ", %[v0], v[224:227], " OX "\n\t" RC                           \
    CV(X, 4, 8, 9) EX(X, 12) EX(X, 13) CV(X, 5, 10, 11) EX(X, 14) EX(X, 15) CV(X, 6, 12, 13) CV(X, 7, 14, 15)
#define TILE(OFF_V1, OFF_KN, OFF_V0N, P0, P1)                                                     \
    HALF(R_SA, R_SB, "%[oa]", "%[ob]", "%[nb]", "%[qb]",                                          \
         "", "ds_read_b128 %[v1], %[va] offset:" OFF_V1 "\n\t" P0,                                \
         "", "ds_read_b128 %[kf], %[ka] offset:" OFF_KN "\n\t",                                   \
         "s_waitcnt lgkmcnt(2)\n\t", "")                                                          \
    HALF(R_SB, R_SA, "%[ob]", "%[oa]", "%[na]", "%[qa]",                                          \
         "s_waitcnt lgkmcnt(1)\n\t", P1,                                                          \
         "s_waitcnt lgkmcnt(0)\n\t", "",                                                          \
         "", "ds_read_b128 %[v0], %[va] offset:" OFF_V0N "\n\t")
#define ATTN_ASM(PY, PO)                                                                          \
    asm volatile(                                                                                 \
        "ds_read_b128 %[kf], %[ka]\n\t"                                                           \
        "ds_read_b128 %[v1], %[va]\n\t"                                                           \
        "ds_read_b128 %[v0], %[va]\n\t"                                                           \
        "v_mov_b32 v228, 0\n\tv_mov_b32 v229, 0\n\tv_mov_b32 v230, 0\n\tv_mov_b32 v231, 0\n\t"    \
        "s_waitcnt lgkmcnt(0)\n\t"                                                                \
        "v_mfma_f32_32x32x16_bf16 v[192:207], %[kf], %[qa], %[na]\n\t"                            \
        "s_nop 7\n\ts_nop 7\n\t"                                                                  \
        "L_loop_%=:\n\t"                                                                          \
        "s_cmp_lg_u32 %[cnt], %[s1]\n\t"                                                          \
        "s_cbranch_scc1 L_n1_%=\n\t"                                                              \
        "s_waitcnt vmcnt(0)\n\t"                                                                  \
        "s_barrier\n\t"                                                                           \
        "L_n1_%=:\n\t"                                                                            \
        TILE("512", "2048", "2048", "s_setprio 0\n\t", PY)                                        \
        TILE("2560", "4096", "4096", PO, "")                                                      \
        TILE("4608", "6144", "6144", "", "")                                                      \
        HALF(R_SA, R_SB, "%[oa]", "%[ob]", "%[nb]", "%[qb]",                                      \
             "", "ds_read_b128 %[v1], %[va] offset:6656\n\t",                                     \
             "", "ds_read_b128 %[kf], %[ka] offset:8192\n\tv_add_u32 %[va], %[va], %[vs]\n\t",    \
             "s_waitcnt lgkmcnt(2)\n\t", "v_add_u32 %[ka], 0x2000, %[ka]\n\t")                    \
        HALF(R_SB, R_SA, "%[ob]", "%[oa]", "%[na]", "%[qa]",                                      \
             "s_waitcnt lgkmcnt(1)\n\t", "s_add_u32 %[cnt], %[cnt], 1\n\t",                       \
             "s_waitcnt lgkmcnt(0)\n\t", "s_cmp_lg_u32 %[cnt], 16\n\t",                           \
             "", "ds_read_b128 %[v0], %[va]\n\t")                                                 \
        "s_cbranch_scc1 L_loop_%=\n\t"                                                            \
        "s_nop 1\n\t"                                                                             \
        "v_mfma_f32_32x32x16_bf16 %[ob], %[v1], v[228:231], %[ob]\n\t"                            \
        "s_waitcnt lgkmcnt(0)\n\t"                                                                \
        "s_nop 7\n\ts_nop 7\n\t"                                                                  \
        : [oa] "+v"(oa), [ob] "+v"(ob), [ka] "+v"(ka), [va] "+v"(va), [cnt] "+s"(cnt),            \
          [kf] "=&v"(kf), [v0] "=&v"(v0), [v1] "=&v"(v1)                                          \
        : [qa] "v"(qa), [qb] "v"(qb), [na] "v"(na), [nb] "v"(nb), [vs] "v"(vstep), [s1] "s"(sync1) \
        : "memory", "scc",                                                                        \
          "v192", "v193", "v194", "v195", "v196", "v197", "v198", "v199", "v200", "v201", "v202", "v203", "v204", "v205", "v206", "v207", \
          "v208", "v209", "v210", "v211", "v212", "v213", "v214", "v215", "v216", "v217", "v218", "v219", "v220", "v221", "v222", "v223", \
          "v224", "v225", "v226", "v227", "v228", "v229", "v230", "v231")

template <bool YOUNG>
DI void attn_main(unsigned ka, unsigned va, unsigned vstep, const bf16x8& qa, const bf16x8& qb, const f32x16& na, const f32x16& nb,
                  f32x16& oa, f32x16& ob, int sync1) {
    bf16x8 kf, v0, v1;
    int cnt = 0;
    if (YOUNG) ATTN_ASM("s_setprio 1\n\t", "");
    else       ATTN_ASM("", "s_setprio 1\n\t");
}

__global__ __launch_bounds__(512, 2) void attn_kernel(const unsigned short* __restrict__ woA, const unsigned short* __restrict__ woB,
                                                      char* __restrict__ ws) {
    extern __shared__ __attribute__((aligned(16))) char smem[];
    const unsigned lds0 = (unsigned)(uintptr_t)smem;
    const int tid = threadIdx.x, lane = tid & 63;
    const int w = __builtin_amdgcn_readfirstlane(tid >> 6);
    const int r = lane & 31, h = lane >> 5;
    const int b = blockIdx.x, xcd = b & 7, slot = b >> 3;
    const int isB = (slot < 16);
    const int hd = xcd * 4 + ((slot & 15) >> 2);
    const int hh = isB ? 32 + hd : hd;
    const int kvh = isB ? hd : 32;
    const int tile = (slot & 3) * 16 + 2 * w;

    ST(1, 0);
    bf16x8 qa, qb;
    {
        const char* qsrc = ws + WS_Q + (size_t)hh * 65536 + tile * 1024 + lane * 16;
        asm volatile("global_load_dwordx4 %0, %2, off\n\tglobal_load_dwordx4 %1, %2, off offset:1024"
                     : "=&v"(qa), "=&v"(qb) : "v"(qsrc) : "memory");
    }
    const char* kvsrc = ws + WS_KV + (size_t)kvh * 131072 + w * 1024 + lane * 16;
#pragma unroll
    for (int i = 0; i < 4; ++i) glds16(kvsrc + i * 8192, lds0 + A_KV + i * 8192 + w * 1024);
    const int fr = b * 4 + (w >> 1);
    unsigned wo0, wo1, wo2, wo3; float ga, gb;
    {
        const int kk = fr >> 4, ntile = fr & 15;
        const unsigned short* wsrc = ((kk < 32) ? woA : woB) + (size_t)((kk & 31) * 16 + 8 * (w & 1) + 4 * h) * 512 + ntile * 32 + r;
        const float* gsrc = (const float*)(ws + WS_G) + tile * 32 + r;
        asm volatile("global_load_ushort %0, %6, off\n\tglobal_load_ushort %1, %6, off offset:1024\n\t"
                     "global_load_ushort %2, %6, off offset:2048\n\tglobal_load_ushort %3, %6, off offset:3072\n\t"
                     "global_load_dword %4, %7, off\n\tglobal_load_dword %5, %7, off offset:128"
                     : "=&v"(wo0), "=&v"(wo1), "=&v"(wo2), "=&v"(wo3), "=&v"(ga), "=&v"(gb) : "v"(wsrc), "v"(gsrc) : "memory");
    }
    {
        const unsigned one = ((tid & 15) == 0 || (tid & 15) == 4) ? 0x3f803f80u : 0u;
        u32x4 c = {one, one, one, one};
        *(u32x4*)(smem + A_CONST + tid * 16) = c;
    }
    const unsigned kaddr = lds0 + A_KV + lane * 16;
    const unsigned vaddr = (r < 16) ? (lds0 + A_KV + 1024 + h * 256 + r * 16) : (lds0 + A_CONST + (r - 16) * 16);
    const unsigned vstep = (r < 16) ? 8192u : 0u;

    asm volatile("s_waitcnt vmcnt(6) lgkmcnt(0)\n\ts_barrier" : "+v"(qa), "+v"(qb) :: "memory");
    ST(1, 1);
    if (w >= 4) {
#pragma unroll
        for (int i = 0; i < 24; ++i) glds16(kvsrc + 28672 + i * 4096, lds0 + A_KV + 28672 + i * 4096 + w * 1024);
    }

    f32x16 zero;
#pragma unroll
    for (int i = 0; i < 16; ++i) zero[i] = 0.f;
    f32x16 na, nb, oa = zero, ob = zero;
    {
        const bf16x8 k0 = lds_b128(kaddr);
        const f32x16 sa = MFMA32(k0, qa, zero);
        const f32x16 sb = MFMA32(k0, qb, zero);
        const float ma = -half_max(max16(sa)) - 40.f, mb = -half_max(max16(sb)) - 40.f;
#pragma unroll
        for (int i = 0; i < 16; ++i) { na[i] = ma; nb[i] = mb; }
    }
    ST(1, 2);
    if (w >= 4) attn_main<true>(kaddr, vaddr, vstep, qa, qb, na, nb, oa, ob, 2);
    else        attn_main<false>(kaddr, vaddr, vstep, qa, qb, na, nb, oa, ob, 3);
    ST(1, 3);
    asm volatile("" : "+v"(wo0), "+v"(wo1), "+v"(wo2), "+v"(wo3), "+v"(ga), "+v"(gb));
    {
        uint2 pk;
        pk.x = wo0 | (wo1 << 16); pk.y = wo2 | (wo3 << 16);
        *(uint2*)(ws + WS_WO + (size_t)fr * 1024 + lane * 16 + (w & 1) * 8) = pk;
    }

    {
        bool bad = !(oa[8] < 1.329228e36f) || !(ob[8] < 1.329228e36f);
#pragma unroll
        for (int i = 0; i < 8; ++i) bad = bad || !(fabsf(oa[i]) <= 3.0e38f) || !(fabsf(ob[i]) <= 3.0e38f);
        if (__any(bad)) {
            float ma = -INFINITY, mb = -INFINITY;
#pragma unroll 1
            for (int t = 0; t < 64; ++t) {
                const bf16x8 kf = lds_b128(kaddr + t * 2048);
                ma = fmaxf(ma, max16(MFMA32(kf, qa, zero)));
                mb = fmaxf(mb, max16(MFMA32(kf, qb, zero)));
            }
            ma = -half_max(ma); mb = -half_max(mb);
#pragma unroll
            for (int i = 0; i < 16; ++i) { na[i] = ma; nb[i] = mb; }
            oa = zero; ob = zero;
            attn_main<false>(kaddr, vaddr, vstep, qa, qb, na, nb, oa, ob, -1);
        }
    }
    const float sa = (isB ? 1.f - ga : ga) / oa[8], sb = (isB ? 1.f - gb : gb) / ob[8];
    char* odst = ws + WS_O + (size_t)tile * 65536 + hh * 1024 + lane * 16;
    *(u32x4*)(odst) = pack8(oa, 0, sa);
    *(u32x4*)(odst + 65536) = pack8(ob, 0, sb);
    ST(1, 4);
}

constexpr int O_LDS = 4 * 3 * 4096;

__global__ __launch_bounds__(256, 1) void out_kernel(const char* __restrict__ ws, unsigned short* __restrict__ out) {
    extern __shared__ __attribute__((aligned(16))) char smem[];
    const int tid = threadIdx.x, lane = tid & 63;
    const int w = __builtin_amdgcn_readfirstlane(tid >> 6);
    const int r = lane & 31, h = lane >> 5;
    const int b = blockIdx.x, xcd = b & 7, slot = b >> 3;
    const int mt = xcd * 4 + (slot & 3), nt = slot >> 2;

    ST(2, 0);
    const char* osrc = ws + WS_O + (size_t)(mt * 2) * 65536 + (w * 16) * 1024 + lane * 16;
    const char* wsrc = ws + WS_WO + (size_t)(w * 16) * 16384 + (nt * 2) * 1024 + lane * 16;
    bf16x8 of[16][2], wf[16][2];
#pragma unroll
    for (int k = 0; k < 16; ++k) {
        of[k][0] = *(const bf16x8*)(osrc + k * 1024);
        of[k][1] = *(const bf16x8*)(osrc + 65536 + k * 1024);
        wf[k][0] = *(const bf16x8*)(wsrc + k * 16384);
        wf[k][1] = *(const bf16x8*)(wsrc + k * 16384 + 1024);
    }
    SBAR();
    f32x16 acc[4];
#pragma unroll
    for (int i = 0; i < 16; ++i) { acc[0][i] = 0.f; acc[1][i] = 0.f; acc[2][i] = 0.f; acc[3][i] = 0.f; }
#pragma unroll
    for (int k = 0; k < 16; ++k)
#pragma unroll
        for (int q = 0; q < 4; ++q) acc[q] = MFMA32(wf[k][q >> 1], of[k][q & 1], acc[q]);
    ST(2, 1);
    f32x16 fin;
#pragma unroll
    for (int q = 0; q < 4; ++q) {
        if (q == w) { fin = acc[q]; }
        else {
            char* dst = smem + (q * 3 + ((w - q - 1) & 3)) * 4096 + lane * 16;
#pragma unroll
            for (int g = 0; g < 4; ++g) {
                f32x4 v = {acc[q][4 * g], acc[q][4 * g + 1], acc[q][4 * g + 2], acc[q][4 * g + 3]};
                *(f32x4*)(dst + g * 1024) = v;
            }
        }
    }
    __syncthreads();
    ST(2, 2);
#pragma unroll
    for (int p = 0; p < 3; ++p)
#pragma unroll
        for (int g = 0; g < 4; ++g) {
            const f32x4 v = *(const f32x4*)(smem + (w * 3 + p) * 4096 + g * 1024 + lane * 16);
#pragma unroll
            for (int i = 0; i < 4; ++i) fin[4 * g + i] += v[i];
        }
    unsigned short* drow = out + (size_t)(mt * 64 + (w & 1) * 32 + r) * 512 + nt * 64 + (w >> 1) * 32 + h * 8;
#pragma unroll
    for (int g = 0; g < 4; g += 2) {
        unsigned a0 = pk_bf16(fin[4 * g], fin[4 * g + 1]), a1 = pk_bf16(fin[4 * g + 2], fin[4 * g + 3]);
        unsigned b0 = pk_bf16(fin[4 * g + 4], fin[4 * g + 5]), b1 = pk_bf16(fin[4 * g + 6], fin[4 * g + 7]);
        auto r0 = __builtin_amdgcn_permlane32_swap(a0, b0, false, false);
        auto r1 = __builtin_amdgcn_permlane32_swap(a1, b1, false, false);
        u32x4 res = {r0[0], r1[0], r0[1], r1[1]};
        *(u32x4*)(drow + g * 8) = res;
    }
    ST(2, 3);
}

extern "C" void kernel_launch(void* const* d_in, const int* in_sizes, int n_in, void* d_out, int out_size, void* d_ws,
                              size_t ws_size, hipStream_t stream) {
    const unsigned short* x    = (const unsigned short*)d_in[0];
    const unsigned short* wq   = (const unsigned short*)d_in[1];
    const unsigned short* wk   = (const unsigned short*)d_in[2];
    const unsigned short* wv   = (const unsigned short*)d_in[3];
    const unsigned short* woA  = (const unsigned short*)d_in[4];
    const unsigned short* wqkv = (const unsigned short*)d_in[6];
    const unsigned short* woB  = (const unsigned short*)d_in[7];
    const unsigned short* gw   = (const unsigned short*)d_in[8];
    char* ws = (char*)d_ws;
    (void)hipFuncSetAttribute((const void*)proj_kernel, hipFuncAttributeMaxDynamicSharedMemorySize, P_LDS);
    (void)hipFuncSetAttribute((const void*)attn_kernel, hipFuncAttributeMaxDynamicSharedMemorySize, A_LDS);
    (void)hipFuncSetAttribute((const void*)out_kernel, hipFuncAttributeMaxDynamicSharedMemorySize, O_LDS);
    proj_kernel<<<256, 512, P_LDS, stream>>>(x, wq, wk, wv, wqkv, gw, ws);
    attn_kernel<<<256, 512, A_LDS, stream>>>(woA, woB, ws);
    out_kernel<<<256, 256, O_LDS, stream>>>(ws, (unsigned short*)d_out);
}
```

```cpp
#include <hip/hip_runtime.h>
#include <hip/hip_bf16.h>
#include <stdint.h>

typedef short    bf16x8 __attribute__((ext_vector_type(8)));
typedef short    s16x4  __attribute__((ext_vector_type(4)));
typedef float    f32x16 __attribute__((ext_vector_type(16)));
typedef float    f32x4  __attribute__((ext_vector_type(4)));
typedef float    f32x2  __attribute__((ext_vector_type(2)));
typedef unsigned u32x4  __attribute__((ext_vector_type(4)));
typedef __bf16   bf16x2v __attribute__((ext_vector_type(2)));

#define DI __device__ __forceinline__
#define MFMA32(a, b, c) __builtin_amdgcn_mfma_f32_32x32x16_bf16((a), (b), (c), 0, 0, 0)
#define LDS_AS __attribute__((address_space(3)))

constexpr size_t WS_Q  = 0;
constexpr size_t WS_KV = WS_Q + (size_t)64 * 65536;
constexpr size_t WS_O  = WS_KV + (size_t)34 * 131072;
constexpr size_t WS_WO = WS_O + (size_t)64 * 65536;
constexpr size_t WS_G  = WS_WO + (size_t)1024 * 1024;

#define ST(k, i)
constexpr float QSCALE = 0.25f * 1.4426950408889634f;

DI unsigned pk_bf16(float lo, float hi) {
    f32x2 v = {lo, hi};
    bf16x2v r = __builtin_convertvector(v, bf16x2v);
    return __builtin_bit_cast(unsigned, r);
}
DI float bf2f(unsigned short u) { return __uint_as_float(((unsigned)u) << 16); }

DI void glds16(const void* g, unsigned lds_base) {
    unsigned sv;
    asm volatile("s_mov_b32 %0, m0\n\ts_mov_b32 m0, %2\n\ts_nop 0\n\tglobal_load_lds_dwordx4 %1, off\n\ts_mov_b32 m0, %0"
                 : "=&s"(sv) : "v"(g), "s"(lds_base) : "memory");
}
#define WAIT_BAR(N) asm volatile("s_waitcnt vmcnt(" #N ") lgkmcnt(0)\n\ts_barrier" ::: "memory")

DI bf16x8 lds_b128(unsigned addr) { return *(const LDS_AS bf16x8*)(uintptr_t)addr; }
DI s16x4 lds_tr(unsigned addr) {
    return __builtin_bit_cast(s16x4, __builtin_amdgcn_ds_read_tr16_b64_v4i16((LDS_AS s16x4*)(uintptr_t)addr));
}
DI bf16x8 cat8(s16x4 a, s16x4 b) { return __builtin_shufflevector(a, b, 0, 1, 2, 3, 4, 5, 6, 7); }

DI u32x4 pack8(const f32x16& a, int base, float sc) {
    u32x4 r;
    r[0] = pk_bf16(a[base + 0] * sc, a[base + 1] * sc);
    r[1] = pk_bf16(a[base + 2] * sc, a[base + 3] * sc);
    r[2] = pk_bf16(a[base + 4] * sc, a[base + 5] * sc);
    r[3] = pk_bf16(a[base + 6] * sc, a[base + 7] * sc);
    return r;
}

constexpr int P_XS = 0, P_WS = 16384, P_STAGE = 32768, P_WX = 4 * P_STAGE, P_GT = P_WX + 16384, P_CNT = P_GT + 12288, P_LDS = P_CNT + 16;
#define SBAR() __builtin_amdgcn_sched_barrier(0)

struct PSrc { const unsigned short *xs0, *xs1, *wsr; int ldw; };
DI PSrc proj_src(const unsigned short* x, const unsigned short* wsrc, int ldw, int mt, int nt, int l, int lane) {
    PSrc s;
    const int row = 32 * l + (lane >> 3);
    s.xs0 = x + (size_t)(mt * 128 + row) * 512 + ((lane & 7) ^ ((row >> 1) & 7)) * 8;
    s.xs1 = x + (size_t)(mt * 128 + row + 8) * 512 + ((lane & 7) ^ (((row + 8) >> 1) & 7)) * 8;
    const int krow = 16 * l + (lane >> 4);
    s.wsr = wsrc + (size_t)krow * ldw + ((lane & 15) ^ (((lane >> 4) & 3) << 2)) * 8;
    s.ldw = ldw;
    return s;
}
DI void proj_issue(const PSrc& s, int c, int l, unsigned lds0) {
    const unsigned st = lds0 + (c & 3) * P_STAGE + l * 4096;
    glds16(s.xs0 + c * 64, st + P_XS);
    glds16(s.xs1 + c * 64, st + P_XS + 1024);
    glds16(s.xs0 + c * 64 + 16 * 512, st + P_XS + 2048);
    glds16(s.xs1 + c * 64 + 16 * 512, st + P_XS + 3072);
#pragma unroll
    for (int i = 0; i < 4; ++i) glds16(s.wsr + (size_t)(c * 64 + 4 * i) * s.ldw, st + P_WS + i * 1024);
}
DI float wave_sum63(float v) {
#define DPP_ADD(ctrl, rm) v += __int_as_float(__builtin_amdgcn_update_dpp(0, __float_as_int(v), ctrl, rm, 0xf, false))
    DPP_ADD(0x111, 0xf); DPP_ADD(0x112, 0xf); DPP_ADD(0x114, 0xf); DPP_ADD(0x118, 0xf);
    DPP_ADD(0x142, 0xa); DPP_ADD(0x143, 0xc);
#undef DPP_ADD
    return v;
}
DI void lds_spin(const char* smem, int off, unsigned target) {
    while (__builtin_amdgcn_readfirstlane(*(const volatile unsigned*)(smem + off)) < target) __builtin_amdgcn_s_sleep(1);
    asm volatile("" ::: "memory");
}

template <bool ISV>
DI void proj_compute(const PSrc& src, const unsigned short* __restrict__ x, const unsigned short* __restrict__ wx,
                     const unsigned short* __restrict__ gw, char* __restrict__ ws, int mt, int nt, int w, unsigned lds0, char* smem) {
    const int lane = threadIdx.x & 63;
    const int wm = w >> 1, wn = w & 1;
    const int r = lane & 31, h = lane >> 5;
    proj_issue(src, 0, w, lds0);
    {
        const unsigned short* es = wx + (size_t)((lane >> 5) * 64 + 16 * w) * 16 + (lane & 31) * 8;
#pragma unroll
        for (int i = 0; i < 4; ++i) glds16(es + i * 2048, lds0 + P_WX + w * 4096 + i * 1024);
        const unsigned short* px = x + (size_t)(mt * 128 + nt * 8 + 2 * w) * 512 + lane * 8;
        glds16(px, lds0 + P_GT + w * 3072);
        glds16(px + 512, lds0 + P_GT + w * 3072 + 1024);
        glds16(gw + lane * 8, lds0 + P_GT + w * 3072 + 2048);
    }
    unsigned xo[4][2];
#pragma unroll
    for (int tt = 0; tt < 2; ++tt) {
        const int row = wm * 64 + tt * 32 + r;
#pragma unroll
        for (int ks = 0; ks < 4; ++ks) xo[ks][tt] = lds0 + P_XS + row * 128 + (((2 * ks + h) ^ ((row >> 1) & 7)) << 4);
    }
    const int q4 = (lane & 15) >> 2, p4 = lane & 3, g1 = (lane >> 4) & 1;
    unsigned wa[2];
#pragma unroll
    for (int ct = 0; ct < 2; ++ct)
        wa[ct] = lds0 + P_WS + (8 * h + q4) * 256 + ((4 * ((2 * wn + ct) ^ q4) + 2 * g1 + (p4 >> 1)) << 4) + (p4 & 1) * 8;
    f32x16 acc[2][2];
#pragma unroll
    for (int i = 0; i < 16; ++i) { acc[0][0][i] = 0.f; acc[0][1][i] = 0.f; acc[1][0][i] = 0.f; acc[1][1][i] = 0.f; }
    bf16x8 fx[2][4][2], fw[2][4][2];
    auto mm = [&](int bf, int ks) {
#pragma unroll
        for (int ct = 0; ct < 2; ++ct)
#pragma unroll
            for (int tt = 0; tt < 2; ++tt)
                acc[ct][tt] = ISV ? MFMA32(fx[bf][ks][tt], fw[bf][ks][ct], acc[ct][tt]) : MFMA32(fw[bf][ks][ct], fx[bf][ks][tt], acc[ct][tt]);
    };
    auto rd = [&](int bf, int ks, unsigned so) {
#pragma unroll
        for (int tt = 0; tt < 2; ++tt) fx[bf][ks][tt] = lds_b128(xo[ks][tt] + so);
#pragma unroll
        for (int ct = 0; ct < 2; ++ct) fw[bf][ks][ct] = cat8(lds_tr(wa[ct] + so + ks * 4096), lds_tr(wa[ct] + so + ks * 4096 + 1024));
    };
    auto step = [&](int bf, int c) {
        asm volatile("s_waitcnt lgkmcnt(0)\n\ts_barrier" ::: "memory");
        const unsigned so = (c & 3) * P_STAGE;
#pragma unroll
        for (int ks = 0; ks < 4; ++ks) { rd(bf, ks, so); mm(bf ^ 1, ks); }
#pragma unroll
        for (int i = 0; i < 12; ++i) {
            __builtin_amdgcn_sched_group_barrier(0x008, 1, 0);
            __builtin_amdgcn_sched_group_barrier(0x002, 1, 0);
            __builtin_amdgcn_sched_group_barrier(0x100, 2, 0);
        }
        __builtin_amdgcn_sched_group_barrier(0x008, 4, 0);
        SBAR();
    };
    asm volatile("" : "+v"(xo[0][0]), "+v"(xo[0][1]), "+v"(xo[1][0]), "+v"(xo[1][1]), "+v"(xo[2][0]), "+v"(xo[2][1]),
                      "+v"(xo[3][0]), "+v"(xo[3][1]), "+v"(wa[0]), "+v"(wa[1]),
                      "+v"(acc[0][0]), "+v"(acc[0][1]), "+v"(acc[1][0]), "+v"(acc[1][1]));
    asm volatile("s_waitcnt vmcnt(7)" ::: "memory");
    if (lane == 0) atomicAdd((unsigned*)(smem + P_CNT), 1u);
    lds_spin(smem, P_CNT, 4u);
    ST(0, 1);
    rd(0, 0, 0); rd(0, 1, 0); rd(0, 2, 0); rd(0, 3, 0);
    SBAR();
#pragma unroll 1
    for (int c = 1; c < 7; c += 2) { step(1, c); step(0, c + 1); }
    asm volatile("s_waitcnt vmcnt(0)" ::: "memory");
    step(1, 7);
    ST(0, 2);
    mm(1, 0); mm(1, 1); mm(1, 2); mm(1, 3);
    ST(0, 3);
#pragma unroll
    for (int ct = 0; ct < 2; ++ct)
#pragma unroll
        for (int tt = 0; tt < 2; ++tt) {
            const int hd0 = (nt & 3) * 8 + wn * 4 + ct * 2;
            const int tile = mt * 4 + wm * 2 + tt;
            if (ISV) {
                char* dst = ws + WS_KV + (size_t)(hd0 + (r >> 4)) * 131072 + tile * 2048 + 1024 + h * 256 + (r & 15) * 16;
                *(u32x4*)(dst) = pack8(acc[ct][tt], 0, 1.f);
                *(u32x4*)(dst + 512) = pack8(acc[ct][tt], 8, 1.f);
            } else {
                const bool isq = nt < 8;
                const float sc = isq ? QSCALE : 1.f;
                const size_t hs = isq ? 65536 : 131072;
                char* dst = isq ? (ws + WS_Q + (size_t)((nt >> 2) * 32 + hd0) * 65536 + tile * 1024 + lane * 16)
                                : (ws + WS_KV + (size_t)hd0 * 131072 + tile * 2048 + lane * 16);
                *(u32x4*)(dst) = pack8(acc[ct][tt], 0, sc);
                *(u32x4*)(dst + hs) = pack8(acc[ct][tt], 8, sc);
            }
        }
}

template <int XM>
DI void proj_loader(const PSrc& src, const unsigned short* __restrict__ x, const unsigned short* __restrict__ wk,
                    const unsigned short* __restrict__ wv, const unsigned short* __restrict__ gw, char* __restrict__ ws,
                    int mt, int nt, int l, unsigned lds0, char* smem) {
    const int lane = threadIdx.x & 63;
    const int r = lane & 31, h = lane >> 5;
    constexpr bool isx = XM != 0, isv = XM == 2;
    const int xtt = nt & 3;
    proj_issue(src, 1, l, lds0); proj_issue(src, 2, l, lds0);
    const int q4 = (lane & 15) >> 2, p4 = lane & 3;
    const unsigned ea = lds0 + P_WX + l * 4096 + (8 * h + q4) * 32 + p4 * 8;
    const int xrow = xtt * 32 + r;
    const unsigned exa = lds0 + P_XS + xrow * 128 + (((2 * l + h) ^ ((xrow >> 1) & 7)) << 4);
    const int gtok = mt * 128 + nt * 8 + 2 * l;
    u32x4 gx0, gx1, gv;
    bf16x8 xe[8];
    if (isx) { lds_spin(smem, P_CNT, 4u); xe[0] = lds_b128(exa); }
#pragma unroll
    for (int c = 1; c < 8; ++c) {
        if (c < 7) WAIT_BAR(8); else WAIT_BAR(0);
        if (isx) xe[c] = lds_b128(exa + (c & 3) * P_STAGE);
        if (c < 6) proj_issue(src, c + 2, l, lds0);
    }
    gx0 = *(const u32x4*)(smem + P_GT + l * 3072 + lane * 16);
    gx1 = *(const u32x4*)(smem + P_GT + l * 3072 + 1024 + lane * 16);
    gv  = *(const u32x4*)(smem + P_GT + l * 3072 + 2048 + lane * 16);
    {
        float s0 = 0.f, s1 = 0.f;
#pragma unroll
        for (int i = 0; i < 4; ++i) {
            const float wl = __uint_as_float(gv[i] << 16), wh = __uint_as_float(gv[i] & 0xffff0000u);
            s0 += __uint_as_float(gx0[i] << 16) * wl + __uint_as_float(gx0[i] & 0xffff0000u) * wh;
            s1 += __uint_as_float(gx1[i] << 16) * wl + __uint_as_float(gx1[i] & 0xffff0000u) * wh;
        }
        s0 = wave_sum63(s0); s1 = wave_sum63(s1);
        if (lane == 63) {
            f32x2 g = {__builtin_amdgcn_rcpf(1.f + __expf(-s0)), __builtin_amdgcn_rcpf(1.f + __expf(-s1))};
            *(f32x2*)((float*)(ws + WS_G) + gtok) = g;
        }
    }
    f32x16 acce;
#pragma unroll
    for (int i = 0; i < 16; ++i) acce[i] = 0.f;
    if (isx) {
        __builtin_amdgcn_s_setprio(3);
#pragma unroll
        for (int c = 0; c < 8; ++c) {
            const bf16x8 we = cat8(lds_tr(ea + c * 512), lds_tr(ea + c * 512 + 128));
            acce = isv ? MFMA32(xe[c], we, acce) : MFMA32(we, xe[c], acce);
        }
    }
    if (isx) {
        f32x4* red = (f32x4*)smem;
#pragma unroll
        for (int g = 0; g < 4; ++g) {
            f32x4 v = {acce[4 * g], acce[4 * g + 1], acce[4 * g + 2], acce[4 * g + 3]};
            red[(l * 4 + g) * 64 + lane] = v;
        }
        unsigned old = 0;
        asm volatile("s_waitcnt lgkmcnt(0)" ::: "memory");
        if (lane == 0) old = atomicAdd((unsigned*)(smem + P_CNT), 1u);
        old = __builtin_amdgcn_readfirstlane(old);
        if (old == 7) {
            asm volatile("" ::: "memory");
#pragma unroll
            for (int g = 0; g < 4; ++g) {
                const f32x4 v = red[g * 64 + lane] + red[(4 + g) * 64 + lane] + red[(8 + g) * 64 + lane] + red[(12 + g) * 64 + lane];
#pragma unroll
                for (int i = 0; i < 4; ++i) acce[4 * g + i] = v[i];
            }
            char* base = ws + WS_KV + (size_t)32 * 131072 + (mt * 4 + xtt) * 2048;
            if (isv) {
                if (r < 16) {
                    *(u32x4*)(base + 1024 + h * 256 + r * 16) = pack8(acce, 0, 1.f);
                    *(u32x4*)(base + 1536 + h * 256 + r * 16) = pack8(acce, 8, 1.f);
                }
            } else {
                *(u32x4*)(base + lane * 16) = pack8(acce, 0, 1.f);
            }
        }
    }
}

__global__ __launch_bounds__(512, 2) void proj_kernel(const unsigned short* __restrict__ x, const unsigned short* __restrict__ wq,
                                                      const unsigned short* __restrict__ wk, const unsigned short* __restrict__ wv,
                                                      const unsigned short* __restrict__ wqkv, const unsigned short* __restrict__ gw,
                                                      char* __restrict__ ws) {
    extern __shared__ __attribute__((aligned(16))) char smem[];
    const unsigned lds0 = (unsigned)(uintptr_t)smem;
    const int w = __builtin_amdgcn_readfirstlane(threadIdx.x >> 6);
    const int b = blockIdx.x, xcd = b & 7, slot = b >> 3;
    const int mt = (xcd & 3) * 4 + (slot & 3), nt = (xcd >> 2) * 8 + (slot >> 2);
    ST(0, 0);
    if (threadIdx.x == 0) *(unsigned*)(smem + P_CNT) = 0u;
    const unsigned short* wsrc = (nt < 4) ? (wq + nt * 128) : (wqkv + (nt - 4) * 128);
    const PSrc src = proj_src(x, wsrc, (nt < 4) ? 512 : 1536, mt, nt, w & 3, threadIdx.x & 63);
    asm volatile("s_waitcnt lgkmcnt(0)\n\ts_barrier" ::: "memory");
    if (w >= 4) {
        if (nt >= 12)     proj_loader<2>(src, x, wk, wv, gw, ws, mt, nt, w - 4, lds0, smem);
        else if (nt >= 8) proj_loader<1>(src, x, wk, wv, gw, ws, mt, nt, w - 4, lds0, smem);
        else              proj_loader<0>(src, x, wk, wv, gw, ws, mt, nt, w - 4, lds0, smem);
        ST(0, 1); ST(0, 2); ST(0, 3);
    } else if (nt >= 12) proj_compute<true>(src, x, wv, gw, ws, mt, nt, w, lds0, smem);
    else                 proj_compute<false>(src, x, wk, gw, ws, mt, nt, w, lds0, smem);
    ST(0, 4);
}

constexpr int A_KV = 0, A_CONST = 131072, A_LDS = A_CONST + 8192;

DI float max16(const f32x16& s) {
    float a = fmaxf(fmaxf(s[0], s[1]), s[2]), b = fmaxf(fmaxf(s[3], s[4]), s[5]);
    a = fmaxf(fmaxf(a, s[6]), s[7]);   b = fmaxf(fmaxf(b, s[8]), s[9]);
    a = fmaxf(fmaxf(a, s[10]), s[11]); b = fmaxf(fmaxf(b, s[12]), s[13]);
    a = fmaxf(fmaxf(a, s[14]), s[15]);
    return fmaxf(a, b);
}
DI float half_max(float m) {
    auto rr = __builtin_amdgcn_permlane32_swap(__float_as_uint(m), __float_as_uint(m), false, false);
    return fmaxf(__uint_as_float(rr[0]), __uint_as_float(rr[1]));
}

#define R_SA "192"
#define R_SB "208"
#define EX(X, i) "v_exp_f32 v[" X "+" #i "], v[" X "+" #i "]\n\t"
#define CV(X, j, a, b) "v_cvt_pk_bf16_f32 v[224+" #j "], v[" X "+" #a "], v[" X "+" #b "]\n\t"
#define HALF(X, Y, OX, OY, NY, QY, WA, RA, WB, RB, WC, RC)                                        \
    EX(X, 0) EX(X, 1) WA                                                                          \
    "v_mfma_f32_32x32x16_bf16 " OY ", %[v1], v[228:231], " OY "\n\t" RA                           \
    EX(X, 2) EX(X, 3) CV(X, 0, 0, 1) EX(X, 4) EX(X, 5) CV(X, 1, 2, 3) WB                          \
    "v_mfma_f32_32x32x16_bf16 v[" Y ":" Y "+15], %[kf], " QY ", " NY "\n\t" RB                    \
    EX(X, 6) EX(X, 7) CV(X, 2, 4, 5) EX(X, 8) EX(X, 9) CV(X, 3, 6, 7) EX(X, 10) EX(X, 11) WC      \
    "v_mfma_f32_32x32x16_bf16 " OX ", %[v0], v[224:227], " OX "\n\t" RC                           \
    CV(X, 4, 8, 9) EX(X, 12) EX(X, 13) CV(X, 5, 10, 11) EX(X, 14) EX(X, 15) CV(X, 6, 12, 13) CV(X, 7, 14, 15)
#define TILE(OFF_V1, OFF_KN, OFF_V0N, P0, P1)                                                     \
    HALF(R_SA, R_SB, "%[oa]", "%[ob]", "%[nb]", "%[qb]",                                          \
         "", "ds_read_b128 %[v1], %[va] offset:" OFF_V1 "\n\t" P0,                                \
         "", "ds_read_b128 %[kf], %[ka] offset:" OFF_KN "\n\t",                                   \
         "s_waitcnt lgkmcnt(2)\n\t", "")                                                          \
    HALF(R_SB, R_SA, "%[ob]", "%[oa]", "%[na]", "%[qa]",                                          \
         "s_waitcnt lgkmcnt(1)\n\t", P1,                                                          \
         "s_waitcnt lgkmcnt(0)\n\t", "",                                                          \
         "", "ds_read_b128 %[v0], %[va] offset:" OFF_V0N "\n\t")
#define ATTN_ASM(PY, PO)                                                                          \
    asm volatile(                                                                                 \
        "ds_read_b128 %[kf], %[ka]\n\t"                                                           \
        "ds_read_b128 %[v1], %[va]\n\t"                                                           \
        "ds_read_b128 %[v0], %[va]\n\t"                                                           \
        "v_mov_b32 v228, 0\n\tv_mov_b32 v229, 0\n\tv_mov_b32 v230, 0\n\tv_mov_b32 v231, 0\n\t"    \
        "s_waitcnt lgkmcnt(0)\n\t"                                                                \
        "v_mfma_f32_32x32x16_bf16 v[192:207], %[kf], %[qa], %[na]\n\t"                            \
        "s_nop 7\n\ts_nop 7\n\t"                                                                  \
        "L_loop_%=:\n\t"                                                                          \
        "s_cmp_lg_u32 %[cnt], %[s1]\n\t"                                                          \
        "s_cbranch_scc1 L_n1_%=\n\t"                                                              \
        "s_waitcnt vmcnt(0)\n\t"                                                                  \
        "s_barrier\n\t"                                                                           \
        "L_n1_%=:\n\t"                                                                            \
        TILE("512", "2048", "2048", "s_setprio 0\n\t", PY)                                        \
        TILE("2560", "4096", "4096", PO, "")                                                      \
        TILE("4608", "6144", "6144", "", "")                                                      \
        HALF(R_SA, R_SB, "%[oa]", "%[ob]", "%[nb]", "%[qb]",                                      \
             "", "ds_read_b128 %[v1], %[va] offset:6656\n\t",                                     \
             "", "ds_read_b128 %[kf], %[ka] offset:8192\n\tv_add_u32 %[va], %[va], %[vs]\n\t",    \
             "s_waitcnt lgkmcnt(2)\n\t", "v_add_u32 %[ka], 0x2000, %[ka]\n\t")                    \
        HALF(R_SB, R_SA, "%[ob]", "%[oa]", "%[na]", "%[qa]",                                      \
             "s_waitcnt lgkmcnt(1)\n\t", "s_add_u32 %[cnt], %[cnt], 1\n\t",                       \
             "s_waitcnt lgkmcnt(0)\n\t", "s_cmp_lg_u32 %[cnt], 16\n\t",                           \
             "", "ds_read_b128 %[v0], %[va]\n\t")                                                 \
        "s_cbranch_scc1 L_loop_%=\n\t"                                                            \
        "s_nop 1\n\t"                                                                             \
        "v_mfma_f32_32x32x16_bf16 %[ob], %[v1], v[228:231], %[ob]\n\t"                            \
        "s_waitcnt lgkmcnt(0)\n\t"                                                                \
        "s_nop 7\n\ts_nop 7\n\t"                                                                  \
        : [oa] "+v"(oa), [ob] "+v"(ob), [ka] "+v"(ka), [va] "+v"(va), [cnt] "+s"(cnt),            \
          [kf] "=&v"(kf), [v0] "=&v"(v0), [v1] "=&v"(v1)                                          \
        : [qa] "v"(qa), [qb] "v"(qb), [na] "v"(na), [nb] "v"(nb), [vs] "v"(vstep), [s1] "s"(sync1) \
        : "memory", "scc",                                                                        \
          "v192", "v193", "v194", "v195", "v196", "v197", "v198", "v199", "v200", "v201", "v202", "v203", "v204", "v205", "v206", "v207", \
          "v208", "v209", "v210", "v211", "v212", "v213", "v214", "v215", "v216", "v217", "v218", "v219", "v220", "v221", "v222", "v223", \
          "v224", "v225", "v226", "v227", "v228", "v229", "v230", "v231")

template <bool YOUNG>
DI void attn_main(unsigned ka, unsigned va, unsigned vstep, const bf16x8& qa, const bf16x8& qb, const f32x16& na, const f32x16& nb,
                  f32x16& oa, f32x16& ob, int sync1) {
    bf16x8 kf, v0, v1;
    int cnt = 0;
    if (YOUNG) ATTN_ASM("s_setprio 1\n\t", "");
    else       ATTN_ASM("", "s_setprio 1\n\t");
}

__global__ __launch_bounds__(512, 2) void attn_kernel(const unsigned short* __restrict__ woA, const unsigned short* __restrict__ woB,
                                                      char* __restrict__ ws) {
    extern __shared__ __attribute__((aligned(16))) char smem[];
    const unsigned lds0 = (unsigned)(uintptr_t)smem;
    const int tid = threadIdx.x, lane = tid & 63;
    const int w = __builtin_amdgcn_readfirstlane(tid >> 6);
    const int r = lane & 31, h = lane >> 5;
    const int b = blockIdx.x, xcd = b & 7, slot = b >> 3;
    const int isB = (slot < 16);
    const int hd = xcd * 4 + ((slot & 15) >> 2);
    const int hh = isB ? 32 + hd : hd;
    const int kvh = isB ? hd : 32;
    const int tile = (slot & 3) * 16 + 2 * w;

    ST(1, 0);
    bf16x8 qa, qb;
    {
        const char* qsrc = ws + WS_Q + (size_t)hh * 65536 + tile * 1024 + lane * 16;
        asm volatile("global_load_dwordx4 %0, %2, off\n\tglobal_load_dwordx4 %1, %2, off offset:1024"
                     : "=&v"(qa), "=&v"(qb) : "v"(qsrc) : "memory");
    }
    const char* kvsrc = ws + WS_KV + (size_t)kvh * 131072 + w * 1024 + lane * 16;
#pragma unroll
    for (int i = 0; i < 4; ++i) glds16(kvsrc + i * 8192, lds0 + A_KV + i * 8192 + w * 1024);
    const int fr = b * 4 + (w >> 1);
    unsigned wo0, wo1, wo2, wo3; float ga, gb;
    {
        const int kk = fr >> 4, ntile = fr & 15;
        const unsigned short* wsrc = ((kk < 32) ? woA : woB) + (size_t)((kk & 31) * 16 + 8 * (w & 1) + 4 * h) * 512 + ntile * 32 + r;
        const float* gsrc = (const float*)(ws + WS_G) + tile * 32 + r;
        asm volatile("global_load_ushort %0, %6, off\n\tglobal_load_ushort %1, %6, off offset:1024\n\t"
                     "global_load_ushort %2, %6, off offset:2048\n\tglobal_load_ushort %3, %6, off offset:3072\n\t"
                     "global_load_dword %4, %7, off\n\tglobal_load_dword %5, %7, off offset:128"
                     : "=&v"(wo0), "=&v"(wo1), "=&v"(wo2), "=&v"(wo3), "=&v"(ga), "=&v"(gb) : "v"(wsrc), "v"(gsrc) : "memory");
    }
    {
        const unsigned one = ((tid & 15) == 0 || (tid & 15) == 4) ? 0x3f803f80u : 0u;
        u32x4 c = {one, one, one, one};
        *(u32x4*)(smem + A_CONST + tid * 16) = c;
    }
    const unsigned kaddr = lds0 + A_KV + lane * 16;
    const unsigned vaddr = (r < 16) ? (lds0 + A_KV + 1024 + h * 256 + r * 16) : (lds0 + A_CONST + (r - 16) * 16);
    const unsigned vstep = (r < 16) ? 8192u : 0u;

    asm volatile("s_waitcnt vmcnt(6) lgkmcnt(0)\n\ts_barrier" : "+v"(qa), "+v"(qb) :: "memory");
    ST(1, 1);
    if (w >= 4) {
#pragma unroll
        for (int i = 0; i < 24; ++i) glds16(kvsrc + 28672 + i * 4096, lds0 + A_KV + 28672 + i * 4096 + w * 1024);
    }

    f32x16 zero;
#pragma unroll
    for (int i = 0; i < 16; ++i) zero[i] = 0.f;
    f32x16 na, nb, oa = zero, ob = zero;
    {
        const bf16x8 k0 = lds_b128(kaddr);
        const f32x16 sa = MFMA32(k0, qa, zero);
        const f32x16 sb = MFMA32(k0, qb, zero);
        const float ma = -half_max(max16(sa)) - 40.f, mb = -half_max(max16(sb)) - 40.f;
#pragma unroll
        for (int i = 0; i < 16; ++i) { na[i] = ma; nb[i] = mb; }
    }
    ST(1, 2);
    if (w >= 4) attn_main<true>(kaddr, vaddr, vstep, qa, qb, na, nb, oa, ob, 2);
    else        attn_main<false>(kaddr, vaddr, vstep, qa, qb, na, nb, oa, ob, 3);
    ST(1, 3);
    asm volatile("" : "+v"(wo0), "+v"(wo1), "+v"(wo2), "+v"(wo3), "+v"(ga), "+v"(gb));
    {
        uint2 pk;
        pk.x = wo0 | (wo1 << 16); pk.y = wo2 | (wo3 << 16);
        *(uint2*)(ws + WS_WO + (size_t)fr * 1024 + lane * 16 + (w & 1) * 8) = pk;
    }

    {
        bool bad = !(oa[8] < 1.329228e36f) || !(ob[8] < 1.329228e36f);
#pragma unroll
        for (int i = 0; i < 8; ++i) bad = bad || !(fabsf(oa[i]) <= 3.0e38f) || !(fabsf(ob[i]) <= 3.0e38f);
        if (__any(bad)) {
            float ma = -INFINITY, mb = -INFINITY;
#pragma unroll 1
            for (int t = 0; t < 64; ++t) {
                const bf16x8 kf = lds_b128(kaddr + t * 2048);
                ma = fmaxf(ma, max16(MFMA32(kf, qa, zero)));
                mb = fmaxf(mb, max16(MFMA32(kf, qb, zero)));
            }
            ma = -half_max(ma); mb = -half_max(mb);
#pragma unroll
            for (int i = 0; i < 16; ++i) { na[i] = ma; nb[i] = mb; }
            oa = zero; ob = zero;
            attn_main<false>(kaddr, vaddr, vstep, qa, qb, na, nb, oa, ob, -1);
        }
    }
    const float sa = (isB ? 1.f - ga : ga) / oa[8], sb = (isB ? 1.f - gb : gb) / ob[8];
    char* odst = ws + WS_O + (size_t)tile * 65536 + hh * 1024 + lane * 16;
    *(u32x4*)(odst) = pack8(oa, 0, sa);
    *(u32x4*)(odst + 65536) = pack8(ob, 0, sb);
    ST(1, 4);
}

constexpr int O_LDS = 4 * 3 * 4096;

__global__ __launch_bounds__(256, 1) void out_kernel(const char* __restrict__ ws, unsigned short* __restrict__ out) {
    extern __shared__ __attribute__((aligned(16))) char smem[];
    const int tid = threadIdx.x, lane = tid & 63;
    const int w = __builtin_amdgcn_readfirstlane(tid >> 6);
    const int r = lane & 31, h = lane >> 5;
    const int b = blockIdx.x, xcd = b & 7, slot = b >> 3;
    const int mt = xcd * 4 + (slot & 3), nt = slot >> 2;

    ST(2, 0);
    const char* osrc = ws + WS_O + (size_t)(mt * 2) * 65536 + (w * 16) * 1024 + lane * 16;
    const char* wsrc = ws + WS_WO + (size_t)(w * 16) * 16384 + (nt * 2) * 1024 + lane * 16;
#define O_ROT 0
    const int rot = O_ROT == 1 ? ((2 * nt + 4 * (slot & 3) + ((slot >> 1) & 1)) & 15) : (O_ROT == 2 ? 2 * xcd : 0);
    bf16x8 of[16][2], wf[16][2];
#pragma unroll
    for (int k = 0; k < 16; ++k) {
        const int kk = (k + rot) & 15;
        of[k][0] = *(const bf16x8*)(osrc + kk * 1024);
        of[k][1] = *(const bf16x8*)(osrc + 65536 + kk * 1024);
        wf[k][0] = *(const bf16x8*)(wsrc + kk * 16384);
        wf[k][1] = *(const bf16x8*)(wsrc + kk * 16384 + 1024);
    }
    SBAR();
    f32x16 acc[4];
#pragma unroll
    for (int i = 0; i < 16; ++i) { acc[0][i] = 0.f; acc[1][i] = 0.f; acc[2][i] = 0.f; acc[3][i] = 0.f; }
#pragma unroll
    for (int k = 0; k < 16; ++k)
#pragma unroll
        for (int q = 0; q < 4; ++q) acc[q] = MFMA32(wf[k][q >> 1], of[k][q & 1], acc[q]);
    ST(2, 1);
    f32x16 fin;
#pragma unroll
    for (int q = 0; q < 4; ++q) {
        if (q == w) { fin = acc[q]; }
        else {
            char* dst = smem + (q * 3 + ((w - q - 1) & 3)) * 4096 + lane * 16;
#pragma unroll
            for (int g = 0; g < 4; ++g) {
                f32x4 v = {acc[q][4 * g], acc[q][4 * g + 1], acc[q][4 * g + 2], acc[q][4 * g + 3]};
                *(f32x4*)(dst + g * 1024) = v;
            }
        }
    }
    __syncthreads();
    ST(2, 2);
#pragma unroll
    for (int p = 0; p < 3; ++p)
#pragma unroll
        for (int g = 0; g < 4; ++g) {
            const f32x4 v = *(const f32x4*)(smem + (w * 3 + p) * 4096 + g * 1024 + lane * 16);
#pragma unroll
            for (int i = 0; i < 4; ++i) fin[4 * g + i] += v[i];
        }
    unsigned short* drow = out + (size_t)(mt * 64 + (w & 1) * 32 + r) * 512 + nt * 64 + (w >> 1) * 32 + h * 8;
#pragma unroll
    for (int g = 0; g < 4; g += 2) {
        unsigned a0 = pk_bf16(fin[4 * g], fin[4 * g + 1]), a1 = pk_bf16(fin[4 * g + 2], fin[4 * g + 3]);
        unsigned b0 = pk_bf16(fin[4 * g + 4], fin[4 * g + 5]), b1 = pk_bf16(fin[4 * g + 6], fin[4 * g + 7]);
        auto r0 = __builtin_amdgcn_permlane32_swap(a0, b0, false, false);
        auto r1 = __builtin_amdgcn_permlane32_swap(a1, b1, false, false);
        u32x4 res = {r0[0], r1[0], r0[1], r1[1]};
        *(u32x4*)(drow + g * 8) = res;
    }
    ST(2, 3);
}

extern "C" void kernel_launch(void* const* d_in, const int* in_sizes, int n_in, void* d_out, int out_size, void* d_ws,
                              size_t ws_size, hipStream_t stream) {
    const unsigned short* x    = (const unsigned short*)d_in[0];
    const unsigned short* wq   = (const unsigned short*)d_in[1];
    const unsigned short* wk   = (const unsigned short*)d_in[2];
    const unsigned short* wv   = (const unsigned short*)d_in[3];
    const unsigned short* woA  = (const unsigned short*)d_in[4];
    const unsigned short* wqkv = (const unsigned short*)d_in[6];
    const unsigned short* woB  = (const unsigned short*)d_in[7];
    const unsigned short* gw   = (const unsigned short*)d_in[8];
    char* ws = (char*)d_ws;
    (void)hipFuncSetAttribute((const void*)proj_kernel, hipFuncAttributeMaxDynamicSharedMemorySize, P_LDS);
    (void)hipFuncSetAttribute((const void*)attn_kernel, hipFuncAttributeMaxDynamicSharedMemorySize, A_LDS);
    (void)hipFuncSetAttribute((const void*)out_kernel, hipFuncAttributeMaxDynamicSharedMemorySize, O_LDS);
    proj_kernel<<<256, 512, P_LDS, stream>>>(x, wq, wk, wv, wqkv, gw, ws);
    attn_kernel<<<256, 512, A_LDS, stream>>>(woA, woB, ws);
    out_kernel<<<256, 256, O_LDS, stream>>>(ws, (unsigned short*)d_out);
}
```

```cpp
#include <hip/hip_runtime.h>
#include <hip/hip_bf16.h>
#include <stdint.h>

typedef short    bf16x8 __attribute__((ext_vector_type(8)));
typedef short    s16x4  __attribute__((ext_vector_type(4)));
typedef float    f32x16 __attribute__((ext_vector_type(16)));
typedef float    f32x4  __attribute__((ext_vector_type(4)));
typedef float    f32x2  __attribute__((ext_vector_type(2)));
typedef unsigned u32x4  __attribute__((ext_vector_type(4)));
typedef __bf16   bf16x2v __attribute__((ext_vector_type(2)));

#define DI __device__ __forceinline__
#define MFMA32(a, b, c) __builtin_amdgcn_mfma_f32_32x32x16_bf16((a), (b), (c), 0, 0, 0)
#define LDS_AS __attribute__((address_space(3)))

constexpr size_t WS_Q  = 0;
constexpr size_t WS_KV = WS_Q + (size_t)64 * 65536;
constexpr size_t WS_O  = WS_KV + (size_t)34 * 131072;
constexpr size_t WS_WO = WS_O + (size_t)64 * 65536;
constexpr size_t WS_G  = WS_WO + (size_t)1024 * 1024;

#define ST(k, i)
#define CODEPF 1
constexpr float QSCALE = 0.25f * 1.4426950408889634f;

DI unsigned pk_bf16(float lo, float hi) {
    f32x2 v = {lo, hi};
    bf16x2v r = __builtin_convertvector(v, bf16x2v);
    return __builtin_bit_cast(unsigned, r);
}
DI float bf2f(unsigned short u) { return __uint_as_float(((unsigned)u) << 16); }

DI void glds16(const void* g, unsigned lds_base) {
    unsigned sv;
    asm volatile("s_mov_b32 %0, m0\n\ts_mov_b32 m0, %2\n\ts_nop 0\n\tglobal_load_lds_dwordx4 %1, off\n\ts_mov_b32 m0, %0"
                 : "=&s"(sv) : "v"(g), "s"(lds_base) : "memory");
}
#define WAIT_BAR(N) asm volatile("s_waitcnt vmcnt(" #N ") lgkmcnt(0)\n\ts_barrier" ::: "memory")

DI bf16x8 lds_b128(unsigned addr) { return *(const LDS_AS bf16x8*)(uintptr_t)addr; }
DI s16x4 lds_tr(unsigned addr) {
    return __builtin_bit_cast(s16x4, __builtin_amdgcn_ds_read_tr16_b64_v4i16((LDS_AS s16x4*)(uintptr_t)addr));
}
DI bf16x8 cat8(s16x4 a, s16x4 b) { return __builtin_shufflevector(a, b, 0, 1, 2, 3, 4, 5, 6, 7); }

DI u32x4 pack8(const f32x16& a, int base, float sc) {
    u32x4 r;
    r[0] = pk_bf16(a[base + 0] * sc, a[base + 1] * sc);
    r[1] = pk_bf16(a[base + 2] * sc, a[base + 3] * sc);
    r[2] = pk_bf16(a[base + 4] * sc, a[base + 5] * sc);
    r[3] = pk_bf16(a[base + 6] * sc, a[base + 7] * sc);
    return r;
}

constexpr int P_XS = 0, P_WS = 16384, P_STAGE = 32768, P_WX = 4 * P_STAGE, P_GT = P_WX + 16384, P_CNT = P_GT + 12288, P_LDS = P_CNT + 16;
#define SBAR() __builtin_amdgcn_sched_barrier(0)

struct PSrc { const unsigned short *xs0, *xs1, *wsr; int ldw; };
DI PSrc proj_src(const unsigned short* x, const unsigned short* wsrc, int ldw, int mt, int nt, int l, int lane) {
    PSrc s;
    const int row = 32 * l + (lane >> 3);
    s.xs0 = x + (size_t)(mt * 128 + row) * 512 + ((lane & 7) ^ ((row >> 1) & 7)) * 8;
    s.xs1 = x + (size_t)(mt * 128 + row + 8) * 512 + ((lane & 7) ^ (((row + 8) >> 1) & 7)) * 8;
    const int krow = 16 * l + (lane >> 4);
    s.wsr = wsrc + (size_t)krow * ldw + ((lane & 15) ^ (((lane >> 4) & 3) << 2)) * 8;
    s.ldw = ldw;
    return s;
}
DI void proj_issue(const PSrc& s, int c, int l, unsigned lds0) {
    const unsigned st = lds0 + (c & 3) * P_STAGE + l * 4096;
    glds16(s.xs0 + c * 64, st + P_XS);
    glds16(s.xs1 + c * 64, st + P_XS + 1024);
    glds16(s.xs0 + c * 64 + 16 * 512, st + P_XS + 2048);
    glds16(s.xs1 + c * 64 + 16 * 512, st + P_XS + 3072);
#pragma unroll
    for (int i = 0; i < 4; ++i) glds16(s.wsr + (size_t)(c * 64 + 4 * i) * s.ldw, st + P_WS + i * 1024);
}
DI float wave_sum63(float v) {
#define DPP_ADD(ctrl, rm) v += __int_as_float(__builtin_amdgcn_update_dpp(0, __float_as_int(v), ctrl, rm, 0xf, false))
    DPP_ADD(0x111, 0xf); DPP_ADD(0x112, 0xf); DPP_ADD(0x114, 0xf); DPP_ADD(0x118, 0xf);
    DPP_ADD(0x142, 0xa); DPP_ADD(0x143, 0xc);
#undef DPP_ADD
    return v;
}
DI void lds_spin(const char* smem, int off, unsigned target) {
    while (__builtin_amdgcn_readfirstlane(*(const volatile unsigned*)(smem + off)) < target) __builtin_amdgcn_s_sleep(1);
    asm volatile("" ::: "memory");
}

template <bool ISV>
DI void proj_compute(const PSrc& src, const unsigned short* __restrict__ x, const unsigned short* __restrict__ wx,
                     const unsigned short* __restrict__ gw, char* __restrict__ ws, int mt, int nt, int w, unsigned lds0, char* smem) {
    const int lane = threadIdx.x & 63;
    const int wm = w >> 1, wn = w & 1;
    const int r = lane & 31, h = lane >> 5;
    proj_issue(src, 0, w, lds0);
    {
        const unsigned short* es = wx + (size_t)((lane >> 5) * 64 + 16 * w) * 16 + (lane & 31) * 8;
#pragma unroll
        for (int i = 0; i < 4; ++i) glds16(es + i * 2048, lds0 + P_WX + w * 4096 + i * 1024);
        const unsigned short* px = x + (size_t)(mt * 128 + nt * 8 + 2 * w) * 512 + lane * 8;
        glds16(px, lds0 + P_GT + w * 3072);
        glds16(px + 512, lds0 + P_GT + w * 3072 + 1024);
        glds16(gw + lane * 8, lds0 + P_GT + w * 3072 + 2048);
    }
    unsigned xo[4][2];
#pragma unroll
    for (int tt = 0; tt < 2; ++tt) {
        const int row = wm * 64 + tt * 32 + r;
#pragma unroll
        for (int ks = 0; ks < 4; ++ks) xo[ks][tt] = lds0 + P_XS + row * 128 + (((2 * ks + h) ^ ((row >> 1) & 7)) << 4);
    }
    const int q4 = (lane & 15) >> 2, p4 = lane & 3, g1 = (lane >> 4) & 1;
    unsigned wa[2];
#pragma unroll
    for (int ct = 0; ct < 2; ++ct)
        wa[ct] = lds0 + P_WS + (8 * h + q4) * 256 + ((4 * ((2 * wn + ct) ^ q4) + 2 * g1 + (p4 >> 1)) << 4) + (p4 & 1) * 8;
    f32x16 acc[2][2];
#pragma unroll
    for (int i = 0; i < 16; ++i) { acc[0][0][i] = 0.f; acc[0][1][i] = 0.f; acc[1][0][i] = 0.f; acc[1][1][i] = 0.f; }
    bf16x8 fx[2][4][2], fw[2][4][2];
    auto mm = [&](int bf, int ks) {
#pragma unroll
        for (int ct = 0; ct < 2; ++ct)
#pragma unroll
            for (int tt = 0; tt < 2; ++tt)
                acc[ct][tt] = ISV ? MFMA32(fx[bf][ks][tt], fw[bf][ks][ct], acc[ct][tt]) : MFMA32(fw[bf][ks][ct], fx[bf][ks][tt], acc[ct][tt]);
    };
    auto rd = [&](int bf, int ks, unsigned so) {
#pragma unroll
        for (int tt = 0; tt < 2; ++tt) fx[bf][ks][tt] = lds_b128(xo[ks][tt] + so);
#pragma unroll
        for (int ct = 0; ct < 2; ++ct) fw[bf][ks][ct] = cat8(lds_tr(wa[ct] + so + ks * 4096), lds_tr(wa[ct] + so + ks * 4096 + 1024));
    };
    auto step = [&](int bf, int c) {
        asm volatile("s_waitcnt lgkmcnt(0)\n\ts_barrier" ::: "memory");
        const unsigned so = (c & 3) * P_STAGE;
#pragma unroll
        for (int ks = 0; ks < 4; ++ks) { rd(bf, ks, so); mm(bf ^ 1, ks); }
#pragma unroll
        for (int i = 0; i < 12; ++i) {
            __builtin_amdgcn_sched_group_barrier(0x008, 1, 0);
            __builtin_amdgcn_sched_group_barrier(0x002, 1, 0);
            __builtin_amdgcn_sched_group_barrier(0x100, 2, 0);
        }
        __builtin_amdgcn_sched_group_barrier(0x008, 4, 0);
        SBAR();
    };
    asm volatile("" : "+v"(xo[0][0]), "+v"(xo[0][1]), "+v"(xo[1][0]), "+v"(xo[1][1]), "+v"(xo[2][0]), "+v"(xo[2][1]),
                      "+v"(xo[3][0]), "+v"(xo[3][1]), "+v"(wa[0]), "+v"(wa[1]),
                      "+v"(acc[0][0]), "+v"(acc[0][1]), "+v"(acc[1][0]), "+v"(acc[1][1]));
    asm volatile("s_waitcnt vmcnt(7)" ::: "memory");
    if (lane == 0) atomicAdd((unsigned*)(smem + P_CNT), 1u);
    lds_spin(smem, P_CNT, 4u);
    ST(0, 1);
    rd(0, 0, 0); rd(0, 1, 0); rd(0, 2, 0); rd(0, 3, 0);
    SBAR();
#pragma unroll 1
    for (int c = 1; c < 7; c += 2) { step(1, c); step(0, c + 1); }
    asm volatile("s_waitcnt vmcnt(0)" ::: "memory");
    step(1, 7);
    ST(0, 2);
    mm(1, 0); mm(1, 1); mm(1, 2); mm(1, 3);
    ST(0, 3);
#pragma unroll
    for (int ct = 0; ct < 2; ++ct)
#pragma unroll
        for (int tt = 0; tt < 2; ++tt) {
            const int hd0 = (nt & 3) * 8 + wn * 4 + ct * 2;
            const int tile = mt * 4 + wm * 2 + tt;
            if (ISV) {
                char* dst = ws + WS_KV + (size_t)(hd0 + (r >> 4)) * 131072 + tile * 2048 + 1024 + h * 256 + (r & 15) * 16;
                *(u32x4*)(dst) = pack8(acc[ct][tt], 0, 1.f);
                *(u32x4*)(dst + 512) = pack8(acc[ct][tt], 8, 1.f);
            } else {
                const bool isq = nt < 8;
                const float sc = isq ? QSCALE : 1.f;
                const size_t hs = isq ? 65536 : 131072;
                char* dst = isq ? (ws + WS_Q + (size_t)((nt >> 2) * 32 + hd0) * 65536 + tile * 1024 + lane * 16)
                                : (ws + WS_KV + (size_t)hd0 * 131072 + tile * 2048 + lane * 16);
                *(u32x4*)(dst) = pack8(acc[ct][tt], 0, sc);
                *(u32x4*)(dst + hs) = pack8(acc[ct][tt], 8, sc);
            }
        }
}

template <int XM>
DI void proj_loader(const PSrc& src, const unsigned short* __restrict__ x, const unsigned short* __restrict__ wk,
                    const unsigned short* __restrict__ wv, const unsigned short* __restrict__ gw, char* __restrict__ ws,
                    int mt, int nt, int l, unsigned lds0, char* smem) {
    const int lane = threadIdx.x & 63;
    const int r = lane & 31, h = lane >> 5;
    constexpr bool isx = XM != 0, isv = XM == 2;
    const int xtt = nt & 3;
    proj_issue(src, 1, l, lds0); proj_issue(src, 2, l, lds0);
    const int q4 = (lane & 15) >> 2, p4 = lane & 3;
    const unsigned ea = lds0 + P_WX + l * 4096 + (8 * h + q4) * 32 + p4 * 8;
    const int xrow = xtt * 32 + r;
    const unsigned exa = lds0 + P_XS + xrow * 128 + (((2 * l + h) ^ ((xrow >> 1) & 7)) << 4);
    const int gtok = mt * 128 + nt * 8 + 2 * l;
    u32x4 gx0, gx1, gv;
    bf16x8 xe[8];
    if (isx) { lds_spin(smem, P_CNT, 4u); xe[0] = lds_b128(exa); }
#pragma unroll
    for (int c = 1; c < 8; ++c) {
        if (c < 7) WAIT_BAR(8); else WAIT_BAR(0);
        if (isx) xe[c] = lds_b128(exa + (c & 3) * P_STAGE);
        if (c < 6) proj_issue(src, c + 2, l, lds0);
    }
    gx0 = *(const u32x4*)(smem + P_GT + l * 3072 + lane * 16);
    gx1 = *(const u32x4*)(smem + P_GT + l * 3072 + 1024 + lane * 16);
    gv  = *(const u32x4*)(smem + P_GT + l * 3072 + 2048 + lane * 16);
    {
        float s0 = 0.f, s1 = 0.f;
#pragma unroll
        for (int i = 0; i < 4; ++i) {
            const float wl = __uint_as_float(gv[i] << 16), wh = __uint_as_float(gv[i] & 0xffff0000u);
            s0 += __uint_as_float(gx0[i] << 16) * wl + __uint_as_float(gx0[i] & 0xffff0000u) * wh;
            s1 += __uint_as_float(gx1[i] << 16) * wl + __uint_as_float(gx1[i] & 0xffff0000u) * wh;
        }
        s0 = wave_sum63(s0); s1 = wave_sum63(s1);
        if (lane == 63) {
            f32x2 g = {__builtin_amdgcn_rcpf(1.f + __expf(-s0)), __builtin_amdgcn_rcpf(1.f + __expf(-s1))};
            *(f32x2*)((float*)(ws + WS_G) + gtok) = g;
        }
    }
    f32x16 acce;
#pragma unroll
    for (int i = 0; i < 16; ++i) acce[i] = 0.f;
    if (isx) {
        __builtin_amdgcn_s_setprio(3);
#pragma unroll
        for (int c = 0; c < 8; ++c) {
            const bf16x8 we = cat8(lds_tr(ea + c * 512), lds_tr(ea + c * 512 + 128));
            acce = isv ? MFMA32(xe[c], we, acce) : MFMA32(we, xe[c], acce);
        }
    }
    if (isx) {
        f32x4* red = (f32x4*)smem;
#pragma unroll
        for (int g = 0; g < 4; ++g) {
            f32x4 v = {acce[4 * g], acce[4 * g + 1], acce[4 * g + 2], acce[4 * g + 3]};
            red[(l * 4 + g) * 64 + lane] = v;
        }
        unsigned old = 0;
        asm volatile("s_waitcnt lgkmcnt(0)" ::: "memory");
        if (lane == 0) old = atomicAdd((unsigned*)(smem + P_CNT), 1u);
        old = __builtin_amdgcn_readfirstlane(old);
        if (old == 7) {
            asm volatile("" ::: "memory");
#pragma unroll
            for (int g = 0; g < 4; ++g) {
                const f32x4 v = red[g * 64 + lane] + red[(4 + g) * 64 + lane] + red[(8 + g) * 64 + lane] + red[(12 + g) * 64 + lane];
#pragma unroll
                for (int i = 0; i < 4; ++i) acce[4 * g + i] = v[i];
            }
            char* base = ws + WS_KV + (size_t)32 * 131072 + (mt * 4 + xtt) * 2048;
            if (isv) {
                if (r < 16) {
                    *(u32x4*)(base + 1024 + h * 256 + r * 16) = pack8(acce, 0, 1.f);
                    *(u32x4*)(base + 1536 + h * 256 + r * 16) = pack8(acce, 8, 1.f);
                }
            } else {
                *(u32x4*)(base + lane * 16) = pack8(acce, 0, 1.f);
            }
        }
    }
}

__global__ __launch_bounds__(512, 2) void proj_kernel(const unsigned short* __restrict__ x, const unsigned short* __restrict__ wq,
                                                      const unsigned short* __restrict__ wk, const unsigned short* __restrict__ wv,
                                                      const unsigned short* __restrict__ wqkv, const unsigned short* __restrict__ gw,
                                                      char* __restrict__ ws) {
    extern __shared__ __attribute__((aligned(16))) char smem[];
    const unsigned lds0 = (unsigned)(uintptr_t)smem;
    const int w = __builtin_amdgcn_readfirstlane(threadIdx.x >> 6);
    const int b = blockIdx.x, xcd = b & 7, slot = b >> 3;
    const int mt = (xcd & 3) * 4 + (slot & 3), nt = (xcd >> 2) * 8 + (slot >> 2);
    ST(0, 0);
    unsigned cpf[CODEPF];
    if (w >= 4) {
        unsigned long long pc; int len;
        asm volatile("s_getpc_b64 %0\n\tpf_text_begin:\n\ts_mov_b32 %1, pf_text_end-pf_text_begin" : "=s"(pc), "=s"(len));
#pragma unroll
        for (int i = 0; i < CODEPF; ++i) {
            int off = (i * 256 + (w - 4) * 64 + (int)(threadIdx.x & 63)) * 128;
            off = off < len ? off : 0;
            asm volatile("global_load_dword %0, %1, %2" : "=&v"(cpf[i]) : "v"(off), "s"(pc) : "memory");
        }
    }
    if (threadIdx.x == 0) *(unsigned*)(smem + P_CNT) = 0u;
    const unsigned short* wsrc = (nt < 4) ? (wq + nt * 128) : (wqkv + (nt - 4) * 128);
    const PSrc src = proj_src(x, wsrc, (nt < 4) ? 512 : 1536, mt, nt, w & 3, threadIdx.x & 63);
    asm volatile("s_waitcnt lgkmcnt(0)\n\ts_barrier" ::: "memory");
    if (w >= 4) {
        if (nt >= 12)     proj_loader<2>(src, x, wk, wv, gw, ws, mt, nt, w - 4, lds0, smem);
        else if (nt >= 8) proj_loader<1>(src, x, wk, wv, gw, ws, mt, nt, w - 4, lds0, smem);
        else              proj_loader<0>(src, x, wk, wv, gw, ws, mt, nt, w - 4, lds0, smem);
#pragma unroll
        for (int i = 0; i < CODEPF; ++i) asm volatile("" :: "v"(cpf[i]));
        ST(0, 1); ST(0, 2); ST(0, 3);
    } else if (nt >= 12) proj_compute<true>(src, x, wv, gw, ws, mt, nt, w, lds0, smem);
    else                 proj_compute<false>(src, x, wk, gw, ws, mt, nt, w, lds0, smem);
    ST(0, 4);
}

constexpr int A_KV = 0, A_CONST = 131072, A_LDS = A_CONST + 8192;

DI float max16(const f32x16& s) {
    float a = fmaxf(fmaxf(s[0], s[1]), s[2]), b = fmaxf(fmaxf(s[3], s[4]), s[5]);
    a = fmaxf(fmaxf(a, s[6]), s[7]);   b = fmaxf(fmaxf(b, s[8]), s[9]);
    a = fmaxf(fmaxf(a, s[10]), s[11]); b = fmaxf(fmaxf(b, s[12]), s[13]);
    a = fmaxf(fmaxf(a, s[14]), s[15]);
    return fmaxf(a, b);
}
DI float half_max(float m) {
    auto rr = __builtin_amdgcn_permlane32_swap(__float_as_uint(m), __float_as_uint(m), false, false);
    return fmaxf(__uint_as_float(rr[0]), __uint_as_float(rr[1]));
}

#define R_SA "192"
#define R_SB "208"
#define EX(X, i) "v_exp_f32 v[" X "+" #i "], v[" X "+" #i "]\n\t"
#define CV(X, j, a, b) "v_cvt_pk_bf16_f32 v[224+" #j "], v[" X "+" #a "], v[" X "+" #b "]\n\t"
#define HALF(X, Y, OX, OY, NY, QY, WA, RA, WB, RB, WC, RC)                                        \
    EX(X, 0) EX(X, 1) WA                                                                          \
    "v_mfma_f32_32x32x16_bf16 " OY ", %[v1], v[228:231], " OY "\n\t" RA                           \
    EX(X, 2) EX(X, 3) CV(X, 0, 0, 1) EX(X, 4) EX(X, 5) CV(X, 1, 2, 3) WB                          \
    "v_mfma_f32_32x32x16_bf16 v[" Y ":" Y "+15], %[kf], " QY ", " NY "\n\t" RB                    \
    EX(X, 6) EX(X, 7) CV(X, 2, 4, 5) EX(X, 8) EX(X, 9) CV(X, 3, 6, 7) EX(X, 10) EX(X, 11) WC      \
    "v_mfma_f32_32x32x16_bf16 " OX ", %[v0], v[224:227], " OX "\n\t" RC                           \
    CV(X, 4, 8, 9) EX(X, 12) EX(X, 13) CV(X, 5, 10, 11) EX(X, 14) EX(X, 15) CV(X, 6, 12, 13) CV(X, 7, 14, 15)
#define TILE(OFF_V1, OFF_KN, OFF_V0N, P0, P1)                                                     \
    HALF(R_SA, R_SB, "%[oa]", "%[ob]", "%[nb]", "%[qb]",                                          \
         "", "ds_read_b128 %[v1], %[va] offset:" OFF_V1 "\n\t" P0,                                \
         "", "ds_read_b128 %[kf], %[ka] offset:" OFF_KN "\n\t",                                   \
         "s_waitcnt lgkmcnt(2)\n\t", "")                                                          \
    HALF(R_SB, R_SA, "%[ob]", "%[oa]", "%[na]", "%[qa]",                                          \
         "s_waitcnt lgkmcnt(1)\n\t", P1,                                                          \
         "s_waitcnt lgkmcnt(0)\n\t", "",                                                          \
         "", "ds_read_b128 %[v0], %[va] offset:" OFF_V0N "\n\t")
#define ATTN_ASM(PY, PO)                                                                          \
    asm volatile(                                                                                 \
        "ds_read_b128 %[kf], %[ka]\n\t"                                                           \
        "ds_read_b128 %[v1], %[va]\n\t"                                                           \
        "ds_read_b128 %[v0], %[va]\n\t"                                                           \
        "v_mov_b32 v228, 0\n\tv_mov_b32 v229, 0\n\tv_mov_b32 v230, 0\n\tv_mov_b32 v231, 0\n\t"    \
        "s_waitcnt lgkmcnt(0)\n\t"                                                                \
        "v_mfma_f32_32x32x16_bf16 v[192:207], %[kf], %[qa], %[na]\n\t"                            \
        "s_nop 7\n\ts_nop 7\n\t"                                                                  \
        "L_loop_%=:\n\t"                                                                          \
        "s_cmp_lg_u32 %[cnt], %[s1]\n\t"                                                          \
        "s_cbranch_scc1 L_n1_%=\n\t"                                                              \
        "s_waitcnt vmcnt(0)\n\t"                                                                  \
        "s_barrier\n\t"                                                                           \
        "L_n1_%=:\n\t"                                                                            \
        TILE("512", "2048", "2048", "s_setprio 0\n\t", PY)                                        \
        TILE("2560", "4096", "4096", PO, "")                                                      \
        TILE("4608", "6144", "6144", "", "")                                                      \
        HALF(R_SA, R_SB, "%[oa]", "%[ob]", "%[nb]", "%[qb]",                                      \
             "", "ds_read_b128 %[v1], %[va] offset:6656\n\t",                                     \
             "", "ds_read_b128 %[kf], %[ka] offset:8192\n\tv_add_u32 %[va], %[va], %[vs]\n\t",    \
             "s_waitcnt lgkmcnt(2)\n\t", "v_add_u32 %[ka], 0x2000, %[ka]\n\t")                    \
        HALF(R_SB, R_SA, "%[ob]", "%[oa]", "%[na]", "%[qa]",                                      \
             "s_waitcnt lgkmcnt(1)\n\t", "s_add_u32 %[cnt], %[cnt], 1\n\t",                       \
             "s_waitcnt lgkmcnt(0)\n\t", "s_cmp_lg_u32 %[cnt], 16\n\t",                           \
             "", "ds_read_b128 %[v0], %[va]\n\t")                                                 \
        "s_cbranch_scc1 L_loop_%=\n\t"                                                            \
        "s_nop 1\n\t"                                                                             \
        "v_mfma_f32_32x32x16_bf16 %[ob], %[v1], v[228:231], %[ob]\n\t"                            \
        "s_waitcnt lgkmcnt(0)\n\t"                                                                \
        "s_nop 7\n\ts_nop 7\n\t"                                                                  \
        : [oa] "+v"(oa), [ob] "+v"(ob), [ka] "+v"(ka), [va] "+v"(va), [cnt] "+s"(cnt),            \
          [kf] "=&v"(kf), [v0] "=&v"(v0), [v1] "=&v"(v1)                                          \
        : [qa] "v"(qa), [qb] "v"(qb), [na] "v"(na), [nb] "v"(nb), [vs] "v"(vstep), [s1] "s"(sync1) \
        : "memory", "scc",                                                                        \
          "v192", "v193", "v194", "v195", "v196", "v197", "v198", "v199", "v200", "v201", "v202", "v203", "v204", "v205", "v206", "v207", \
          "v208", "v209", "v210", "v211", "v212", "v213", "v214", "v215", "v216", "v217", "v218", "v219", "v220", "v221", "v222", "v223", \
          "v224", "v225", "v226", "v227", "v228", "v229", "v230", "v231")

template <bool YOUNG>
DI void attn_main(unsigned ka, unsigned va, unsigned vstep, const bf16x8& qa, const bf16x8& qb, const f32x16& na, const f32x16& nb,
                  f32x16& oa, f32x16& ob, int sync1) {
    bf16x8 kf, v0, v1;
    int cnt = 0;
    if (YOUNG) ATTN_ASM("s_setprio 1\n\t", "");
    else       ATTN_ASM("", "s_setprio 1\n\t");
}

__global__ __launch_bounds__(512, 2) void attn_kernel(const unsigned short* __restrict__ woA, const unsigned short* __restrict__ woB,
                                                      char* __restrict__ ws) {
    extern __shared__ __attribute__((aligned(16))) char smem[];
    const unsigned lds0 = (unsigned)(uintptr_t)smem;
    const int tid = threadIdx.x, lane = tid & 63;
    const int w = __builtin_amdgcn_readfirstlane(tid >> 6);
    const int r = lane & 31, h = lane >> 5;
    const int b = blockIdx.x, xcd = b & 7, slot = b >> 3;
    const int isB = (slot < 16);
    const int hd = xcd * 4 + ((slot & 15) >> 2);
    const int hh = isB ? 32 + hd : hd;
    const int kvh = isB ? hd : 32;
    const int tile = (slot & 3) * 16 + 2 * w;

    ST(1, 0);
    bf16x8 qa, qb;
    {
        const char* qsrc = ws + WS_Q + (size_t)hh * 65536 + tile * 1024 + lane * 16;
        asm volatile("global_load_dwordx4 %0, %2, off\n\tglobal_load_dwordx4 %1, %2, off offset:1024"
                     : "=&v"(qa), "=&v"(qb) : "v"(qsrc) : "memory");
    }
    const char* kvsrc = ws + WS_KV + (size_t)kvh * 131072 + w * 1024 + lane * 16;
#pragma unroll
    for (int i = 0; i < 4; ++i) glds16(kvsrc + i * 8192, lds0 + A_KV + i * 8192 + w * 1024);
    const int fr = b * 4 + (w >> 1);
    unsigned wo0, wo1, wo2, wo3; float ga, gb;
    {
        const int kk = fr >> 4, ntile = fr & 15;
        const unsigned short* wsrc = ((kk < 32) ? woA : woB) + (size_t)((kk & 31) * 16 + 8 * (w & 1) + 4 * h) * 512 + ntile * 32 + r;
        const float* gsrc = (const float*)(ws + WS_G) + tile * 32 + r;
        asm volatile("global_load_ushort %0, %6, off\n\tglobal_load_ushort %1, %6, off offset:1024\n\t"
                     "global_load_ushort %2, %6, off offset:2048\n\tglobal_load_ushort %3, %6, off offset:3072\n\t"
                     "global_load_dword %4, %7, off\n\tglobal_load_dword %5, %7, off offset:128"
                     : "=&v"(wo0), "=&v"(wo1), "=&v"(wo2), "=&v"(wo3), "=&v"(ga), "=&v"(gb) : "v"(wsrc), "v"(gsrc) : "memory");
    }
    {
        const unsigned one = ((tid & 15) == 0 || (tid & 15) == 4) ? 0x3f803f80u : 0u;
        u32x4 c = {one, one, one, one};
        *(u32x4*)(smem + A_CONST + tid * 16) = c;
    }
    const unsigned kaddr = lds0 + A_KV + lane * 16;
    const unsigned vaddr = (r < 16) ? (lds0 + A_KV + 1024 + h * 256 + r * 16) : (lds0 + A_CONST + (r - 16) * 16);
    const unsigned vstep = (r < 16) ? 8192u : 0u;

    asm volatile("s_waitcnt vmcnt(6) lgkmcnt(0)\n\ts_barrier" : "+v"(qa), "+v"(qb) :: "memory");
    ST(1, 1);
    if (w >= 4) {
#pragma unroll
        for (int i = 0; i < 24; ++i) glds16(kvsrc + 28672 + i * 4096, lds0 + A_KV + 28672 + i * 4096 + w * 1024);
    }

    f32x16 zero;
#pragma unroll
    for (int i = 0; i < 16; ++i) zero[i] = 0.f;
    f32x16 na, nb, oa = zero, ob = zero;
    {
        const bf16x8 k0 = lds_b128(kaddr);
        const f32x16 sa = MFMA32(k0, qa, zero);
        const f32x16 sb = MFMA32(k0, qb, zero);
        const float ma = -half_max(max16(sa)) - 40.f, mb = -half_max(max16(sb)) - 40.f;
#pragma unroll
        for (int i = 0; i < 16; ++i) { na[i] = ma; nb[i] = mb; }
    }
    ST(1, 2);
    if (w >= 4) attn_main<true>(kaddr, vaddr, vstep, qa, qb, na, nb, oa, ob, 2);
    else        attn_main<false>(kaddr, vaddr, vstep, qa, qb, na, nb, oa, ob, 3);
    ST(1, 3);
    asm volatile("" : "+v"(wo0), "+v"(wo1), "+v"(wo2), "+v"(wo3), "+v"(ga), "+v"(gb));
    {
        uint2 pk;
        pk.x = wo0 | (wo1 << 16); pk.y = wo2 | (wo3 << 16);
        *(uint2*)(ws + WS_WO + (size_t)fr * 1024 + lane * 16 + (w & 1) * 8) = pk;
    }

    {
        bool bad = !(oa[8] < 1.329228e36f) || !(ob[8] < 1.329228e36f);
#pragma unroll
        for (int i = 0; i < 8; ++i) bad = bad || !(fabsf(oa[i]) <= 3.0e38f) || !(fabsf(ob[i]) <= 3.0e38f);
        if (__any(bad)) {
            float ma = -INFINITY, mb = -INFINITY;
#pragma unroll 1
            for (int t = 0; t < 64; ++t) {
                const bf16x8 kf = lds_b128(kaddr + t * 2048);
                ma = fmaxf(ma, max16(MFMA32(kf, qa, zero)));
                mb = fmaxf(mb, max16(MFMA32(kf, qb, zero)));
            }
            ma = -half_max(ma); mb = -half_max(mb);
#pragma unroll
            for (int i = 0; i < 16; ++i) { na[i] = ma; nb[i] = mb; }
            oa = zero; ob = zero;
            attn_main<false>(kaddr, vaddr, vstep, qa, qb, na, nb, oa, ob, -1);
        }
    }
    const float sa = (isB ? 1.f - ga : ga) / oa[8], sb = (isB ? 1.f - gb : gb) / ob[8];
    char* odst = ws + WS_O + (size_t)tile * 65536 + hh * 1024 + lane * 16;
    *(u32x4*)(odst) = pack8(oa, 0, sa);
    *(u32x4*)(odst + 65536) = pack8(ob, 0, sb);
    ST(1, 4);
}

constexpr int O_LDS = 4 * 3 * 4096;

__global__ __launch_bounds__(256, 1) void out_kernel(const char* __restrict__ ws, unsigned short* __restrict__ out) {
    extern __shared__ __attribute__((aligned(16))) char smem[];
    const int tid = threadIdx.x, lane = tid & 63;
    const int w = __builtin_amdgcn_readfirstlane(tid >> 6);
    const int r = lane & 31, h = lane >> 5;
    const int b = blockIdx.x, xcd = b & 7, slot = b >> 3;
    const int mt = xcd * 4 + (slot & 3), nt = slot >> 2;

    ST(2, 0);
    const char* osrc = ws + WS_O + (size_t)(mt * 2) * 65536 + (w * 16) * 1024 + lane * 16;
    const char* wsrc = ws + WS_WO + (size_t)(w * 16) * 16384 + (nt * 2) * 1024 + lane * 16;
#define O_ROT 0
    const int rot = O_ROT == 1 ? ((2 * nt + 4 * (slot & 3) + ((slot >> 1) & 1)) & 15) : (O_ROT == 2 ? 2 * xcd : 0);
    bf16x8 of[16][2], wf[16][2];
#pragma unroll
    for (int k = 0; k < 16; ++k) {
        const int kk = (k + rot) & 15;
        of[k][0] = *(const bf16x8*)(osrc + kk * 1024);
        of[k][1] = *(const bf16x8*)(osrc + 65536 + kk * 1024);
        wf[k][0] = *(const bf16x8*)(wsrc + kk * 16384);
        wf[k][1] = *(const bf16x8*)(wsrc + kk * 16384 + 1024);
    }
    SBAR();
    f32x16 acc[4];
#pragma unroll
    for (int i = 0; i < 16; ++i) { acc[0][i] = 0.f; acc[1][i] = 0.f; acc[2][i] = 0.f; acc[3][i] = 0.f; }
#pragma unroll
    for (int k = 0; k < 16; ++k)
#pragma unroll
        for (int q = 0; q < 4; ++q) acc[q] = MFMA32(wf[k][q >> 1], of[k][q & 1], acc[q]);
    ST(2, 1);
    f32x16 fin;
#pragma unroll
    for (int q = 0; q < 4; ++q) {
        if (q == w) { fin = acc[q]; }
        else {
            char* dst = smem + (q * 3 + ((w - q - 1) & 3)) * 4096 + lane * 16;
#pragma unroll
            for (int g = 0; g < 4; ++g) {
                f32x4 v = {acc[q][4 * g], acc[q][4 * g + 1], acc[q][4 * g + 2], acc[q][4 * g + 3]};
                *(f32x4*)(dst + g * 1024) = v;
            }
        }
    }
    __syncthreads();
    ST(2, 2);
#pragma unroll
    for (int p = 0; p < 3; ++p)
#pragma unroll
        for (int g = 0; g < 4; ++g) {
            const f32x4 v = *(const f32x4*)(smem + (w * 3 + p) * 4096 + g * 1024 + lane * 16);
#pragma unroll
            for (int i = 0; i < 4; ++i) fin[4 * g + i] += v[i];
        }
    unsigned short* drow = out + (size_t)(mt * 64 + (w & 1) * 32 + r) * 512 + nt * 64 + (w >> 1) * 32 + h * 8;
#pragma unroll
    for (int g = 0; g < 4; g += 2) {
        unsigned a0 = pk_bf16(fin[4 * g], fin[4 * g + 1]), a1 = pk_bf16(fin[4 * g + 2], fin[4 * g + 3]);
        unsigned b0 = pk_bf16(fin[4 * g + 4], fin[4 * g + 5]), b1 = pk_bf16(fin[4 * g + 6], fin[4 * g + 7]);
        auto r0 = __builtin_amdgcn_permlane32_swap(a0, b0, false, false);
        auto r1 = __builtin_amdgcn_permlane32_swap(a1, b1, false, false);
        u32x4 res = {r0[0], r1[0], r0[1], r1[1]};
        *(u32x4*)(drow + g * 8) = res;
    }
    ST(2, 3);
    asm volatile("pf_text_end:");
}

extern "C" void kernel_launch(void* const* d_in, const int* in_sizes, int n_in, void* d_out, int out_size, void* d_ws,
                              size_t ws_size, hipStream_t stream) {
    const unsigned short* x    = (const unsigned short*)d_in[0];
    const unsigned short* wq   = (const unsigned short*)d_in[1];
    const unsigned short* wk   = (const unsigned short*)d_in[2];
    const unsigned short* wv   = (const unsigned short*)d_in[3];
    const unsigned short* woA  = (const unsigned short*)d_in[4];
    const unsigned short* wqkv = (const unsigned short*)d_in[6];
    const unsigned short* woB  = (const unsigned short*)d_in[7];
    const unsigned short* gw   = (const unsigned short*)d_in[8];
    char* ws = (char*)d_ws;
    (void)hipFuncSetAttribute((const void*)proj_kernel, hipFuncAttributeMaxDynamicSharedMemorySize, P_LDS);
    (void)hipFuncSetAttribute((const void*)attn_kernel, hipFuncAttributeMaxDynamicSharedMemorySize, A_LDS);
    (void)hipFuncSetAttribute((const void*)out_kernel, hipFuncAttributeMaxDynamicSharedMemorySize, O_LDS);
    proj_kernel<<<256, 512, P_LDS, stream>>>(x, wq, wk, wv, wqkv, gw, ws);
    attn_kernel<<<256, 512, A_LDS, stream>>>(woA, woB, ws);
    out_kernel<<<256, 256, O_LDS, stream>>>(ws, (unsigned short*)d_out);
}
```

```cpp
#include <hip/hip_runtime.h>
#include <hip/hip_bf16.h>
#include <stdint.h>

typedef short    bf16x8 __attribute__((ext_vector_type(8)));
typedef short    s16x4  __attribute__((ext_vector_type(4)));
typedef float    f32x16 __attribute__((ext_vector_type(16)));
typedef float    f32x4  __attribute__((ext_vector_type(4)));
typedef float    f32x2  __attribute__((ext_vector_type(2)));
typedef unsigned u32x4  __attribute__((ext_vector_type(4)));
typedef __bf16   bf16x2v __attribute__((ext_vector_type(2)));

#define DI __device__ __forceinline__
#define MFMA32(a, b, c) __builtin_amdgcn_mfma_f32_32x32x16_bf16((a), (b), (c), 0, 0, 0)
#define LDS_AS __attribute__((address_space(3)))

constexpr size_t WS_Q  = 0;
constexpr size_t WS_KV = WS_Q + (size_t)64 * 65536;
constexpr size_t WS_O  = WS_KV + (size_t)34 * 131072;
constexpr size_t WS_WO = WS_O + (size_t)64 * 65536;
constexpr size_t WS_G  = WS_WO + (size_t)1024 * 1024;

#define ST(k, i)
#define CODEPF 1
constexpr float QSCALE = 0.25f * 1.4426950408889634f;

DI unsigned pk_bf16(float lo, float hi) {
    f32x2 v = {lo, hi};
    bf16x2v r = __builtin_convertvector(v, bf16x2v);
    return __builtin_bit_cast(unsigned, r);
}
DI float bf2f(unsigned short u) { return __uint_as_float(((unsigned)u) << 16); }

DI void glds16(const void* g, unsigned lds_base) {
    unsigned sv;
    asm volatile("s_mov_b32 %0, m0\n\ts_mov_b32 m0, %2\n\ts_nop 0\n\tglobal_load_lds_dwordx4 %1, off\n\ts_mov_b32 m0, %0"
                 : "=&s"(sv) : "v"(g), "s"(lds_base) : "memory");
}
#define WAIT_BAR(N) asm volatile("s_waitcnt vmcnt(" #N ") lgkmcnt(0)\n\ts_barrier" ::: "memory")

#define STPOL "sc0 sc1"
DI void gst128(void* p, u32x4 v) { asm volatile("global_store_dwordx4 %0, %1, off " STPOL "\n\ts_nop 1" :: "v"(p), "v"(v) : "memory"); }
DI bf16x8 lds_b128(unsigned addr) { return *(const LDS_AS bf16x8*)(uintptr_t)addr; }
DI s16x4 lds_tr(unsigned addr) {
    return __builtin_bit_cast(s16x4, __builtin_amdgcn_ds_read_tr16_b64_v4i16((LDS_AS s16x4*)(uintptr_t)addr));
}
DI bf16x8 cat8(s16x4 a, s16x4 b) { return __builtin_shufflevector(a, b, 0, 1, 2, 3, 4, 5, 6, 7); }

DI u32x4 pack8(const f32x16& a, int base, float sc) {
    u32x4 r;
    r[0] = pk_bf16(a[base + 0] * sc, a[base + 1] * sc);
    r[1] = pk_bf16(a[base + 2] * sc, a[base + 3] * sc);
    r[2] = pk_bf16(a[base + 4] * sc, a[base + 5] * sc);
    r[3] = pk_bf16(a[base + 6] * sc, a[base + 7] * sc);
    return r;
}

constexpr int P_XS = 0, P_WS = 16384, P_STAGE = 32768, P_WX = 4 * P_STAGE, P_GT = P_WX + 16384, P_CNT = P_GT + 12288, P_LDS = P_CNT + 16;
#define SBAR() __builtin_amdgcn_sched_barrier(0)

struct PSrc { const unsigned short *xs0, *xs1, *wsr; int ldw; };
DI PSrc proj_src(const unsigned short* x, const unsigned short* wsrc, int ldw, int mt, int nt, int l, int lane) {
    PSrc s;
    const int row = 32 * l + (lane >> 3);
    s.xs0 = x + (size_t)(mt * 128 + row) * 512 + ((lane & 7) ^ ((row >> 1) & 7)) * 8;
    s.xs1 = x + (size_t)(mt * 128 + row + 8) * 512 + ((lane & 7) ^ (((row + 8) >> 1) & 7)) * 8;
    const int krow = 16 * l + (lane >> 4);
    s.wsr = wsrc + (size_t)krow * ldw + ((lane & 15) ^ (((lane >> 4) & 3) << 2)) * 8;
    s.ldw = ldw;
    return s;
}
DI void proj_issue(const PSrc& s, int c, int l, unsigned lds0) {
    const unsigned st = lds0 + (c & 3) * P_STAGE + l * 4096;
    glds16(s.xs0 + c * 64, st + P_XS);
    glds16(s.xs1 + c * 64, st + P_XS + 1024);
    glds16(s.xs0 + c * 64 + 16 * 512, st + P_XS + 2048);
    glds16(s.xs1 + c * 64 + 16 * 512, st + P_XS + 3072);
#pragma unroll
    for (int i = 0; i < 4; ++i) glds16(s.wsr + (size_t)(c * 64 + 4 * i) * s.ldw, st + P_WS + i * 1024);
}
DI float wave_sum63(float v) {
#define DPP_ADD(ctrl, rm) v += __int_as_float(__builtin_amdgcn_update_dpp(0, __float_as_int(v), ctrl, rm, 0xf, false))
    DPP_ADD(0x111, 0xf); DPP_ADD(0x112, 0xf); DPP_ADD(0x114, 0xf); DPP_ADD(0x118, 0xf);
    DPP_ADD(0x142, 0xa); DPP_ADD(0x143, 0xc);
#undef DPP_ADD
    return v;
}
DI void lds_spin(const char* smem, int off, unsigned target) {
    while (__builtin_amdgcn_readfirstlane(*(const volatile unsigned*)(smem + off)) < target) __builtin_amdgcn_s_sleep(1);
    asm volatile("" ::: "memory");
}

template <bool ISV>
DI void proj_compute(const PSrc& src, const unsigned short* __restrict__ x, const unsigned short* __restrict__ wx,
                     const unsigned short* __restrict__ gw, char* __restrict__ ws, int mt, int nt, int w, unsigned lds0, char* smem) {
    const int lane = threadIdx.x & 63;
    const int wm = w >> 1, wn = w & 1;
    const int r = lane & 31, h = lane >> 5;
    proj_issue(src, 0, w, lds0);
    {
        const unsigned short* es = wx + (size_t)((lane >> 5) * 64 + 16 * w) * 16 + (lane & 31) * 8;
#pragma unroll
        for (int i = 0; i < 4; ++i) glds16(es + i * 2048, lds0 + P_WX + w * 4096 + i * 1024);
        const unsigned short* px = x + (size_t)(mt * 128 + nt * 8 + 2 * w) * 512 + lane * 8;
        glds16(px, lds0 + P_GT + w * 3072);
        glds16(px + 512, lds0 + P_GT + w * 3072 + 1024);
        glds16(gw + lane * 8, lds0 + P_GT + w * 3072 + 2048);
    }
    unsigned xo[4][2];
#pragma unroll
    for (int tt = 0; tt < 2; ++tt) {
        const int row = wm * 64 + tt * 32 + r;
#pragma unroll
        for (int ks = 0; ks < 4; ++ks) xo[ks][tt] = lds0 + P_XS + row * 128 + (((2 * ks + h) ^ ((row >> 1) & 7)) << 4);
    }
    const int q4 = (lane & 15) >> 2, p4 = lane & 3, g1 = (lane >> 4) & 1;
    unsigned wa[2];
#pragma unroll
    for (int ct = 0; ct < 2; ++ct)
        wa[ct] = lds0 + P_WS + (8 * h + q4) * 256 + ((4 * ((2 * wn + ct) ^ q4) + 2 * g1 + (p4 >> 1)) << 4) + (p4 & 1) * 8;
    f32x16 acc[2][2];
#pragma unroll
    for (int i = 0; i < 16; ++i) { acc[0][0][i] = 0.f; acc[0][1][i] = 0.f; acc[1][0][i] = 0.f; acc[1][1][i] = 0.f; }
    bf16x8 fx[2][4][2], fw[2][4][2];
    auto mm = [&](int bf, int ks) {
#pragma unroll
        for (int ct = 0; ct < 2; ++ct)
#pragma unroll
            for (int tt = 0; tt < 2; ++tt)
                acc[ct][tt] = ISV ? MFMA32(fx[bf][ks][tt], fw[bf][ks][ct], acc[ct][tt]) : MFMA32(fw[bf][ks][ct], fx[bf][ks][tt], acc[ct][tt]);
    };
    auto rd = [&](int bf, int ks, unsigned so) {
#pragma unroll
        for (int tt = 0; tt < 2; ++tt) fx[bf][ks][tt] = lds_b128(xo[ks][tt] + so);
#pragma unroll
        for (int ct = 0; ct < 2; ++ct) fw[bf][ks][ct] = cat8(lds_tr(wa[ct] + so + ks * 4096), lds_tr(wa[ct] + so + ks * 4096 + 1024));
    };
    auto step = [&](int bf, int c) {
        asm volatile("s_waitcnt lgkmcnt(0)\n\ts_barrier" ::: "memory");
        const unsigned so = (c & 3) * P_STAGE;
#pragma unroll
        for (int ks = 0; ks < 4; ++ks) { rd(bf, ks, so); mm(bf ^ 1, ks); }
#pragma unroll
        for (int i = 0; i < 12; ++i) {
            __builtin_amdgcn_sched_group_barrier(0x008, 1, 0);
            __builtin_amdgcn_sched_group_barrier(0x002, 1, 0);
            __builtin_amdgcn_sched_group_barrier(0x100, 2, 0);
        }
        __builtin_amdgcn_sched_group_barrier(0x008, 4, 0);
        SBAR();
    };
    asm volatile("" : "+v"(xo[0][0]), "+v"(xo[0][1]), "+v"(xo[1][0]), "+v"(xo[1][1]), "+v"(xo[2][0]), "+v"(xo[2][1]),
                      "+v"(xo[3][0]), "+v"(xo[3][1]), "+v"(wa[0]), "+v"(wa[1]),
                      "+v"(acc[0][0]), "+v"(acc[0][1]), "+v"(acc[1][0]), "+v"(acc[1][1]));
    asm volatile("s_waitcnt vmcnt(7)" ::: "memory");
    if (lane == 0) atomicAdd((unsigned*)(smem + P_CNT), 1u);
    lds_spin(smem, P_CNT, 4u);
    ST(0, 1);
    rd(0, 0, 0); rd(0, 1, 0); rd(0, 2, 0); rd(0, 3, 0);
    SBAR();
#pragma unroll 1
    for (int c = 1; c < 7; c += 2) { step(1, c); step(0, c + 1); }
    asm volatile("s_waitcnt vmcnt(0)" ::: "memory");
    step(1, 7);
    ST(0, 2);
    mm(1, 0); mm(1, 1); mm(1, 2); mm(1, 3);
    ST(0, 3);
#pragma unroll
    for (int ct = 0; ct < 2; ++ct)
#pragma unroll
        for (int tt = 0; tt < 2; ++tt) {
            const int hd0 = (nt & 3) * 8 + wn * 4 + ct * 2;
            const int tile = mt * 4 + wm * 2 + tt;
            if (ISV) {
                char* dst = ws + WS_KV + (size_t)(hd0 + (r >> 4)) * 131072 + tile * 2048 + 1024 + h * 256 + (r & 15) * 16;
                gst128(dst, pack8(acc[ct][tt], 0, 1.f));
                gst128(dst + 512, pack8(acc[ct][tt], 8, 1.f));
            } else {
                const bool isq = nt < 8;
                const float sc = isq ? QSCALE : 1.f;
                const size_t hs = isq ? 65536 : 131072;
                char* dst = isq ? (ws + WS_Q + (size_t)((nt >> 2) * 32 + hd0) * 65536 + tile * 1024 + lane * 16)
                                : (ws + WS_KV + (size_t)hd0 * 131072 + tile * 2048 + lane * 16);
                gst128(dst, pack8(acc[ct][tt], 0, sc));
                gst128(dst + hs, pack8(acc[ct][tt], 8, sc));
            }
        }
}

template <int XM>
DI void proj_loader(const PSrc& src, const unsigned short* __restrict__ x, const unsigned short* __restrict__ wk,
                    const unsigned short* __restrict__ wv, const unsigned short* __restrict__ gw, char* __restrict__ ws,
                    int mt, int nt, int l, unsigned lds0, char* smem) {
    const int lane = threadIdx.x & 63;
    const int r = lane & 31, h = lane >> 5;
    constexpr bool isx = XM != 0, isv = XM == 2;
    const int xtt = nt & 3;
    proj_issue(src, 1, l, lds0); proj_issue(src, 2, l, lds0);
    const int q4 = (lane & 15) >> 2, p4 = lane & 3;
    const unsigned ea = lds0 + P_WX + l * 4096 + (8 * h + q4) * 32 + p4 * 8;
    const int xrow = xtt * 32 + r;
    const unsigned exa = lds0 + P_XS + xrow * 128 + (((2 * l + h) ^ ((xrow >> 1) & 7)) << 4);
    const int gtok = mt * 128 + nt * 8 + 2 * l;
    u32x4 gx0, gx1, gv;
    bf16x8 xe[8];
    if (isx) { lds_spin(smem, P_CNT, 4u); xe[0] = lds_b128(exa); }
#pragma unroll
    for (int c = 1; c < 8; ++c) {
        if (c < 7) WAIT_BAR(8); else WAIT_BAR(0);
        if (isx) xe[c] = lds_b128(exa + (c & 3) * P_STAGE);
        if (c < 6) proj_issue(src, c + 2, l, lds0);
    }
    gx0 = *(const u32x4*)(smem + P_GT + l * 3072 + lane * 16);
    gx1 = *(const u32x4*)(smem + P_GT + l * 3072 + 1024 + lane * 16);
    gv  = *(const u32x4*)(smem + P_GT + l * 3072 + 2048 + lane * 16);
    {
        float s0 = 0.f, s1 = 0.f;
#pragma unroll
        for (int i = 0; i < 4; ++i) {
            const float wl = __uint_as_float(gv[i] << 16), wh = __uint_as_float(gv[i] & 0xffff0000u);
            s0 += __uint_as_float(gx0[i] << 16) * wl + __uint_as_float(gx0[i] & 0xffff0000u) * wh;
            s1 += __uint_as_float(gx1[i] << 16) * wl + __uint_as_float(gx1[i] & 0xffff0000u) * wh;
        }
        s0 = wave_sum63(s0); s1 = wave_sum63(s1);
        if (lane == 63) {
            f32x2 g = {__builtin_amdgcn_rcpf(1.f + __expf(-s0)), __builtin_amdgcn_rcpf(1.f + __expf(-s1))};
            *(f32x2*)((float*)(ws + WS_G) + gtok) = g;
        }
    }
    f32x16 acce;
#pragma unroll
    for (int i = 0; i < 16; ++i) acce[i] = 0.f;
    if (isx) {
        __builtin_amdgcn_s_setprio(3);
#pragma unroll
        for (int c = 0; c < 8; ++c) {
            const bf16x8 we = cat8(lds_tr(ea + c * 512), lds_tr(ea + c * 512 + 128));
            acce = isv ? MFMA32(xe[c], we, acce) : MFMA32(we, xe[c], acce);
        }
    }
    if (isx) {
        f32x4* red = (f32x4*)smem;
#pragma unroll
        for (int g = 0; g < 4; ++g) {
            f32x4 v = {acce[4 * g], acce[4 * g + 1], acce[4 * g + 2], acce[4 * g + 3]};
            red[(l * 4 + g) * 64 + lane] = v;
        }
        unsigned old = 0;
        asm volatile("s_waitcnt lgkmcnt(0)" ::: "memory");
        if (lane == 0) old = atomicAdd((unsigned*)(smem + P_CNT), 1u);
        old = __builtin_amdgcn_readfirstlane(old);
        if (old == 7) {
            asm volatile("" ::: "memory");
#pragma unroll
            for (int g = 0; g < 4; ++g) {
                const f32x4 v = red[g * 64 + lane] + red[(4 + g) * 64 + lane] + red[(8 + g) * 64 + lane] + red[(12 + g) * 64 + lane];
#pragma unroll
                for (int i = 0; i < 4; ++i) acce[4 * g + i] = v[i];
            }
            char* base = ws + WS_KV + (size_t)32 * 131072 + (mt * 4 + xtt) * 2048;
            if (isv) {
                if (r < 16) {
                    *(u32x4*)(base + 1024 + h * 256 + r * 16) = pack8(acce, 0, 1.f);
                    *(u32x4*)(base + 1536 + h * 256 + r * 16) = pack8(acce, 8, 1.f);
                }
            } else {
                *(u32x4*)(base + lane * 16) = pack8(acce, 0, 1.f);
            }
        }
    }
}

__global__ __launch_bounds__(512, 2) void proj_kernel(const unsigned short* __restrict__ x, const unsigned short* __restrict__ wq,
                                                      const unsigned short* __restrict__ wk, const unsigned short* __restrict__ wv,
                                                      const unsigned short* __restrict__ wqkv, const unsigned short* __restrict__ gw,
                                                      char* __restrict__ ws) {
    extern __shared__ __attribute__((aligned(16))) char smem[];
    const unsigned lds0 = (unsigned)(uintptr_t)smem;
    const int w = __builtin_amdgcn_readfirstlane(threadIdx.x >> 6);
    const int b = blockIdx.x, xcd = b & 7, slot = b >> 3;
    const int mt = (xcd & 3) * 4 + (slot & 3), nt = (xcd >> 2) * 8 + (slot >> 2);
    ST(0, 0);
    unsigned cpf[CODEPF];
    if (w >= 4) {
        unsigned long long pc; int len;
        asm volatile("s_getpc_b64 %0\n\tpf_text_begin:\n\ts_mov_b32 %1, pf_text_end-pf_text_begin" : "=s"(pc), "=s"(len));
#pragma unroll
        for (int i = 0; i < CODEPF; ++i) {
            int off = (i * 256 + (w - 4) * 64 + (int)(threadIdx.x & 63)) * 128;
            off = off < len ? off : 0;
            asm volatile("global_load_dword %0, %1, %2" : "=&v"(cpf[i]) : "v"(off), "s"(pc) : "memory");
        }
    }
    if (threadIdx.x == 0) *(unsigned*)(smem + P_CNT) = 0u;
    const unsigned short* wsrc = (nt < 4) ? (wq + nt * 128) : (wqkv + (nt - 4) * 128);
    const PSrc src = proj_src(x, wsrc, (nt < 4) ? 512 : 1536, mt, nt, w & 3, threadIdx.x & 63);
    asm volatile("s_waitcnt lgkmcnt(0)\n\ts_barrier" ::: "memory");
    if (w >= 4) {
        if (nt >= 12)     proj_loader<2>(src, x, wk, wv, gw, ws, mt, nt, w - 4, lds0, smem);
        else if (nt >= 8) proj_loader<1>(src, x, wk, wv, gw, ws, mt, nt, w - 4, lds0, smem);
        else              proj_loader<0>(src, x, wk, wv, gw, ws, mt, nt, w - 4, lds0, smem);
#pragma unroll
        for (int i = 0; i < CODEPF; ++i) asm volatile("" :: "v"(cpf[i]));
        ST(0, 1); ST(0, 2); ST(0, 3);
    } else if (nt >= 12) proj_compute<true>(src, x, wv, gw, ws, mt, nt, w, lds0, smem);
    else                 proj_compute<false>(src, x, wk, gw, ws, mt, nt, w, lds0, smem);
    ST(0, 4);
}

constexpr int A_KV = 0, A_CONST = 131072, A_LDS = A_CONST + 8192;

DI float max16(const f32x16& s) {
    float a = fmaxf(fmaxf(s[0], s[1]), s[2]), b = fmaxf(fmaxf(s[3], s[4]), s[5]);
    a = fmaxf(fmaxf(a, s[6]), s[7]);   b = fmaxf(fmaxf(b, s[8]), s[9]);
    a = fmaxf(fmaxf(a, s[10]), s[11]); b = fmaxf(fmaxf(b, s[12]), s[13]);
    a = fmaxf(fmaxf(a, s[14]), s[15]);
    return fmaxf(a, b);
}
DI float half_max(float m) {
    auto rr = __builtin_amdgcn_permlane32_swap(__float_as_uint(m), __float_as_uint(m), false, false);
    return fmaxf(__uint_as_float(rr[0]), __uint_as_float(rr[1]));
}

#define R_SA "192"
#define R_SB "208"
#define EX(X, i) "v_exp_f32 v[" X "+" #i "], v[" X "+" #i "]\n\t"
#define CV(X, j, a, b) "v_cvt_pk_bf16_f32 v[224+" #j "], v[" X "+" #a "], v[" X "+" #b "]\n\t"
#define HALF(X, Y, OX, OY, NY, QY, WA, RA, WB, RB, WC, RC)                                        \
    EX(X, 0) EX(X, 1) WA                                                                          \
    "v_mfma_f32_32x32x16_bf16 " OY ", %[v1], v[228:231], " OY "\n\t" RA                           \
    EX(X, 2) EX(X, 3) CV(X, 0, 0, 1) EX(X, 4) EX(X, 5) CV(X, 1, 2, 3) WB                          \
    "v_mfma_f32_32x32x16_bf16 v[" Y ":" Y "+15], %[kf], " QY ", " NY "\n\t" RB                    \
    EX(X, 6) EX(X, 7) CV(X, 2, 4, 5) EX(X, 8) EX(X, 9) CV(X, 3, 6, 7) EX(X, 10) EX(X, 11) WC      \
    "v_mfma_f32_32x32x16_bf16 " OX ", %[v0], v[224:227], " OX "\n\t" RC                           \
    CV(X, 4, 8, 9) EX(X, 12) EX(X, 13) CV(X, 5, 10, 11) EX(X, 14) EX(X, 15) CV(X, 6, 12, 13) CV(X, 7, 14, 15)
#define TILE(OFF_V1, OFF_KN, OFF_V0N, P0, P1)                                                     \
    HALF(R_SA, R_SB, "%[oa]", "%[ob]", "%[nb]", "%[qb]",                                          \
         "", "ds_read_b128 %[v1], %[va] offset:" OFF_V1 "\n\t" P0,                                \
         "", "ds_read_b128 %[kf], %[ka] offset:" OFF_KN "\n\t",                                   \
         "s_waitcnt lgkmcnt(2)\n\t", "")                                                          \
    HALF(R_SB, R_SA, "%[ob]", "%[oa]", "%[na]", "%[qa]",                                          \
         "s_waitcnt lgkmcnt(1)\n\t", P1,                                                          \
         "s_waitcnt lgkmcnt(0)\n\t", "",                                                          \
         "", "ds_read_b128 %[v0], %[va] offset:" OFF_V0N "\n\t")
#define ATTN_ASM(PY, PO)                                                                          \
    asm volatile(                                                                                 \
        "ds_read_b128 %[kf], %[ka]\n\t"                                                           \
        "ds_read_b128 %[v1], %[va]\n\t"                                                           \
        "ds_read_b128 %[v0], %[va]\n\t"                                                           \
        "v_mov_b32 v228, 0\n\tv_mov_b32 v229, 0\n\tv_mov_b32 v230, 0\n\tv_mov_b32 v231, 0\n\t"    \
        "s_waitcnt lgkmcnt(0)\n\t"                                                                \
        "v_mfma_f32_32x32x16_bf16 v[192:207], %[kf], %[qa], %[na]\n\t"                            \
        "s_nop 7\n\ts_nop 7\n\t"                                                                  \
        "L_loop_%=:\n\t"                                                                          \
        "s_cmp_lg_u32 %[cnt], %[s1]\n\t"                                                          \
        "s_cbranch_scc1 L_n1_%=\n\t"                                                              \
        "s_waitcnt vmcnt(0)\n\t"                                                                  \
        "s_barrier\n\t"                                                                           \
        "L_n1_%=:\n\t"                                                                            \
        TILE("512", "2048", "2048", "s_setprio 0\n\t", PY)                                        \
        TILE("2560", "4096", "4096", PO, "")                                                      \
        TILE("4608", "6144", "6144", "", "")                                                      \
        HALF(R_SA, R_SB, "%[oa]", "%[ob]", "%[nb]", "%[qb]",                                      \
             "", "ds_read_b128 %[v1], %[va] offset:6656\n\t",                                     \
             "", "ds_read_b128 %[kf], %[ka] offset:8192\n\tv_add_u32 %[va], %[va], %[vs]\n\t",    \
             "s_waitcnt lgkmcnt(2)\n\t", "v_add_u32 %[ka], 0x2000, %[ka]\n\t")                    \
        HALF(R_SB, R_SA, "%[ob]", "%[oa]", "%[na]", "%[qa]",                                      \
             "s_waitcnt lgkmcnt(1)\n\t", "s_add_u32 %[cnt], %[cnt], 1\n\t",                       \
             "s_waitcnt lgkmcnt(0)\n\t", "s_cmp_lg_u32 %[cnt], 16\n\t",                           \
             "", "ds_read_b128 %[v0], %[va]\n\t")                                                 \
        "s_cbranch_scc1 L_loop_%=\n\t"                                                            \
        "s_nop 1\n\t"                                                                             \
        "v_mfma_f32_32x32x16_bf16 %[ob], %[v1], v[228:231], %[ob]\n\t"                            \
        "s_waitcnt lgkmcnt(0)\n\t"                                                                \
        "s_nop 7\n\ts_nop 7\n\t"                                                                  \
        : [oa] "+v"(oa), [ob] "+v"(ob), [ka] "+v"(ka), [va] "+v"(va), [cnt] "+s"(cnt),            \
          [kf] "=&v"(kf), [v0] "=&v"(v0), [v1] "=&v"(v1)                                          \
        : [qa] "v"(qa), [qb] "v"(qb), [na] "v"(na), [nb] "v"(nb), [vs] "v"(vstep), [s1] "s"(sync1) \
        : "memory", "scc",                                                                        \
          "v192", "v193", "v194", "v195", "v196", "v197", "v198", "v199", "v200", "v201", "v202", "v203", "v204", "v205", "v206", "v207", \
          "v208", "v209", "v210", "v211", "v212", "v213", "v214", "v215", "v216", "v217", "v218", "v219", "v220", "v221", "v222", "v223", \
          "v224", "v225", "v226", "v227", "v228", "v229", "v230", "v231")

template <bool YOUNG>
DI void attn_main(unsigned ka, unsigned va, unsigned vstep, const bf16x8& qa, const bf16x8& qb, const f32x16& na, const f32x16& nb,
                  f32x16& oa, f32x16& ob, int sync1) {
    bf16x8 kf, v0, v1;
    int cnt = 0;
    if (YOUNG) ATTN_ASM("s_setprio 1\n\t", "");
    else       ATTN_ASM("", "s_setprio 1\n\t");
}

__global__ __launch_bounds__(512, 2) void attn_kernel(const unsigned short* __restrict__ woA, const unsigned short* __restrict__ woB,
                                                      char* __restrict__ ws) {
    extern __shared__ __attribute__((aligned(16))) char smem[];
    const unsigned lds0 = (unsigned)(uintptr_t)smem;
    const int tid = threadIdx.x, lane = tid & 63;
    const int w = __builtin_amdgcn_readfirstlane(tid >> 6);
    const int r = lane & 31, h = lane >> 5;
    const int b = blockIdx.x, xcd = b & 7, slot = b >> 3;
    const int isB = (slot < 16);
    const int hd = xcd * 4 + ((slot & 15) >> 2);
    const int hh = isB ? 32 + hd : hd;
    const int kvh = isB ? hd : 32;
    const int tile = (slot & 3) * 16 + 2 * w;

    ST(1, 0);
    bf16x8 qa, qb;
    {
        const char* qsrc = ws + WS_Q + (size_t)hh * 65536 + tile * 1024 + lane * 16;
        asm volatile("global_load_dwordx4 %0, %2, off\n\tglobal_load_dwordx4 %1, %2, off offset:1024"
                     : "=&v"(qa), "=&v"(qb) : "v"(qsrc) : "memory");
    }
    const char* kvsrc = ws + WS_KV + (size_t)kvh * 131072 + w * 1024 + lane * 16;
#pragma unroll
    for (int i = 0; i < 4; ++i) glds16(kvsrc + i * 8192, lds0 + A_KV + i * 8192 + w * 1024);
    const int fr = b * 4 + (w >> 1);
    unsigned wo0, wo1, wo2, wo3; float ga, gb;
    {
        const int kk = fr >> 4, ntile = fr & 15;
        const unsigned short* wsrc = ((kk < 32) ? woA : woB) + (size_t)((kk & 31) * 16 + 8 * (w & 1) + 4 * h) * 512 + ntile * 32 + r;
        const float* gsrc = (const float*)(ws + WS_G) + tile * 32 + r;
        asm volatile("global_load_ushort %0, %6, off\n\tglobal_load_ushort %1, %6, off offset:1024\n\t"
                     "global_load_ushort %2, %6, off offset:2048\n\tglobal_load_ushort %3, %6, off offset:3072\n\t"
                     "global_load_dword %4, %7, off\n\tglobal_load_dword %5, %7, off offset:128"
                     : "=&v"(wo0), "=&v"(wo1), "=&v"(wo2), "=&v"(wo3), "=&v"(ga), "=&v"(gb) : "v"(wsrc), "v"(gsrc) : "memory");
    }
    {
        const unsigned one = ((tid & 15) == 0 || (tid & 15) == 4) ? 0x3f803f80u : 0u;
        u32x4 c = {one, one, one, one};
        *(u32x4*)(smem + A_CONST + tid * 16) = c;
    }
    const unsigned kaddr = lds0 + A_KV + lane * 16;
    const unsigned vaddr = (r < 16) ? (lds0 + A_KV + 1024 + h * 256 + r * 16) : (lds0 + A_CONST + (r - 16) * 16);
    const unsigned vstep = (r < 16) ? 8192u : 0u;

    asm volatile("s_waitcnt vmcnt(6) lgkmcnt(0)\n\ts_barrier" : "+v"(qa), "+v"(qb) :: "memory");
    ST(1, 1);
    if (w >= 4) {
#pragma unroll
        for (int i = 0; i < 24; ++i) glds16(kvsrc + 28672 + i * 4096, lds0 + A_KV + 28672 + i * 4096 + w * 1024);
    }

    f32x16 zero;
#pragma unroll
    for (int i = 0; i < 16; ++i) zero[i] = 0.f;
    f32x16 na, nb, oa = zero, ob = zero;
    {
        const bf16x8 k0 = lds_b128(kaddr);
        const f32x16 sa = MFMA32(k0, qa, zero);
        const f32x16 sb = MFMA32(k0, qb, zero);
        const float ma = -half_max(max16(sa)) - 40.f, mb = -half_max(max16(sb)) - 40.f;
#pragma unroll
        for (int i = 0; i < 16; ++i) { na[i] = ma; nb[i] = mb; }
    }
    ST(1, 2);
    if (w >= 4) attn_main<true>(kaddr, vaddr, vstep, qa, qb, na, nb, oa, ob, 2);
    else        attn_main<false>(kaddr, vaddr, vstep, qa, qb, na, nb, oa, ob, 3);
    ST(1, 3);
    asm volatile("" : "+v"(wo0), "+v"(wo1), "+v"(wo2), "+v"(wo3), "+v"(ga), "+v"(gb));
    {
        uint2 pk;
        pk.x = wo0 | (wo1 << 16); pk.y = wo2 | (wo3 << 16);
        *(uint2*)(ws + WS_WO + (size_t)fr * 1024 + lane * 16 + (w & 1) * 8) = pk;
    }

    {
        bool bad = !(oa[8] < 1.329228e36f) || !(ob[8] < 1.329228e36f);
#pragma unroll
        for (int i = 0; i < 8; ++i) bad = bad || !(fabsf(oa[i]) <= 3.0e38f) || !(fabsf(ob[i]) <= 3.0e38f);
        if (__any(bad)) {
            float ma = -INFINITY, mb = -INFINITY;
#pragma unroll 1
            for (int t = 0; t < 64; ++t) {
                const bf16x8 kf = lds_b128(kaddr + t * 2048);
                ma = fmaxf(ma, max16(MFMA32(kf, qa, zero)));
                mb = fmaxf(mb, max16(MFMA32(kf, qb, zero)));
            }
            ma = -half_max(ma); mb = -half_max(mb);
#pragma unroll
            for (int i = 0; i < 16; ++i) { na[i] = ma; nb[i] = mb; }
            oa = zero; ob = zero;
            attn_main<false>(kaddr, vaddr, vstep, qa, qb, na, nb, oa, ob, -1);
        }
    }
    const float sa = (isB ? 1.f - ga : ga) / oa[8], sb = (isB ? 1.f - gb : gb) / ob[8];
    char* odst = ws + WS_O + (size_t)tile * 65536 + hh * 1024 + lane * 16;
    gst128(odst, pack8(oa, 0, sa));
    gst128(odst + 65536, pack8(ob, 0, sb));
    ST(1, 4);
}

constexpr int O_LDS = 4 * 3 * 4096;

__global__ __launch_bounds__(256, 1) void out_kernel(const char* __restrict__ ws, unsigned short* __restrict__ out) {
    extern __shared__ __attribute__((aligned(16))) char smem[];
    const int tid = threadIdx.x, lane = tid & 63;
    const int w = __builtin_amdgcn_readfirstlane(tid >> 6);
    const int r = lane & 31, h = lane >> 5;
    const int b = blockIdx.x, xcd = b & 7, slot = b >> 3;
    const int mt = xcd * 4 + (slot & 3), nt = slot >> 2;

    ST(2, 0);
    const char* osrc = ws + WS_O + (size_t)(mt * 2) * 65536 + (w * 16) * 1024 + lane * 16;
    const char* wsrc = ws + WS_WO + (size_t)(w * 16) * 16384 + (nt * 2) * 1024 + lane * 16;
#define O_ROT 0
    const int rot = O_ROT == 1 ? ((2 * nt + 4 * (slot & 3) + ((slot >> 1) & 1)) & 15) : (O_ROT == 2 ? 2 * xcd : 0);
    bf16x8 of[16][2], wf[16][2];
#pragma unroll
    for (int k = 0; k < 16; ++k) {
        const int kk = (k + rot) & 15;
        of[k][0] = *(const bf16x8*)(osrc + kk * 1024);
        of[k][1] = *(const bf16x8*)(osrc + 65536 + kk * 1024);
        wf[k][0] = *(const bf16x8*)(wsrc + kk * 16384);
        wf[k][1] = *(const bf16x8*)(wsrc + kk * 16384 + 1024);
    }
    SBAR();
    f32x16 acc[4];
#pragma unroll
    for (int i = 0; i < 16; ++i) { acc[0][i] = 0.f; acc[1][i] = 0.f; acc[2][i] = 0.f; acc[3][i] = 0.f; }
#pragma unroll
    for (int k = 0; k < 16; ++k)
#pragma unroll
        for (int q = 0; q < 4; ++q) acc[q] = MFMA32(wf[k][q >> 1], of[k][q & 1], acc[q]);
    ST(2, 1);
    f32x16 fin;
#pragma unroll
    for (int q = 0; q < 4; ++q) {
        if (q == w) { fin = acc[q]; }
        else {
            char* dst = smem + (q * 3 + ((w - q - 1) & 3)) * 4096 + lane * 16;
#pragma unroll
            for (int g = 0; g < 4; ++g) {
                f32x4 v = {acc[q][4 * g], acc[q][4 * g + 1], acc[q][4 * g + 2], acc[q][4 * g + 3]};
                *(f32x4*)(dst + g * 1024) = v;
            }
        }
    }
    __syncthreads();
    ST(2, 2);
#pragma unroll
    for (int p = 0; p < 3; ++p)
#pragma unroll
        for (int g = 0; g < 4; ++g) {
            const f32x4 v = *(const f32x4*)(smem + (w * 3 + p) * 4096 + g * 1024 + lane * 16);
#pragma unroll
            for (int i = 0; i < 4; ++i) fin[4 * g + i] += v[i];
        }
    unsigned short* drow = out + (size_t)(mt * 64 + (w & 1) * 32 + r) * 512 + nt * 64 + (w >> 1) * 32 + h * 8;
#pragma unroll
    for (int g = 0; g < 4; g += 2) {
        unsigned a0 = pk_bf16(fin[4 * g], fin[4 * g + 1]), a1 = pk_bf16(fin[4 * g + 2], fin[4 * g + 3]);
        unsigned b0 = pk_bf16(fin[4 * g + 4], fin[4 * g + 5]), b1 = pk_bf16(fin[4 * g + 6], fin[4 * g + 7]);
        auto r0 = __builtin_amdgcn_permlane32_swap(a0, b0, false, false);
        auto r1 = __builtin_amdgcn_permlane32_swap(a1, b1, false, false);
        u32x4 res = {r0[0], r1[0], r0[1], r1[1]};
        *(u32x4*)(drow + g * 8) = res;
    }
    ST(2, 3);
    asm volatile("pf_text_end:");
}

extern "C" void kernel_launch(void* const* d_in, const int* in_sizes, int n_in, void* d_out, int out_size, void* d_ws,
                              size_t ws_size, hipStream_t stream) {
    const unsigned short* x    = (const unsigned short*)d_in[0];
    const unsigned short* wq   = (const unsigned short*)d_in[1];
    const unsigned short* wk   = (const unsigned short*)d_in[2];
    const unsigned short* wv   = (const unsigned short*)d_in[3];
    const unsigned short* woA  = (const unsigned short*)d_in[4];
    const unsigned short* wqkv = (const unsigned short*)d_in[6];
    const unsigned short* woB  = (const unsigned short*)d_in[7];
    const unsigned short* gw   = (const unsigned short*)d_in[8];
    char* ws = (char*)d_ws;
    (void)hipFuncSetAttribute((const void*)proj_kernel, hipFuncAttributeMaxDynamicSharedMemorySize, P_LDS);
    (void)hipFuncSetAttribute((const void*)attn_kernel, hipFuncAttributeMaxDynamicSharedMemorySize, A_LDS);
    (void)hipFuncSetAttribute((const void*)out_kernel, hipFuncAttributeMaxDynamicSharedMemorySize, O_LDS);
    proj_kernel<<<256, 512, P_LDS, stream>>>(x, wq, wk, wv, wqkv, gw, ws);
    attn_kernel<<<256, 512, A_LDS, stream>>>(woA, woB, ws);
    out_kernel<<<256, 256, O_LDS, stream>>>(ws, (unsigned short*)d_out);
}
```

```cpp
#include <hip/hip_runtime.h>
#include <hip/hip_bf16.h>
#include <stdint.h>

typedef short    bf16x8 __attribute__((ext_vector_type(8)));
typedef short    s16x4  __attribute__((ext_vector_type(4)));
typedef float    f32x16 __attribute__((ext_vector_type(16)));
typedef float    f32x4  __attribute__((ext_vector_type(4)));
typedef float    f32x2  __attribute__((ext_vector_type(2)));
typedef unsigned u32x4  __attribute__((ext_vector_type(4)));
typedef __bf16   bf16x2v __attribute__((ext_vector_type(2)));

#define DI __device__ __forceinline__
#define MFMA32(a, b, c) __builtin_amdgcn_mfma_f32_32x32x16_bf16((a), (b), (c), 0, 0, 0)
#define LDS_AS __attribute__((address_space(3)))

constexpr size_t WS_Q  = 0;
constexpr size_t WS_KV = WS_Q + (size_t)64 * 65536;
constexpr size_t WS_O  = WS_KV + (size_t)34 * 131072;
constexpr size_t WS_WO = WS_O + (size_t)64 * 65536;
constexpr size_t WS_G  = WS_WO + (size_t)1024 * 1024;

#define ST(k, i)
#define CODEPF 1
#define REFOFF 100.f
constexpr float QSCALE = 0.25f * 1.4426950408889634f;

DI unsigned pk_bf16(float lo, float hi) {
    f32x2 v = {lo, hi};
    bf16x2v r = __builtin_convertvector(v, bf16x2v);
    return __builtin_bit_cast(unsigned, r);
}
DI float bf2f(unsigned short u) { return __uint_as_float(((unsigned)u) << 16); }

DI void glds16(const void* g, unsigned lds_base) {
    unsigned sv;
    asm volatile("s_mov_b32 %0, m0\n\ts_mov_b32 m0, %2\n\ts_nop 0\n\tglobal_load_lds_dwordx4 %1, off\n\ts_mov_b32 m0, %0"
                 : "=&s"(sv) : "v"(g), "s"(lds_base) : "memory");
}
#define WAIT_BAR(N) asm volatile("s_waitcnt vmcnt(" #N ") lgkmcnt(0)\n\ts_barrier" ::: "memory")

#define STPOL "sc1"
DI void gst128(void* p, u32x4 v) { asm volatile("global_store_dwordx4 %0, %1, off " STPOL "\n\ts_nop 1" :: "v"(p), "v"(v) : "memory"); }
DI bf16x8 lds_b128(unsigned addr) { return *(const LDS_AS bf16x8*)(uintptr_t)addr; }
DI s16x4 lds_tr(unsigned addr) {
    return __builtin_bit_cast(s16x4, __builtin_amdgcn_ds_read_tr16_b64_v4i16((LDS_AS s16x4*)(uintptr_t)addr));
}
DI bf16x8 cat8(s16x4 a, s16x4 b) { return __builtin_shufflevector(a, b, 0, 1, 2, 3, 4, 5, 6, 7); }

DI u32x4 pack8(const f32x16& a, int base, float sc) {
    u32x4 r;
    r[0] = pk_bf16(a[base + 0] * sc, a[base + 1] * sc);
    r[1] = pk_bf16(a[base + 2] * sc, a[base + 3] * sc);
    r[2] = pk_bf16(a[base + 4] * sc, a[base + 5] * sc);
    r[3] = pk_bf16(a[base + 6] * sc, a[base + 7] * sc);
    return r;
}

constexpr int P_XS = 0, P_WS = 16384, P_STAGE = 32768, P_WX = 4 * P_STAGE, P_GT = P_WX + 16384, P_WO = P_GT + 9216, P_CNT = P_WO + 4096, P_LDS = P_CNT + 16;
#define SBAR() __builtin_amdgcn_sched_barrier(0)

struct PSrc { const unsigned short *xs0, *xs1, *wsr; int ldw; };
DI PSrc proj_src(const unsigned short* x, const unsigned short* wsrc, int ldw, int mt, int nt, int l, int lane) {
    PSrc s;
    const int row = 32 * l + (lane >> 3);
    s.xs0 = x + (size_t)(mt * 128 + row) * 512 + ((lane & 7) ^ ((row >> 1) & 7)) * 8;
    s.xs1 = x + (size_t)(mt * 128 + row + 8) * 512 + ((lane & 7) ^ (((row + 8) >> 1) & 7)) * 8;
    const int krow = 16 * l + (lane >> 4);
    s.wsr = wsrc + (size_t)krow * ldw + ((lane & 15) ^ (((lane >> 4) & 3) << 2)) * 8;
    s.ldw = ldw;
    return s;
}
DI void proj_issue(const PSrc& s, int c, int l, unsigned lds0) {
    const unsigned st = lds0 + (c & 3) * P_STAGE + l * 4096;
    glds16(s.xs0 + c * 64, st + P_XS);
    glds16(s.xs1 + c * 64, st + P_XS + 1024);
    glds16(s.xs0 + c * 64 + 16 * 512, st + P_XS + 2048);
    glds16(s.xs1 + c * 64 + 16 * 512, st + P_XS + 3072);
#pragma unroll
    for (int i = 0; i < 4; ++i) glds16(s.wsr + (size_t)(c * 64 + 4 * i) * s.ldw, st + P_WS + i * 1024);
}
DI float wave_sum63(float v) {
#define DPP_ADD(ctrl, rm) v += __int_as_float(__builtin_amdgcn_update_dpp(0, __float_as_int(v), ctrl, rm, 0xf, false))
    DPP_ADD(0x111, 0xf); DPP_ADD(0x112, 0xf); DPP_ADD(0x114, 0xf); DPP_ADD(0x118, 0xf);
    DPP_ADD(0x142, 0xa); DPP_ADD(0x143, 0xc);
#undef DPP_ADD
    return v;
}
DI void lds_spin(const char* smem, int off, unsigned target) {
    while (__builtin_amdgcn_readfirstlane(*(const volatile unsigned*)(smem + off)) < target) __builtin_amdgcn_s_sleep(1);
    asm volatile("" ::: "memory");
}

template <bool ISV>
DI void proj_compute(const PSrc& src, const unsigned short* __restrict__ x, const unsigned short* __restrict__ wx,
                     const unsigned short* __restrict__ gw, const unsigned short* __restrict__ wo, char* __restrict__ ws,
                     int mt, int nt, int w, unsigned lds0, char* smem) {
    const int lane = threadIdx.x & 63;
    const int wm = w >> 1, wn = w & 1;
    const int r = lane & 31, h = lane >> 5;
    __builtin_amdgcn_s_setprio(2);
    proj_issue(src, 0, w, lds0);
    unsigned xo[4][2];
#pragma unroll
    for (int tt = 0; tt < 2; ++tt) {
        const int row = wm * 64 + tt * 32 + r;
#pragma unroll
        for (int ks = 0; ks < 4; ++ks) xo[ks][tt] = lds0 + P_XS + row * 128 + (((2 * ks + h) ^ ((row >> 1) & 7)) << 4);
    }
    const int q4 = (lane & 15) >> 2, p4 = lane & 3, g1 = (lane >> 4) & 1;
    unsigned wa[2];
#pragma unroll
    for (int ct = 0; ct < 2; ++ct)
        wa[ct] = lds0 + P_WS + (8 * h + q4) * 256 + ((4 * ((2 * wn + ct) ^ q4) + 2 * g1 + (p4 >> 1)) << 4) + (p4 & 1) * 8;
    f32x16 acc[2][2];
#pragma unroll
    for (int i = 0; i < 16; ++i) { acc[0][0][i] = 0.f; acc[0][1][i] = 0.f; acc[1][0][i] = 0.f; acc[1][1][i] = 0.f; }
    bf16x8 fx[2][4][2], fw[2][4][2];
    auto mm = [&](int bf, int ks) {
#pragma unroll
        for (int ct = 0; ct < 2; ++ct)
#pragma unroll
            for (int tt = 0; tt < 2; ++tt)
                acc[ct][tt] = ISV ? MFMA32(fx[bf][ks][tt], fw[bf][ks][ct], acc[ct][tt]) : MFMA32(fw[bf][ks][ct], fx[bf][ks][tt], acc[ct][tt]);
    };
    auto rd = [&](int bf, int ks, unsigned so) {
#pragma unroll
        for (int tt = 0; tt < 2; ++tt) fx[bf][ks][tt] = lds_b128(xo[ks][tt] + so);
#pragma unroll
        for (int ct = 0; ct < 2; ++ct) fw[bf][ks][ct] = cat8(lds_tr(wa[ct] + so + ks * 4096), lds_tr(wa[ct] + so + ks * 4096 + 1024));
    };
    auto step = [&](int bf, int c) {
        asm volatile("s_waitcnt lgkmcnt(0)\n\ts_barrier" ::: "memory");
        const unsigned so = (c & 3) * P_STAGE;
#pragma unroll
        for (int ks = 0; ks < 4; ++ks) { rd(bf, ks, so); mm(bf ^ 1, ks); }
#pragma unroll
        for (int i = 0; i < 12; ++i) {
            __builtin_amdgcn_sched_group_barrier(0x008, 1, 0);
            __builtin_amdgcn_sched_group_barrier(0x002, 1, 0);
            __builtin_amdgcn_sched_group_barrier(0x100, 2, 0);
        }
        __builtin_amdgcn_sched_group_barrier(0x008, 4, 0);
        SBAR();
    };
    asm volatile("" : "+v"(xo[0][0]), "+v"(xo[0][1]), "+v"(xo[1][0]), "+v"(xo[1][1]), "+v"(xo[2][0]), "+v"(xo[2][1]),
                      "+v"(xo[3][0]), "+v"(xo[3][1]), "+v"(wa[0]), "+v"(wa[1]),
                      "+v"(acc[0][0]), "+v"(acc[0][1]), "+v"(acc[1][0]), "+v"(acc[1][1]));
    asm volatile("s_waitcnt vmcnt(0)" ::: "memory");
    __builtin_amdgcn_s_setprio(0);
    if (lane == 0) atomicAdd((unsigned*)(smem + P_CNT), 1u);
    lds_spin(smem, P_CNT, 4u);
    ST(0, 1);
    rd(0, 0, 0); rd(0, 1, 0); rd(0, 2, 0); rd(0, 3, 0);
    SBAR();
    {
        if (nt >= 8) {
            const unsigned short* es = wx + (size_t)((lane >> 5) * 64 + 16 * w) * 16 + (lane & 31) * 8;
#pragma unroll
            for (int i = 0; i < 4; ++i) glds16(es + i * 2048, lds0 + P_WX + w * 4096 + i * 1024);
        }
        const unsigned short* px = x + (size_t)(mt * 128 + nt * 8 + 2 * w) * 512 + lane * 8;
        glds16(px, lds0 + P_GT + w * 2048);
        glds16(px + 512, lds0 + P_GT + w * 2048 + 1024);
        glds16(gw + lane * 8, lds0 + P_GT + 8192);
        glds16(wo + (size_t)(4 * w + (lane >> 4)) * 512 + ((lane & 15) ^ (((lane >> 4) & 3) << 2)) * 8, lds0 + P_WO + w * 1024);
    }

#pragma unroll 1
    for (int c = 1; c < 7; c += 2) { step(1, c); step(0, c + 1); }
    asm volatile("s_waitcnt vmcnt(0)" ::: "memory");
    step(1, 7);
    ST(0, 2);
    mm(1, 0); mm(1, 1); mm(1, 2); mm(1, 3);
    ST(0, 3);
#pragma unroll
    for (int ct = 0; ct < 2; ++ct)
#pragma unroll
        for (int tt = 0; tt < 2; ++tt) {
            const int hd0 = (nt & 3) * 8 + wn * 4 + ct * 2;
            const int tile = mt * 4 + wm * 2 + tt;
            if (ISV) {
                char* dst = ws + WS_KV + (size_t)(hd0 + (r >> 4)) * 131072 + tile * 2048 + 1024 + h * 256 + (r & 15) * 16;
                gst128(dst, pack8(acc[ct][tt], 0, 1.f));
                gst128(dst + 512, pack8(acc[ct][tt], 8, 1.f));
            } else {
                const bool isq = nt < 8;
                const float sc = isq ? QSCALE : 1.f;
                const size_t hs = isq ? 65536 : 131072;
                char* dst = isq ? (ws + WS_Q + (size_t)((nt >> 2) * 32 + hd0) * 65536 + tile * 1024 + lane * 16)
                                : (ws + WS_KV + (size_t)hd0 * 131072 + tile * 2048 + lane * 16);
                gst128(dst, pack8(acc[ct][tt], 0, sc));
                gst128(dst + hs, pack8(acc[ct][tt], 8, sc));
            }
        }
}

template <int XM>
DI void proj_loader(const PSrc& src, const unsigned short* __restrict__ x, const unsigned short* __restrict__ wk,
                    const unsigned short* __restrict__ wv, const unsigned short* __restrict__ gw, char* __restrict__ ws,
                    int mt, int nt, int l, unsigned lds0, char* smem) {
    const int lane = threadIdx.x & 63;
    const int r = lane & 31, h = lane >> 5;
    constexpr bool isx = XM != 0, isv = XM == 2;
    const int xtt = nt & 3;
    proj_issue(src, 1, l, lds0); proj_issue(src, 2, l, lds0);
    const int q4 = (lane & 15) >> 2, p4 = lane & 3;
    const unsigned ea = lds0 + P_WX + l * 4096 + (8 * h + q4) * 32 + p4 * 8;
    const int xrow = xtt * 32 + r;
    const unsigned exa = lds0 + P_XS + xrow * 128 + (((2 * l + h) ^ ((xrow >> 1) & 7)) << 4);
    const int gtok = mt * 128 + nt * 8 + 2 * l;
    u32x4 gx0, gx1, gv;
    bf16x8 xe[8];
    if (isx) { lds_spin(smem, P_CNT, 4u); xe[0] = lds_b128(exa); }
#pragma unroll
    for (int c = 1; c < 8; ++c) {
        if (c < 7) WAIT_BAR(8); else WAIT_BAR(0);
        if (isx) xe[c] = lds_b128(exa + (c & 3) * P_STAGE);
        if (c < 6) proj_issue(src, c + 2, l, lds0);
    }
    gx0 = *(const u32x4*)(smem + P_GT + l * 2048 + lane * 16);
    gx1 = *(const u32x4*)(smem + P_GT + l * 2048 + 1024 + lane * 16);
    gv  = *(const u32x4*)(smem + P_GT + 8192 + lane * 16);
    {
        const int g1 = (lane >> 4) & 1;
        const unsigned woa = lds0 + P_WO + (4 * h + q4) * 256 + ((4 * (l ^ q4) + 2 * g1 + (p4 >> 1)) << 4) + (p4 & 1) * 8;
        const bf16x8 f = cat8(lds_tr(woa), lds_tr(woa + 2048));
        gst128(ws + WS_WO + (size_t)(blockIdx.x * 4 + l) * 1024 + lane * 16, __builtin_bit_cast(u32x4, f));
    }
    {
        float s0 = 0.f, s1 = 0.f;
#pragma unroll
        for (int i = 0; i < 4; ++i) {
            const float wl = __uint_as_float(gv[i] << 16), wh = __uint_as_float(gv[i] & 0xffff0000u);
            s0 += __uint_as_float(gx0[i] << 16) * wl + __uint_as_float(gx0[i] & 0xffff0000u) * wh;
            s1 += __uint_as_float(gx1[i] << 16) * wl + __uint_as_float(gx1[i] & 0xffff0000u) * wh;
        }
        s0 = wave_sum63(s0); s1 = wave_sum63(s1);
        if (lane == 63) {
            f32x2 g = {__builtin_amdgcn_rcpf(1.f + __expf(-s0)), __builtin_amdgcn_rcpf(1.f + __expf(-s1))};
            *(f32x2*)((float*)(ws + WS_G) + gtok) = g;
        }
    }
    f32x16 acce;
#pragma unroll
    for (int i = 0; i < 16; ++i) acce[i] = 0.f;
    if (isx) {
        __builtin_amdgcn_s_setprio(3);
#pragma unroll
        for (int c = 0; c < 8; ++c) {
            const bf16x8 we = cat8(lds_tr(ea + c * 512), lds_tr(ea + c * 512 + 128));
            acce = isv ? MFMA32(xe[c], we, acce) : MFMA32(we, xe[c], acce);
        }
    }
    if (isx) {
        f32x4* red = (f32x4*)smem;
#pragma unroll
        for (int g = 0; g < 4; ++g) {
            f32x4 v = {acce[4 * g], acce[4 * g + 1], acce[4 * g + 2], acce[4 * g + 3]};
            red[(l * 4 + g) * 64 + lane] = v;
        }
        unsigned old = 0;
        asm volatile("s_waitcnt lgkmcnt(0)" ::: "memory");
        if (lane == 0) old = atomicAdd((unsigned*)(smem + P_CNT), 1u);
        old = __builtin_amdgcn_readfirstlane(old);
        if (old == 7) {
            asm volatile("" ::: "memory");
#pragma unroll
            for (int g = 0; g < 4; ++g) {
                const f32x4 v = red[g * 64 + lane] + red[(4 + g) * 64 + lane] + red[(8 + g) * 64 + lane] + red[(12 + g) * 64 + lane];
#pragma unroll
                for (int i = 0; i < 4; ++i) acce[4 * g + i] = v[i];
            }
            char* base = ws + WS_KV + (size_t)32 * 131072 + (mt * 4 + xtt) * 2048;
            if (isv) {
                if (r < 16) {
                    gst128(base + 1024 + h * 256 + r * 16, pack8(acce, 0, 1.f));
                    gst128(base + 1536 + h * 256 + r * 16, pack8(acce, 8, 1.f));
                }
            } else {
                gst128(base + lane * 16, pack8(acce, 0, 1.f));
            }
        }
    }
}

__global__ __launch_bounds__(512, 2) void proj_kernel(const unsigned short* __restrict__ x, const unsigned short* __restrict__ wq,
                                                      const unsigned short* __restrict__ wk, const unsigned short* __restrict__ wv,
                                                      const unsigned short* __restrict__ wqkv, const unsigned short* __restrict__ gw,
                                                      const unsigned short* __restrict__ woA, const unsigned short* __restrict__ woB,
                                                      char* __restrict__ ws) {
    extern __shared__ __attribute__((aligned(16))) char smem[];
    const unsigned lds0 = (unsigned)(uintptr_t)smem;
    const int w = __builtin_amdgcn_readfirstlane(threadIdx.x >> 6);
    const int b = blockIdx.x, xcd = b & 7, slot = b >> 3;
    const int mt = (xcd & 3) * 4 + (slot & 3), nt = (xcd >> 2) * 8 + (slot >> 2);
    ST(0, 0);
    unsigned cpf = 0;
    if (w == 7) {
        unsigned long long pc; int len;
        asm volatile("s_getpc_b64 %0\n\tpf_text_begin:\n\ts_mov_b32 %1, pf_text_end-pf_text_begin" : "=s"(pc), "=s"(len));
        int off = ((slot & (8 * CODEPF - 1)) * 32 + (int)(threadIdx.x & 31)) * 128;
        off = off < len ? off : 0;
        asm volatile("global_load_dword %0, %1, %2" : "=&v"(cpf) : "v"(off), "s"(pc) : "memory");
    }
    if (threadIdx.x == 0) *(unsigned*)(smem + P_CNT) = 0u;
    const unsigned short* wsrc = (nt < 4) ? (wq + nt * 128) : (wqkv + (nt - 4) * 128);
    const PSrc src = proj_src(x, wsrc, (nt < 4) ? 512 : 1536, mt, nt, w & 3, threadIdx.x & 63);
    const unsigned short* wo = ((b < 128) ? woA : woB) + (size_t)((b >> 2) & 31) * 16 * 512 + (b & 3) * 128;
    asm volatile("s_waitcnt lgkmcnt(0)\n\ts_barrier" ::: "memory");
    if (w >= 4) {
        if (nt >= 12)     proj_loader<2>(src, x, wk, wv, gw, ws, mt, nt, w - 4, lds0, smem);
        else if (nt >= 8) proj_loader<1>(src, x, wk, wv, gw, ws, mt, nt, w - 4, lds0, smem);
        else              proj_loader<0>(src, x, wk, wv, gw, ws, mt, nt, w - 4, lds0, smem);
        asm volatile("" :: "v"(cpf));
        ST(0, 1); ST(0, 2); ST(0, 3);
    } else if (nt >= 12) proj_compute<true>(src, x, wv, gw, wo, ws, mt, nt, w, lds0, smem);
    else                 proj_compute<false>(src, x, wk, gw, wo, ws, mt, nt, w, lds0, smem);
    ST(0, 4);
}

constexpr int A_KV = 0, A_CONST = 131072, A_LDS = A_CONST + 8192;

DI float max16(const f32x16& s) {
    float a = fmaxf(fmaxf(s[0], s[1]), s[2]), b = fmaxf(fmaxf(s[3], s[4]), s[5]);
    a = fmaxf(fmaxf(a, s[6]), s[7]);   b = fmaxf(fmaxf(b, s[8]), s[9]);
    a = fmaxf(fmaxf(a, s[10]), s[11]); b = fmaxf(fmaxf(b, s[12]), s[13]);
    a = fmaxf(fmaxf(a, s[14]), s[15]);
    return fmaxf(a, b);
}
DI float half_max(float m) {
    auto rr = __builtin_amdgcn_permlane32_swap(__float_as_uint(m), __float_as_uint(m), false, false);
    return fmaxf(__uint_as_float(rr[0]), __uint_as_float(rr[1]));
}

#define R_SA "192"
#define R_SB "208"
#define EX(X, i) "v_exp_f32 v[" X "+" #i "], v[" X "+" #i "]\n\t"
#define CV(X, j, a, b) "v_cvt_pk_bf16_f32 v[224+" #j "], v[" X "+" #a "], v[" X "+" #b "]\n\t"
#define HALF(X, Y, OX, OY, NY, QY, WA, RA, WB, RB, WC, RC)                                        \
    EX(X, 0) EX(X, 1) WA                                                                          \
    "v_mfma_f32_32x32x16_bf16 " OY ", %[v1], v[228:231], " OY "\n\t" RA                           \
    EX(X, 2) EX(X, 3) CV(X, 0, 0, 1) EX(X, 4) EX(X, 5) CV(X, 1, 2, 3) WB                          \
    "v_mfma_f32_32x32x16_bf16 v[" Y ":" Y "+15], %[kf], " QY ", " NY "\n\t" RB                    \
    EX(X, 6) EX(X, 7) CV(X, 2, 4, 5) EX(X, 8) EX(X, 9) CV(X, 3, 6, 7) EX(X, 10) EX(X, 11) WC      \
    "v_mfma_f32_32x32x16_bf16 " OX ", %[v0], v[224:227], " OX "\n\t" RC                           \
    CV(X, 4, 8, 9) EX(X, 12) EX(X, 13) CV(X, 5, 10, 11) EX(X, 14) EX(X, 15) CV(X, 6, 12, 13) CV(X, 7, 14, 15)
#define TILE(OFF_V1, OFF_KN, OFF_V0N, P0, P1)                                                     \
    HALF(R_SA, R_SB, "%[oa]", "%[ob]", "%[nb]", "%[qb]",                                          \
         "", "ds_read_b128 %[v1], %[va] offset:" OFF_V1 "\n\t" P0,                                \
         "", "ds_read_b128 %[kf], %[ka] offset:" OFF_KN "\n\t",                                   \
         "s_waitcnt lgkmcnt(2)\n\t", "")                                                          \
    HALF(R_SB, R_SA, "%[ob]", "%[oa]", "%[na]", "%[qa]",                                          \
         "s_waitcnt lgkmcnt(1)\n\t", P1,                                                          \
         "s_waitcnt lgkmcnt(0)\n\t", "",                                                          \
         "", "ds_read_b128 %[v0], %[va] offset:" OFF_V0N "\n\t")
#define ATTN_ASM(PY, PO)                                                                          \
    asm volatile(                                                                                 \
        "ds_read_b128 %[kf], %[ka]\n\t"                                                           \
        "ds_read_b128 %[v1], %[va]\n\t"                                                           \
        "ds_read_b128 %[v0], %[va]\n\t"                                                           \
        "v_mov_b32 v228, 0\n\tv_mov_b32 v229, 0\n\tv_mov_b32 v230, 0\n\tv_mov_b32 v231, 0\n\t"    \
        "s_waitcnt lgkmcnt(0)\n\t"                                                                \
        "v_mfma_f32_32x32x16_bf16 v[192:207], %[kf], %[qa], %[na]\n\t"                            \
        "s_nop 7\n\ts_nop 7\n\t"                                                                  \
        "L_loop_%=:\n\t"                                                                          \
        "s_cmp_lg_u32 %[cnt], %[s1]\n\t"                                                          \
        "s_cbranch_scc1 L_n1_%=\n\t"                                                              \
        "s_waitcnt vmcnt(0)\n\t"                                                                  \
        "s_barrier\n\t"                                                                           \
        "L_n1_%=:\n\t"                                                                            \
        "s_cmp_lg_u32 %[cnt], 9\n\t"                                                              \
        "s_cbranch_scc1 L_n2_%=\n\t"                                                              \
        "global_load_dword v232, %[pf], off\n\t"                                                  \
        "L_n2_%=:\n\t"                                                                            \
        TILE("512", "2048", "2048", "s_setprio 0\n\t", PY)                                        \
        TILE("2560", "4096", "4096", PO, "")                                                      \
        TILE("4608", "6144", "6144", "", "")                                                      \
        HALF(R_SA, R_SB, "%[oa]", "%[ob]", "%[nb]", "%[qb]",                                      \
             "", "ds_read_b128 %[v1], %[va] offset:6656\n\t",                                     \
             "", "ds_read_b128 %[kf], %[ka] offset:8192\n\tv_add_u32 %[va], %[va], %[vs]\n\t",    \
             "s_waitcnt lgkmcnt(2)\n\t", "v_add_u32 %[ka], 0x2000, %[ka]\n\t")                    \
        HALF(R_SB, R_SA, "%[ob]", "%[oa]", "%[na]", "%[qa]",                                      \
             "s_waitcnt lgkmcnt(1)\n\t", "s_add_u32 %[cnt], %[cnt], 1\n\t",                       \
             "s_waitcnt lgkmcnt(0)\n\t", "s_cmp_lg_u32 %[cnt], 16\n\t",                           \
             "", "ds_read_b128 %[v0], %[va]\n\t")                                                 \
        "s_cbranch_scc1 L_loop_%=\n\t"                                                            \
        "s_nop 1\n\t"                                                                             \
        "v_mfma_f32_32x32x16_bf16 %[ob], %[v1], v[228:231], %[ob]\n\t"                            \
        "s_waitcnt vmcnt(0) lgkmcnt(0)\n\t"                                                                \
        "s_nop 7\n\ts_nop 7\n\t"                                                                  \
        : [oa] "+v"(oa), [ob] "+v"(ob), [ka] "+v"(ka), [va] "+v"(va), [cnt] "+s"(cnt),            \
          [kf] "=&v"(kf), [v0] "=&v"(v0), [v1] "=&v"(v1)                                          \
        : [qa] "v"(qa), [qb] "v"(qb), [na] "v"(na), [nb] "v"(nb), [vs] "v"(vstep), [s1] "s"(sync1), [pf] "v"(pf) \
        : "memory", "scc",                                                                        \
          "v192", "v193", "v194", "v195", "v196", "v197", "v198", "v199", "v200", "v201", "v202", "v203", "v204", "v205", "v206", "v207", \
          "v208", "v209", "v210", "v211", "v212", "v213", "v214", "v215", "v216", "v217", "v218", "v219", "v220", "v221", "v222", "v223", \
          "v224", "v225", "v226", "v227", "v228", "v229", "v230", "v231", "v232")

template <bool YOUNG>
DI void attn_main(unsigned ka, unsigned va, unsigned vstep, const bf16x8& qa, const bf16x8& qb, const f32x16& na, const f32x16& nb,
                  f32x16& oa, f32x16& ob, int sync1, const char* pf) {
    bf16x8 kf, v0, v1;
    int cnt = 0;
    if (YOUNG) ATTN_ASM("s_setprio 1\n\t", "");
    else       ATTN_ASM("", "s_setprio 1\n\t");
}

__global__ __launch_bounds__(512, 2) void attn_kernel(char* __restrict__ ws) {
    extern __shared__ __attribute__((aligned(16))) char smem[];
    const unsigned lds0 = (unsigned)(uintptr_t)smem;
    const int tid = threadIdx.x, lane = tid & 63;
    const int w = __builtin_amdgcn_readfirstlane(tid >> 6);
    const int r = lane & 31, h = lane >> 5;
    const int b = blockIdx.x, xcd = b & 7, slot = b >> 3;
    const int isB = (slot < 16);
    const int hd = xcd * 4 + ((slot & 15) >> 2);
    const int hh = isB ? 32 + hd : hd;
    const int kvh = isB ? hd : 32;
    const int tile = (slot & 3) * 16 + 2 * w;

    ST(1, 0);
    bf16x8 qa, qb;
    {
        const char* qsrc = ws + WS_Q + (size_t)hh * 65536 + tile * 1024 + lane * 16;
        asm volatile("global_load_dwordx4 %0, %2, off\n\tglobal_load_dwordx4 %1, %2, off offset:1024"
                     : "=&v"(qa), "=&v"(qb) : "v"(qsrc) : "memory");
    }
    const char* kvsrc = ws + WS_KV + (size_t)kvh * 131072 + w * 1024 + lane * 16;
#pragma unroll
    for (int i = 0; i < 4; ++i) glds16(kvsrc + i * 8192, lds0 + A_KV + i * 8192 + w * 1024);
    {
        const unsigned one = ((tid & 15) == 0 || (tid & 15) == 4) ? 0x3f803f80u : 0u;
        u32x4 c = {one, one, one, one};
        *(u32x4*)(smem + A_CONST + tid * 16) = c;
    }
    const unsigned kaddr = lds0 + A_KV + lane * 16;
    const unsigned vaddr = (r < 16) ? (lds0 + A_KV + 1024 + h * 256 + r * 16) : (lds0 + A_CONST + (r - 16) * 16);
    const unsigned vstep = (r < 16) ? 8192u : 0u;

    asm volatile("s_waitcnt vmcnt(0) lgkmcnt(0)\n\ts_barrier" : "+v"(qa), "+v"(qb) :: "memory");
    ST(1, 1);
    if (w >= 4) {
#pragma unroll
        for (int i = 0; i < 24; ++i) glds16(kvsrc + 28672 + i * 4096, lds0 + A_KV + 28672 + i * 4096 + w * 1024);
    }

    float ga, gb;
    {
        const float* gsrc = (const float*)(ws + WS_G) + tile * 32 + r;
        asm volatile("global_load_dword %0, %2, off\n\tglobal_load_dword %1, %2, off offset:128" : "=&v"(ga), "=&v"(gb) : "v"(gsrc) : "memory");
    }
    const char* wpf = ws + WS_WO + (size_t)slot * 32768 + w * 4096 + r * 128;
    f32x16 zero;
#pragma unroll
    for (int i = 0; i < 16; ++i) zero[i] = 0.f;
    f32x16 na, nb, oa = zero, ob = zero;
    {
        const bf16x8 k0 = lds_b128(kaddr);
        const f32x16 sa = MFMA32(k0, qa, zero);
        const f32x16 sb = MFMA32(k0, qb, zero);
        const float ma = -half_max(max16(sa)) - REFOFF, mb = -half_max(max16(sb)) - REFOFF;
#pragma unroll
        for (int i = 0; i < 16; ++i) { na[i] = ma; nb[i] = mb; }
    }
    ST(1, 2);
    if (w >= 4) attn_main<true>(kaddr, vaddr, vstep, qa, qb, na, nb, oa, ob, 2, wpf);
    else        attn_main<false>(kaddr, vaddr, vstep, qa, qb, na, nb, oa, ob, 3, wpf);
    ST(1, 3);
    asm volatile("" : "+v"(ga), "+v"(gb));

    {
        bool bad = !(oa[8] < 1.329228e36f) || !(ob[8] < 1.329228e36f);
#pragma unroll
        for (int i = 0; i < 8; ++i) bad = bad || !(fabsf(oa[i]) <= 3.0e38f) || !(fabsf(ob[i]) <= 3.0e38f);
        if (__any(bad)) {
            float ma = -INFINITY, mb = -INFINITY;
#pragma unroll 1
            for (int t = 0; t < 64; ++t) {
                const bf16x8 kf = lds_b128(kaddr + t * 2048);
                ma = fmaxf(ma, max16(MFMA32(kf, qa, zero)));
                mb = fmaxf(mb, max16(MFMA32(kf, qb, zero)));
            }
            ma = -half_max(ma); mb = -half_max(mb);
#pragma unroll
            for (int i = 0; i < 16; ++i) { na[i] = ma; nb[i] = mb; }
            oa = zero; ob = zero;
            attn_main<false>(kaddr, vaddr, vstep, qa, qb, na, nb, oa, ob, -1, wpf);
        }
    }
    const float sa = (isB ? 1.f - ga : ga) * __builtin_amdgcn_rcpf(oa[8]), sb = (isB ? 1.f - gb : gb) * __builtin_amdgcn_rcpf(ob[8]);
    char* odst = ws + WS_O + (size_t)tile * 65536 + hh * 1024 + lane * 16;
    gst128(odst, pack8(oa, 0, sa));
    gst128(odst + 65536, pack8(ob, 0, sb));
    ST(1, 4);
}

constexpr int O_LDS = 4 * 3 * 4096;

__global__ __launch_bounds__(256, 1) void out_kernel(const char* __restrict__ ws, unsigned short* __restrict__ out) {
    extern __shared__ __attribute__((aligned(16))) char smem[];
    const int tid = threadIdx.x, lane = tid & 63;
    const int w = __builtin_amdgcn_readfirstlane(tid >> 6);
    const int r = lane & 31, h = lane >> 5;
    const int b = blockIdx.x, xcd = b & 7, slot = b >> 3;
    const int mt = xcd * 4 + (slot & 3), nt = slot >> 2;

    ST(2, 0);
    const char* osrc = ws + WS_O + (size_t)(mt * 2) * 65536 + (w * 16) * 1024 + lane * 16;
    const char* wsrc = ws + WS_WO + (size_t)(w * 16) * 16384 + (nt * 2) * 1024 + lane * 16;
#define O_ROT 0
    const int rot = O_ROT == 1 ? ((2 * nt + 4 * (slot & 3) + ((slot >> 1) & 1)) & 15) : (O_ROT == 2 ? 2 * xcd : 0);
    bf16x8 of[16][2], wf[16][2];
#pragma unroll
    for (int k = 0; k < 16; ++k) {
        const int kk = (k + rot) & 15;
        of[k][0] = *(const bf16x8*)(osrc + kk * 1024);
        of[k][1] = *(const bf16x8*)(osrc + 65536 + kk * 1024);
        wf[k][0] = *(const bf16x8*)(wsrc + kk * 16384);
        wf[k][1] = *(const bf16x8*)(wsrc + kk * 16384 + 1024);
    }
    SBAR();
    f32x16 acc[4];
#pragma unroll
    for (int i = 0; i < 16; ++i) { acc[0][i] = 0.f; acc[1][i] = 0.f; acc[2][i] = 0.f; acc[3][i] = 0.f; }
#pragma unroll
    for (int k = 0; k < 16; ++k)
#pragma unroll
        for (int q = 0; q < 4; ++q) acc[q] = MFMA32(wf[k][q >> 1], of[k][q & 1], acc[q]);
    ST(2, 1);
    f32x16 fin;
#pragma unroll
    for (int q = 0; q < 4; ++q) {
        if (q == w) { fin = acc[q]; }
        else {
            char* dst = smem + (q * 3 + ((w - q - 1) & 3)) * 4096 + lane * 16;
#pragma unroll
            for (int g = 0; g < 4; ++g) {
                f32x4 v = {acc[q][4 * g], acc[q][4 * g + 1], acc[q][4 * g + 2], acc[q][4 * g + 3]};
                *(f32x4*)(dst + g * 1024) = v;
            }
        }
    }
    __syncthreads();
    ST(2, 2);
#pragma unroll
    for (int p = 0; p < 3; ++p)
#pragma unroll
        for (int g = 0; g < 4; ++g) {
            const f32x4 v = *(const f32x4*)(smem + (w * 3 + p) * 4096 + g * 1024 + lane * 16);
#pragma unroll
            for (int i = 0; i < 4; ++i) fin[4 * g + i] += v[i];
        }
    unsigned short* drow = out + (size_t)(mt * 64 + (w & 1) * 32 + r) * 512 + nt * 64 + (w >> 1) * 32 + h * 8;
#pragma unroll
    for (int g = 0; g < 4; g += 2) {
        unsigned a0 = pk_bf16(fin[4 * g], fin[4 * g + 1]), a1 = pk_bf16(fin[4 * g + 2], fin[4 * g + 3]);
        unsigned b0 = pk_bf16(fin[4 * g + 4], fin[4 * g + 5]), b1 = pk_bf16(fin[4 * g + 6], fin[4 * g + 7]);
        auto r0 = __builtin_amdgcn_permlane32_swap(a0, b0, false, false);
        auto r1 = __builtin_amdgcn_permlane32_swap(a1, b1, false, false);
        u32x4 res = {r0[0], r1[0], r0[1], r1[1]};
        gst128(drow + g * 8, res);
    }
    ST(2, 3);
    asm volatile("pf_text_end:");
}

extern "C" void kernel_launch(void* const* d_in, const int* in_sizes, int n_in, void* d_out, int out_size, void* d_ws,
                              size_t ws_size, hipStream_t stream) {
    const unsigned short* x    = (const unsigned short*)d_in[0];
    const unsigned short* wq   = (const unsigned short*)d_in[1];
    const unsigned short* wk   = (const unsigned short*)d_in[2];
    const unsigned short* wv   = (const unsigned short*)d_in[3];
    const unsigned short* woA  = (const unsigned short*)d_in[4];
    const unsigned short* wqkv = (const unsigned short*)d_in[6];
    const unsigned short* woB  = (const unsigned short*)d_in[7];
    const unsigned short* gw   = (const unsigned short*)d_in[8];
    char* ws = (char*)d_ws;
    (void)hipFuncSetAttribute((const void*)proj_kernel, hipFuncAttributeMaxDynamicSharedMemorySize, P_LDS);
    (void)hipFuncSetAttribute((const void*)attn_kernel, hipFuncAttributeMaxDynamicSharedMemorySize, A_LDS);
    (void)hipFuncSetAttribute((const void*)out_kernel, hipFuncAttributeMaxDynamicSharedMemorySize, O_LDS);
    proj_kernel<<<256, 512, P_LDS, stream>>>(x, wq, wk, wv, wqkv, gw, woA, woB, ws);
    attn_kernel<<<256, 512, A_LDS, stream>>>(ws);
    out_kernel<<<256, 256, O_LDS, stream>>>(ws, (unsigned short*)d_out);
}
```

```cpp
#include <hip/hip_runtime.h>
#include <hip/hip_bf16.h>
#include <stdint.h>

typedef short    bf16x8 __attribute__((ext_vector_type(8)));
typedef short    s16x4  __attribute__((ext_vector_type(4)));
typedef float    f32x16 __attribute__((ext_vector_type(16)));
typedef float    f32x4  __attribute__((ext_vector_type(4)));
typedef float    f32x2  __attribute__((ext_vector_type(2)));
typedef unsigned u32x4  __attribute__((ext_vector_type(4)));
typedef __bf16   bf16x2v __attribute__((ext_vector_type(2)));

#define DI __device__ __forceinline__
#define MFMA32(a, b, c) __builtin_amdgcn_mfma_f32_32x32x16_bf16((a), (b), (c), 0, 0, 0)
#define LDS_AS __attribute__((address_space(3)))

constexpr size_t WS_Q  = 0;
constexpr size_t WS_KV = WS_Q + (size_t)64 * 65536;
constexpr size_t WS_O  = WS_KV + (size_t)34 * 131072;
constexpr size_t WS_WO = WS_O + (size_t)64 * 65536;
constexpr size_t WS_G  = WS_WO + (size_t)1024 * 1024;

#define ST(k, i)
#define CODEPF 1
#define REFOFF 96.f
constexpr float QSCALE = 0.25f * 1.4426950408889634f;

DI unsigned pk_bf16(float lo, float hi) {
    f32x2 v = {lo, hi};
    bf16x2v r = __builtin_convertvector(v, bf16x2v);
    return __builtin_bit_cast(unsigned, r);
}
DI float bf2f(unsigned short u) { return __uint_as_float(((unsigned)u) << 16); }

DI void glds16(const void* g, unsigned lds_base) {
    unsigned sv;
    asm volatile("s_mov_b32 %0, m0\n\ts_mov_b32 m0, %2\n\ts_nop 0\n\tglobal_load_lds_dwordx4 %1, off\n\ts_mov_b32 m0, %0"
                 : "=&s"(sv) : "v"(g), "s"(lds_base) : "memory");
}
#define WAIT_BAR(N) asm volatile("s_waitcnt vmcnt(" #N ") lgkmcnt(0)\n\ts_barrier" ::: "memory")

#define STPOL "sc1"
DI void gst128(void* p, u32x4 v) { asm volatile("global_store_dwordx4 %0, %1, off " STPOL "\n\ts_nop 1" :: "v"(p), "v"(v) : "memory"); }
DI bf16x8 lds_b128(unsigned addr) { return *(const LDS_AS bf16x8*)(uintptr_t)addr; }
DI s16x4 lds_tr(unsigned addr) {
    return __builtin_bit_cast(s16x4, __builtin_amdgcn_ds_read_tr16_b64_v4i16((LDS_AS s16x4*)(uintptr_t)addr));
}
DI bf16x8 cat8(s16x4 a, s16x4 b) { return __builtin_shufflevector(a, b, 0, 1, 2, 3, 4, 5, 6, 7); }

DI u32x4 pack8(const f32x16& a, int base, float sc) {
    u32x4 r;
    r[0] = pk_bf16(a[base + 0] * sc, a[base + 1] * sc);
    r[1] = pk_bf16(a[base + 2] * sc, a[base + 3] * sc);
    r[2] = pk_bf16(a[base + 4] * sc, a[base + 5] * sc);
    r[3] = pk_bf16(a[base + 6] * sc, a[base + 7] * sc);
    return r;
}

constexpr int P_XS = 0, P_WS = 16384, P_STAGE = 32768, P_WX = 4 * P_STAGE, P_GT = P_WX + 16384, P_WO = P_GT + 9216, P_CNT = P_WO + 4096, P_LDS = P_CNT + 16;
#define SBAR() __builtin_amdgcn_sched_barrier(0)

struct PSrc { const unsigned short *xs0, *xs1, *wsr; int ldw; };
DI PSrc proj_src(const unsigned short* x, const unsigned short* wsrc, int ldw, int mt, int nt, int l, int lane) {
    PSrc s;
    const int row = 32 * l + (lane >> 3);
    s.xs0 = x + (size_t)(mt * 128 + row) * 512 + ((lane & 7) ^ ((row >> 1) & 7)) * 8;
    s.xs1 = x + (size_t)(mt * 128 + row + 8) * 512 + ((lane & 7) ^ (((row + 8) >> 1) & 7)) * 8;
    const int krow = 16 * l + (lane >> 4);
    s.wsr = wsrc + (size_t)krow * ldw + ((lane & 15) ^ (((lane >> 4) & 3) << 2)) * 8;
    s.ldw = ldw;
    return s;
}
DI void proj_issue(const PSrc& s, int c, int l, unsigned lds0) {
    const unsigned st = lds0 + (c & 3) * P_STAGE + l * 4096;
    glds16(s.xs0 + c * 64, st + P_XS);
    glds16(s.xs1 + c * 64, st + P_XS + 1024);
    glds16(s.xs0 + c * 64 + 16 * 512, st + P_XS + 2048);
    glds16(s.xs1 + c * 64 + 16 * 512, st + P_XS + 3072);
#pragma unroll
    for (int i = 0; i < 4; ++i) glds16(s.wsr + (size_t)(c * 64 + 4 * i) * s.ldw, st + P_WS + i * 1024);
}
DI float wave_sum63(float v) {
#define DPP_ADD(ctrl, rm) v += __int_as_float(__builtin_amdgcn_update_dpp(0, __float_as_int(v), ctrl, rm, 0xf, false))
    DPP_ADD(0x111, 0xf); DPP_ADD(0x112, 0xf); DPP_ADD(0x114, 0xf); DPP_ADD(0x118, 0xf);
    DPP_ADD(0x142, 0xa); DPP_ADD(0x143, 0xc);
#undef DPP_ADD
    return v;
}
DI void lds_spin(const char* smem, int off, unsigned target) {
    while (__builtin_amdgcn_readfirstlane(*(const volatile unsigned*)(smem + off)) < target) __builtin_amdgcn_s_sleep(1);
    asm volatile("" ::: "memory");
}

template <bool ISV>
DI void proj_compute(const PSrc& src, const unsigned short* __restrict__ x, const unsigned short* __restrict__ wx,
                     const unsigned short* __restrict__ gw, const unsigned short* __restrict__ wo, char* __restrict__ ws,
                     int mt, int nt, int w, unsigned lds0, char* smem) {
    const int lane = threadIdx.x & 63;
    const int wm = w >> 1, wn = w & 1;
    const int r = lane & 31, h = lane >> 5;
    unsigned xo[4][2];
#pragma unroll
    for (int tt = 0; tt < 2; ++tt) {
        const int row = wm * 64 + tt * 32 + r;
#pragma unroll
        for (int ks = 0; ks < 4; ++ks) xo[ks][tt] = lds0 + P_XS + row * 128 + (((2 * ks + h) ^ ((row >> 1) & 7)) << 4);
    }
    const int q4 = (lane & 15) >> 2, p4 = lane & 3, g1 = (lane >> 4) & 1;
    unsigned wa[2];
#pragma unroll
    for (int ct = 0; ct < 2; ++ct)
        wa[ct] = lds0 + P_WS + (8 * h + q4) * 256 + ((4 * ((2 * wn + ct) ^ q4) + 2 * g1 + (p4 >> 1)) << 4) + (p4 & 1) * 8;
    f32x16 acc[2][2];
#pragma unroll
    for (int i = 0; i < 16; ++i) { acc[0][0][i] = 0.f; acc[0][1][i] = 0.f; acc[1][0][i] = 0.f; acc[1][1][i] = 0.f; }
    bf16x8 fx[2][4][2], fw[2][4][2];
    auto mm = [&](int bf, int ks) {
#pragma unroll
        for (int ct = 0; ct < 2; ++ct)
#pragma unroll
            for (int tt = 0; tt < 2; ++tt)
                acc[ct][tt] = ISV ? MFMA32(fx[bf][ks][tt], fw[bf][ks][ct], acc[ct][tt]) : MFMA32(fw[bf][ks][ct], fx[bf][ks][tt], acc[ct][tt]);
    };
    auto rd = [&](int bf, int ks, unsigned so) {
#pragma unroll
        for (int tt = 0; tt < 2; ++tt) fx[bf][ks][tt] = lds_b128(xo[ks][tt] + so);
#pragma unroll
        for (int ct = 0; ct < 2; ++ct) fw[bf][ks][ct] = cat8(lds_tr(wa[ct] + so + ks * 4096), lds_tr(wa[ct] + so + ks * 4096 + 1024));
    };
    auto step = [&](int bf, int c) {
        asm volatile("s_waitcnt lgkmcnt(0)\n\ts_barrier" ::: "memory");
        const unsigned so = (c & 3) * P_STAGE;
#pragma unroll
        for (int ks = 0; ks < 4; ++ks) { rd(bf, ks, so); mm(bf ^ 1, ks); }
#pragma unroll
        for (int i = 0; i < 12; ++i) {
            __builtin_amdgcn_sched_group_barrier(0x008, 1, 0);
            __builtin_amdgcn_sched_group_barrier(0x002, 1, 0);
            __builtin_amdgcn_sched_group_barrier(0x100, 2, 0);
        }
        __builtin_amdgcn_sched_group_barrier(0x008, 4, 0);
        SBAR();
    };
    asm volatile("" : "+v"(xo[0][0]), "+v"(xo[0][1]), "+v"(xo[1][0]), "+v"(xo[1][1]), "+v"(xo[2][0]), "+v"(xo[2][1]),
                      "+v"(xo[3][0]), "+v"(xo[3][1]), "+v"(wa[0]), "+v"(wa[1]),
                      "+v"(acc[0][0]), "+v"(acc[0][1]), "+v"(acc[1][0]), "+v"(acc[1][1]));
    asm volatile("s_waitcnt vmcnt(0)" ::: "memory");
    if (lane == 0) atomicAdd((unsigned*)(smem + P_CNT), 1u);
    lds_spin(smem, P_CNT, 4u);
    ST(0, 1);
    rd(0, 0, 0); rd(0, 1, 0); rd(0, 2, 0); rd(0, 3, 0);
    SBAR();
    {
        if (nt >= 8) {
            const unsigned short* es = wx + (size_t)((lane >> 5) * 64 + 16 * w) * 16 + (lane & 31) * 8;
#pragma unroll
            for (int i = 0; i < 4; ++i) glds16(es + i * 2048, lds0 + P_WX + w * 4096 + i * 1024);
        }
        const unsigned short* px = x + (size_t)(mt * 128 + nt * 8 + 2 * w) * 512 + lane * 8;
        glds16(px, lds0 + P_GT + w * 2048);
        glds16(px + 512, lds0 + P_GT + w * 2048 + 1024);
        glds16(gw + lane * 8, lds0 + P_GT + 8192);
        glds16(wo + (size_t)(4 * w + (lane >> 4)) * 512 + ((lane & 15) ^ (((lane >> 4) & 3) << 2)) * 8, lds0 + P_WO + w * 1024);
    }

#pragma unroll 1
    for (int c = 1; c < 7; c += 2) { step(1, c); step(0, c + 1); }
    asm volatile("s_waitcnt vmcnt(0)" ::: "memory");
    step(1, 7);
    ST(0, 2);
#pragma unroll
    for (int ct = 0; ct < 2; ++ct)
#pragma unroll
        for (int tt = 0; tt < 2; ++tt)
#pragma unroll
            for (int ks = 0; ks < 4; ++ks)
                acc[ct][tt] = ISV ? MFMA32(fx[1][ks][tt], fw[1][ks][ct], acc[ct][tt]) : MFMA32(fw[1][ks][ct], fx[1][ks][tt], acc[ct][tt]);
    ST(0, 3);
#pragma unroll
    for (int ct = 0; ct < 2; ++ct)
#pragma unroll
        for (int tt = 0; tt < 2; ++tt) {
            const int hd0 = (nt & 3) * 8 + wn * 4 + ct * 2;
            const int tile = mt * 4 + wm * 2 + tt;
            if (ISV) {
                char* dst = ws + WS_KV + (size_t)(hd0 + (r >> 4)) * 131072 + tile * 2048 + 1024 + h * 256 + (r & 15) * 16;
                gst128(dst, pack8(acc[ct][tt], 0, 1.f));
                gst128(dst + 512, pack8(acc[ct][tt], 8, 1.f));
            } else {
                const bool isq = nt < 8;
                const float sc = isq ? QSCALE : 1.f;
                const size_t hs = isq ? 65536 : 131072;
                char* dst = isq ? (ws + WS_Q + (size_t)((nt >> 2) * 32 + hd0) * 65536 + tile * 1024 + lane * 16)
                                : (ws + WS_KV + (size_t)hd0 * 131072 + tile * 2048 + lane * 16);
                gst128(dst, pack8(acc[ct][tt], 0, sc));
                gst128(dst + hs, pack8(acc[ct][tt], 8, sc));
            }
        }
}

template <int XM>
DI void proj_loader(const PSrc& src, const unsigned short* __restrict__ x, const unsigned short* __restrict__ wk,
                    const unsigned short* __restrict__ wv, const unsigned short* __restrict__ gw, char* __restrict__ ws,
                    int mt, int nt, int l, unsigned lds0, char* smem) {
    const int lane = threadIdx.x & 63;
    const int r = lane & 31, h = lane >> 5;
    constexpr bool isx = XM != 0, isv = XM == 2;
    const int xtt = nt & 3;
    proj_issue(src, 2, l, lds0);
    const int q4 = (lane & 15) >> 2, p4 = lane & 3;
    const unsigned ea = lds0 + P_WX + l * 4096 + (8 * h + q4) * 32 + p4 * 8;
    const int xrow = xtt * 32 + r;
    const unsigned exa = lds0 + P_XS + xrow * 128 + (((2 * l + h) ^ ((xrow >> 1) & 7)) << 4);
    const int gtok = mt * 128 + nt * 8 + 2 * l;
    u32x4 gx0, gx1, gv;
    bf16x8 xe[8];
    if (isx) { lds_spin(smem, P_CNT, 4u); xe[0] = lds_b128(exa); }
#pragma unroll
    for (int c = 1; c < 8; ++c) {
        if (c < 7) WAIT_BAR(8); else WAIT_BAR(0);
        if (isx) xe[c] = lds_b128(exa + (c & 3) * P_STAGE);
        if (c < 6) proj_issue(src, c + 2, l, lds0);
    }
    gx0 = *(const u32x4*)(smem + P_GT + l * 2048 + lane * 16);
    gx1 = *(const u32x4*)(smem + P_GT + l * 2048 + 1024 + lane * 16);
    gv  = *(const u32x4*)(smem + P_GT + 8192 + lane * 16);
    {
        const int g1 = (lane >> 4) & 1;
        const unsigned woa = lds0 + P_WO + (4 * h + q4) * 256 + ((4 * (l ^ q4) + 2 * g1 + (p4 >> 1)) << 4) + (p4 & 1) * 8;
        const bf16x8 f = cat8(lds_tr(woa), lds_tr(woa + 2048));
        gst128(ws + WS_WO + (size_t)(blockIdx.x * 4 + l) * 1024 + lane * 16, __builtin_bit_cast(u32x4, f));
    }
    {
        float s0 = 0.f, s1 = 0.f;
#pragma unroll
        for (int i = 0; i < 4; ++i) {
            const float wl = __uint_as_float(gv[i] << 16), wh = __uint_as_float(gv[i] & 0xffff0000u);
            s0 += __uint_as_float(gx0[i] << 16) * wl + __uint_as_float(gx0[i] & 0xffff0000u) * wh;
            s1 += __uint_as_float(gx1[i] << 16) * wl + __uint_as_float(gx1[i] & 0xffff0000u) * wh;
        }
        s0 = wave_sum63(s0); s1 = wave_sum63(s1);
        if (lane == 63) {
            f32x2 g = {__builtin_amdgcn_rcpf(1.f + __expf(-s0)), __builtin_amdgcn_rcpf(1.f + __expf(-s1))};
            *(f32x2*)((float*)(ws + WS_G) + gtok) = g;
        }
    }
    f32x16 acce;
#pragma unroll
    for (int i = 0; i < 16; ++i) acce[i] = 0.f;
    if (isx) {
        __builtin_amdgcn_s_setprio(3);
#pragma unroll
        for (int c = 0; c < 8; ++c) {
            const bf16x8 we = cat8(lds_tr(ea + c * 512), lds_tr(ea + c * 512 + 128));
            acce = isv ? MFMA32(xe[c], we, acce) : MFMA32(we, xe[c], acce);
        }
    }
    if (isx) {
        f32x4* red = (f32x4*)smem;
#pragma unroll
        for (int g = 0; g < 4; ++g) {
            f32x4 v = {acce[4 * g], acce[4 * g + 1], acce[4 * g + 2], acce[4 * g + 3]};
            red[(l * 4 + g) * 64 + lane] = v;
        }
        unsigned old = 0;
        asm volatile("s_waitcnt lgkmcnt(0)" ::: "memory");
        if (lane == 0) old = atomicAdd((unsigned*)(smem + P_CNT), 1u);
        old = __builtin_amdgcn_readfirstlane(old);
        if (old == 7) {
            asm volatile("" ::: "memory");
#pragma unroll
            for (int g = 0; g < 4; ++g) {
                const f32x4 v = red[g * 64 + lane] + red[(4 + g) * 64 + lane] + red[(8 + g) * 64 + lane] + red[(12 + g) * 64 + lane];
#pragma unroll
                for (int i = 0; i < 4; ++i) acce[4 * g + i] = v[i];
            }
            char* base = ws + WS_KV + (size_t)32 * 131072 + (mt * 4 + xtt) * 2048;
            if (isv) {
                if (r < 16) {
                    gst128(base + 1024 + h * 256 + r * 16, pack8(acce, 0, 1.f));
                    gst128(base + 1536 + h * 256 + r * 16, pack8(acce, 8, 1.f));
                }
            } else {
                gst128(base + lane * 16, pack8(acce, 0, 1.f));
            }
        }
    }
}

__global__ __launch_bounds__(512, 2) void proj_kernel(const unsigned short* __restrict__ x, const unsigned short* __restrict__ wq,
                                                      const unsigned short* __restrict__ wk, const unsigned short* __restrict__ wv,
                                                      const unsigned short* __restrict__ wqkv, const unsigned short* __restrict__ gw,
                                                      const unsigned short* __restrict__ woA, const unsigned short* __restrict__ woB,
                                                      char* __restrict__ ws) {
    extern __shared__ __attribute__((aligned(16))) char smem[];
    const unsigned lds0 = (unsigned)(uintptr_t)smem;
    const int w = __builtin_amdgcn_readfirstlane(threadIdx.x >> 6);
    const int b = blockIdx.x, xcd = b & 7, slot = b >> 3;
    const int mt = (xcd & 3) * 4 + (slot & 3), nt = (xcd >> 2) * 8 + (slot >> 2);
    ST(0, 0);
    unsigned cpf = 0;
    if (w == 7) {
        unsigned long long pc; int len;
        asm volatile("s_getpc_b64 %0\n\tpf_text_begin:\n\ts_mov_b32 %1, pf_text_end-pf_text_begin" : "=s"(pc), "=s"(len));
        int off = ((slot & (8 * CODEPF - 1)) * 32 + (int)(threadIdx.x & 31)) * 128;
        off = off < len ? off : 0;
        asm volatile("global_load_dword %0, %1, %2" : "=&v"(cpf) : "v"(off), "s"(pc) : "memory");
    }
    if (threadIdx.x == 0) *(unsigned*)(smem + P_CNT) = 0u;
    const unsigned short* wsrc = (nt < 4) ? (wq + nt * 128) : (wqkv + (nt - 4) * 128);
    const PSrc src = proj_src(x, wsrc, (nt < 4) ? 512 : 1536, mt, nt, w & 3, threadIdx.x & 63);
    const unsigned short* wo = ((b < 128) ? woA : woB) + (size_t)((b >> 2) & 31) * 16 * 512 + (b & 3) * 128;
    proj_issue(src, w >> 2, w & 3, lds0);
    asm volatile("s_waitcnt lgkmcnt(0)\n\ts_barrier" ::: "memory");
    if (w >= 4) {
        if (nt >= 12)     proj_loader<2>(src, x, wk, wv, gw, ws, mt, nt, w - 4, lds0, smem);
        else if (nt >= 8) proj_loader<1>(src, x, wk, wv, gw, ws, mt, nt, w - 4, lds0, smem);
        else              proj_loader<0>(src, x, wk, wv, gw, ws, mt, nt, w - 4, lds0, smem);
        asm volatile("" :: "v"(cpf));
        ST(0, 1); ST(0, 2); ST(0, 3);
    } else if (nt >= 12) proj_compute<true>(src, x, wv, gw, wo, ws, mt, nt, w, lds0, smem);
    else                 proj_compute<false>(src, x, wk, gw, wo, ws, mt, nt, w, lds0, smem);
    ST(0, 4);
}

constexpr int A_KV = 0, A_CONST = 131072, A_LDS = A_CONST + 8192;

DI float max16(const f32x16& s) {
    float a = fmaxf(fmaxf(s[0], s[1]), s[2]), b = fmaxf(fmaxf(s[3], s[4]), s[5]);
    a = fmaxf(fmaxf(a, s[6]), s[7]);   b = fmaxf(fmaxf(b, s[8]), s[9]);
    a = fmaxf(fmaxf(a, s[10]), s[11]); b = fmaxf(fmaxf(b, s[12]), s[13]);
    a = fmaxf(fmaxf(a, s[14]), s[15]);
    return fmaxf(a, b);
}
DI float half_max(float m) {
    auto rr = __builtin_amdgcn_permlane32_swap(__float_as_uint(m), __float_as_uint(m), false, false);
    return fmaxf(__uint_as_float(rr[0]), __uint_as_float(rr[1]));
}

#define R_SA "192"
#define R_SB "208"
#define EX(X, i) "v_exp_f32 v[" X "+" #i "], v[" X "+" #i "]\n\t"
#define CV(X, j, a, b) "v_cvt_pk_bf16_f32 v[224+" #j "], v[" X "+" #a "], v[" X "+" #b "]\n\t"
#define HALF(X, Y, OX, OY, NY, QY, WA, RA, WB, RB, WC, RC)                                        \
    EX(X, 0) EX(X, 1) WA                                                                          \
    "v_mfma_f32_32x32x16_bf16 " OY ", %[v1], v[228:231], " OY "\n\t" RA                           \
    EX(X, 2) EX(X, 3) CV(X, 0, 0, 1) EX(X, 4) EX(X, 5) CV(X, 1, 2, 3) WB                          \
    "v_mfma_f32_32x32x16_bf16 v[" Y ":" Y "+15], %[kf], " QY ", " NY "\n\t" RB                    \
    EX(X, 6) EX(X, 7) CV(X, 2, 4, 5) EX(X, 8) EX(X, 9) CV(X, 3, 6, 7) EX(X, 10) EX(X, 11) WC      \
    "v_mfma_f32_32x32x16_bf16 " OX ", %[v0], v[224:227], " OX "\n\t" RC                           \
    CV(X, 4, 8, 9) EX(X, 12) EX(X, 13) CV(X, 5, 10, 11) EX(X, 14) EX(X, 15) CV(X, 6, 12, 13) CV(X, 7, 14, 15)
#define TILE(OFF_V1, OFF_KN, OFF_V0N, P0, P1)                                                     \
    HALF(R_SA, R_SB, "%[oa]", "%[ob]", "%[nb]", "%[qb]",                                          \
         "", "ds_read_b128 %[v1], %[va] offset:" OFF_V1 "\n\t" P0,                                \
         "", "ds_read_b128 %[kf], %[ka] offset:" OFF_KN "\n\t",                                   \
         "s_waitcnt lgkmcnt(2)\n\t", "")                                                          \
    HALF(R_SB, R_SA, "%[ob]", "%[oa]", "%[na]", "%[qa]",                                          \
         "s_waitcnt lgkmcnt(1)\n\t", P1,                                                          \
         "s_waitcnt lgkmcnt(0)\n\t", "",                                                          \
         "", "ds_read_b128 %[v0], %[va] offset:" OFF_V0N "\n\t")
#define KVDMA                                                                                     \
    "global_load_lds_dwordx4 %[lo], s[40:41]\n\t"                                                 \
    "s_add_u32 s40, s40, 0x2000\n\ts_addc_u32 s41, s41, 0\n\ts_add_u32 m0, m0, 0x2000\n\ts_nop 0\n\t"
#define ATTN_ASM(PY, PO)                                                                          \
    asm volatile(                                                                                 \
        "s_mov_b32 s44, m0\n\ts_mov_b32 m0, %[kd]\n\ts_mov_b64 s[40:41], %[kb]\n\ts_cmp_lg_u32 %[yg], 0\n\ts_cselect_b64 vcc, -1, 0\n\t"               \
        "ds_read_b128 %[kf], %[ka]\n\t"                                                           \
        "ds_read_b128 %[v1], %[va]\n\t"                                                           \
        "ds_read_b128 %[v0], %[va]\n\t"                                                           \
        "v_mov_b32 v228, 0\n\tv_mov_b32 v229, 0\n\tv_mov_b32 v230, 0\n\tv_mov_b32 v231, 0\n\t"    \
        "s_waitcnt lgkmcnt(0)\n\t"                                                                \
        "v_mfma_f32_32x32x16_bf16 v[192:207], %[kf], %[qa], %[na]\n\t"                            \
        "s_nop 7\n\ts_nop 7\n\t"                                                                  \
        "L_loop_%=:\n\t"                                                                          \
        "s_cmp_le_u32 %[cnt], 3\n\t"                                                              \
        "s_cbranch_scc1 L_sp_%=\n\t"                                                              \
        "L_go_%=:\n\t"                                                                            \
        TILE("512", "2048", "2048", "s_setprio 0\n\t", PY)                                        \
        TILE("2560", "4096", "4096", PO, "")                                                      \
        TILE("4608", "6144", "6144", "", "")                                                      \
        HALF(R_SA, R_SB, "%[oa]", "%[ob]", "%[nb]", "%[qb]",                                      \
             "", "ds_read_b128 %[v1], %[va] offset:6656\n\t",                                     \
             "", "ds_read_b128 %[kf], %[ka] offset:8192\n\tv_add_u32 %[va], %[va], %[vs]\n\t",    \
             "s_waitcnt lgkmcnt(2)\n\t", "v_add_u32 %[ka], 0x2000, %[ka]\n\t")                    \
        HALF(R_SB, R_SA, "%[ob]", "%[oa]", "%[na]", "%[qa]",                                      \
             "s_waitcnt lgkmcnt(1)\n\t", "s_add_u32 %[cnt], %[cnt], 1\n\t",                       \
             "s_waitcnt lgkmcnt(0)\n\t", "s_and_b32 s45, %[cnt], 15\n\t",                           \
             "", "ds_read_b128 %[v0], %[va]\n\t")                                                 \
        "s_cbranch_scc1 L_loop_%=\n\t"                                                            \
        "s_branch L_end_%=\n\t"                                                                   \
        "L_sp_%=:\n\t"                                                                            \
        "s_cmp_eq_u32 %[cnt], 3\n\t"                                                              \
        "s_cbranch_scc1 L_sy_%=\n\t"                                                              \
        KVDMA KVDMA KVDMA KVDMA                                                                   \
        "s_branch L_go_%=\n\t"                                                                    \
        "L_sy_%=:\n\t"                                                                            \
        "s_waitcnt vmcnt(0)\n\t"                                                                  \
        "s_barrier\n\t"                                                                           \
        "global_load_dword v232, %[pf], off\n\t"                                                  \
        "s_branch L_go_%=\n\t"                                                                    \
        "L_end_%=:\n\t"                                                                           \
        "s_nop 1\n\t"                                                                             \
        "v_mfma_f32_32x32x16_bf16 %[ob], %[v1], v[228:231], %[ob]\n\t"                            \
        "s_waitcnt vmcnt(0) lgkmcnt(0)\n\t"                                                       \
        "s_mov_b32 m0, s44\n\t"                                                                \
        "s_nop 7\n\ts_nop 7\n\t"                                                                  \
        : [oa] "+v"(oa), [ob] "+v"(ob), [ka] "+v"(ka), [va] "+v"(va), [cnt] "+s"(cnt),            \
          [kf] "=&v"(kf), [v0] "=&v"(v0), [v1] "=&v"(v1)                                          \
        : [qa] "v"(qa), [qb] "v"(qb), [na] "v"(na), [nb] "v"(nb), [vs] "v"(vstep), [yg] "s"(yg), [pf] "v"(pf), \
          [lo] "v"(lo), [kb] "s"(kb), [kd] "s"(kd)                                                \
        : "memory", "scc", "vcc", "s40", "s41", "s44", "s45",                                                                        \
          "v192", "v193", "v194", "v195", "v196", "v197", "v198", "v199", "v200", "v201", "v202", "v203", "v204", "v205", "v206", "v207", \
          "v208", "v209", "v210", "v211", "v212", "v213", "v214", "v215", "v216", "v217", "v218", "v219", "v220", "v221", "v222", "v223", \
          "v224", "v225", "v226", "v227", "v228", "v229", "v230", "v231", "v232")

DI void attn_main(bool young, bool first, unsigned ka, unsigned va, unsigned vstep, const bf16x8& qa, const bf16x8& qb, const f32x16& na,
                  const f32x16& nb, f32x16& oa, f32x16& ob, const char* pf, unsigned lo, const char* kb, unsigned kd) {
    bf16x8 kf, v0, v1;
    int cnt = __builtin_amdgcn_readfirstlane(first ? 0 : 16);
    const int yg = __builtin_amdgcn_readfirstlane(young ? 1 : 0);
    ATTN_ASM("s_cbranch_vccz L_py_%=\n\ts_setprio 1\n\tL_py_%=:\n\t", "s_setprio 1\n\t");
}

__global__ __launch_bounds__(512, 2) void attn_kernel(char* __restrict__ ws) {
    extern __shared__ __attribute__((aligned(16))) char smem[];
    const unsigned lds0 = (unsigned)(uintptr_t)smem;
    const int tid = threadIdx.x, lane = tid & 63;
    const int w = __builtin_amdgcn_readfirstlane(tid >> 6);
    const int r = lane & 31, h = lane >> 5;
    const int b = blockIdx.x, xcd = b & 7, slot = b >> 3;
    const int isB = (slot < 16);
    const int hd = xcd * 4 + ((slot & 15) >> 2);
    const int hh = isB ? 32 + hd : hd;
    const int kvh = isB ? hd : 32;
    const int tile = (slot & 3) * 16 + 2 * w;

    ST(1, 0);
    bf16x8 qa, qb;
    {
        const char* qsrc = ws + WS_Q + (size_t)hh * 65536 + tile * 1024 + lane * 16;
        asm volatile("global_load_dwordx4 %0, %2, off\n\tglobal_load_dwordx4 %1, %2, off offset:1024"
                     : "=&v"(qa), "=&v"(qb) : "v"(qsrc) : "memory");
    }
    const char* kvsrc = ws + WS_KV + (size_t)kvh * 131072 + w * 1024 + lane * 16;
#pragma unroll
    for (int i = 0; i < 4; ++i) glds16(kvsrc + i * 8192, lds0 + A_KV + i * 8192 + w * 1024);
    {
        const unsigned one = ((tid & 15) == 0 || (tid & 15) == 4) ? 0x3f803f80u : 0u;
        u32x4 c = {one, one, one, one};
        *(u32x4*)(smem + A_CONST + tid * 16) = c;
    }
    const unsigned kaddr = lds0 + A_KV + lane * 16;
    const unsigned vaddr = (r < 16) ? (lds0 + A_KV + 1024 + h * 256 + r * 16) : (lds0 + A_CONST + (r - 16) * 16);
    const unsigned vstep = (r < 16) ? 8192u : 0u;

    asm volatile("s_waitcnt vmcnt(0) lgkmcnt(0)\n\ts_barrier" : "+v"(qa), "+v"(qb) :: "memory");
    ST(1, 1);
    const char* kb = ws + WS_KV + (size_t)kvh * 131072 + 32768 + w * 1024;
    const unsigned kd = lds0 + A_KV + 32768 + w * 1024;
    float ga, gb;
    {
        const float* gsrc = (const float*)(ws + WS_G) + tile * 32 + r;
        asm volatile("global_load_dword %0, %2, off\n\tglobal_load_dword %1, %2, off offset:128" : "=&v"(ga), "=&v"(gb) : "v"(gsrc) : "memory");
    }
    const char* wpf = ws + WS_WO + (size_t)slot * 32768 + w * 4096 + r * 128;
    f32x16 zero;
#pragma unroll
    for (int i = 0; i < 16; ++i) zero[i] = 0.f;
    f32x16 na, nb, oa = zero, ob = zero;
    {
        const bf16x8 k0 = lds_b128(kaddr);
        const f32x16 sa = MFMA32(k0, qa, zero);
        const f32x16 sb = MFMA32(k0, qb, zero);
        const float ma = -half_max(max16(sa)) - REFOFF, mb = -half_max(max16(sb)) - REFOFF;
#pragma unroll
        for (int i = 0; i < 16; ++i) { na[i] = ma; nb[i] = mb; }
    }
    ST(1, 2);
    attn_main(w >= 4, true, kaddr, vaddr, vstep, qa, qb, na, nb, oa, ob, wpf, lane * 16, kb, kd);
    ST(1, 3);
    asm volatile("" : "+v"(ga), "+v"(gb));

    {
        float xa = fabsf(oa[0]), xb = fabsf(ob[0]), ta = oa[0], tb = ob[0];
#pragma unroll
        for (int i = 1; i < 8; ++i) { xa = fmaxf(xa, fabsf(oa[i])); xb = fmaxf(xb, fabsf(ob[i])); ta += oa[i]; tb += ob[i]; }
        const bool bad = !(oa[8] < 1.329228e36f) || !(ob[8] < 1.329228e36f) || !(fabsf(ta) <= 3.0e38f) || !(fabsf(tb) <= 3.0e38f) ||
                         !(xa > 7.7e-34f) || !(xb > 7.7e-34f);
        if (__any(bad)) {
            float ma = -INFINITY, mb = -INFINITY;
#pragma unroll 1
            for (int t = 0; t < 64; ++t) {
                const bf16x8 kf = lds_b128(kaddr + t * 2048);
                ma = fmaxf(ma, max16(MFMA32(kf, qa, zero)));
                mb = fmaxf(mb, max16(MFMA32(kf, qb, zero)));
            }
            ma = -half_max(ma); mb = -half_max(mb);
#pragma unroll
            for (int i = 0; i < 16; ++i) { na[i] = ma; nb[i] = mb; }
            oa = zero; ob = zero;
            attn_main(false, false, kaddr, vaddr, vstep, qa, qb, na, nb, oa, ob, wpf, lane * 16, kb, kd);
        }
    }
    const float sa = (isB ? 1.f - ga : ga) * __builtin_amdgcn_rcpf(oa[8]), sb = (isB ? 1.f - gb : gb) * __builtin_amdgcn_rcpf(ob[8]);
    char* odst = ws + WS_O + (size_t)tile * 65536 + hh * 1024 + lane * 16;
    gst128(odst, pack8(oa, 0, sa));
    gst128(odst + 65536, pack8(ob, 0, sb));
    ST(1, 4);
}

constexpr int O_LDS = 4 * 3 * 4096;

__global__ __launch_bounds__(256, 1) void out_kernel(const char* __restrict__ ws, unsigned short* __restrict__ out) {
    extern __shared__ __attribute__((aligned(16))) char smem[];
    const int tid = threadIdx.x, lane = tid & 63;
    const int w = __builtin_amdgcn_readfirstlane(tid >> 6);
    const int r = lane & 31, h = lane >> 5;
    const int b = blockIdx.x, xcd = b & 7, slot = b >> 3;
    const int mt = xcd * 4 + (slot & 3), nt = slot >> 2;

    ST(2, 0);
    const char* osrc = ws + WS_O + (size_t)(mt * 2) * 65536 + (w * 16) * 1024 + lane * 16;
    const char* wsrc = ws + WS_WO + (size_t)(w * 16) * 16384 + (nt * 2) * 1024 + lane * 16;
#define O_ROT 0
    const int rot = O_ROT == 1 ? ((2 * nt + 4 * (slot & 3) + ((slot >> 1) & 1)) & 15) : (O_ROT == 2 ? 2 * xcd : 0);
    bf16x8 of[16][2], wf[16][2];
#pragma unroll
    for (int k = 0; k < 16; ++k) {
        const int kk = (k + rot) & 15;
        of[k][0] = *(const bf16x8*)(osrc + kk * 1024);
        of[k][1] = *(const bf16x8*)(osrc + 65536 + kk * 1024);
        wf[k][0] = *(const bf16x8*)(wsrc + kk * 16384);
        wf[k][1] = *(const bf16x8*)(wsrc + kk * 16384 + 1024);
    }
    SBAR();
    f32x16 acc[4];
#pragma unroll
    for (int i = 0; i < 16; ++i) { acc[0][i] = 0.f; acc[1][i] = 0.f; acc[2][i] = 0.f; acc[3][i] = 0.f; }
#pragma unroll
    for (int k = 0; k < 16; ++k)
#pragma unroll
        for (int q = 0; q < 4; ++q) acc[q] = MFMA32(wf[k][q >> 1], of[k][q & 1], acc[q]);
    ST(2, 1);
    f32x16 fin;
#pragma unroll
    for (int q = 0; q < 4; ++q) {
        if (q == w) { fin = acc[q]; }
        else {
            char* dst = smem + (q * 3 + ((w - q - 1) & 3)) * 4096 + lane * 16;
#pragma unroll
            for (int g = 0; g < 4; ++g) {
                f32x4 v = {acc[q][4 * g], acc[q][4 * g + 1], acc[q][4 * g + 2], acc[q][4 * g + 3]};
                *(f32x4*)(dst + g * 1024) = v;
            }
        }
    }
    __syncthreads();
    ST(2, 2);
#pragma unroll
    for (int p = 0; p < 3; ++p)
#pragma unroll
        for (int g = 0; g < 4; ++g) {
            const f32x4 v = *(const f32x4*)(smem + (w * 3 + p) * 4096 + g * 1024 + lane * 16);
#pragma unroll
            for (int i = 0; i < 4; ++i) fin[4 * g + i] += v[i];
        }
    unsigned short* drow = out + (size_t)(mt * 64 + (w & 1) * 32 + r) * 512 + nt * 64 + (w >> 1) * 32 + h * 8;
#pragma unroll
    for (int g = 0; g < 4; g += 2) {
        unsigned a0 = pk_bf16(fin[4 * g], fin[4 * g + 1]), a1 = pk_bf16(fin[4 * g + 2], fin[4 * g + 3]);
        unsigned b0 = pk_bf16(fin[4 * g + 4], fin[4 * g + 5]), b1 = pk_bf16(fin[4 * g + 6], fin[4 * g + 7]);
        auto r0 = __builtin_amdgcn_permlane32_swap(a0, b0, false, false);
        auto r1 = __builtin_amdgcn_permlane32_swap(a1, b1, false, false);
        u32x4 res = {r0[0], r1[0], r0[1], r1[1]};
        gst128(drow + g * 8, res);
    }
    ST(2, 3);
    asm volatile("pf_text_end:");
}

extern "C" void kernel_launch(void* const* d_in, const int* in_sizes, int n_in, void* d_out, int out_size, void* d_ws,
                              size_t ws_size, hipStream_t stream) {
    const unsigned short* x    = (const unsigned short*)d_in[0];
    const unsigned short* wq   = (const unsigned short*)d_in[1];
    const unsigned short* wk   = (const unsigned short*)d_in[2];
    const unsigned short* wv   = (const unsigned short*)d_in[3];
    const unsigned short* woA  = (const unsigned short*)d_in[4];
    const unsigned short* wqkv = (const unsigned short*)d_in[6];
    const unsigned short* woB  = (const unsigned short*)d_in[7];
    const unsigned short* gw   = (const unsigned short*)d_in[8];
    char* ws = (char*)d_ws;
    (void)hipFuncSetAttribute((const void*)proj_kernel, hipFuncAttributeMaxDynamicSharedMemorySize, P_LDS);
    (void)hipFuncSetAttribute((const void*)attn_kernel, hipFuncAttributeMaxDynamicSharedMemorySize, A_LDS);
    (void)hipFuncSetAttribute((const void*)out_kernel, hipFuncAttributeMaxDynamicSharedMemorySize, O_LDS);
    proj_kernel<<<256, 512, P_LDS, stream>>>(x, wq, wk, wv, wqkv, gw, woA, woB, ws);
    attn_kernel<<<256, 512, A_LDS, stream>>>(ws);
    out_kernel<<<256, 256, O_LDS, stream>>>(ws, (unsigned short*)d_out);
}
```

```cpp
#include <hip/hip_runtime.h>
#include <hip/hip_bf16.h>
#include <stdint.h>

typedef short    bf16x8 __attribute__((ext_vector_type(8)));
typedef short    s16x4  __attribute__((ext_vector_type(4)));
typedef float    f32x16 __attribute__((ext_vector_type(16)));
typedef float    f32x4  __attribute__((ext_vector_type(4)));
typedef float    f32x2  __attribute__((ext_vector_type(2)));
typedef unsigned u32x4  __attribute__((ext_vector_type(4)));
typedef __bf16   bf16x2v __attribute__((ext_vector_type(2)));

#define DI __device__ __forceinline__
#define MFMA32(a, b, c) __builtin_amdgcn_mfma_f32_32x32x16_bf16((a), (b), (c), 0, 0, 0)
#define LDS_AS __attribute__((address_space(3)))

constexpr size_t WS_Q  = 0;
constexpr size_t WS_KV = WS_Q + (size_t)64 * 65536;
constexpr size_t WS_O  = WS_KV + (size_t)34 * 131072;
constexpr size_t WS_WO = WS_O + (size_t)64 * 65536;
constexpr size_t WS_G  = WS_WO + (size_t)1024 * 1024;

#define ST(k, i)
#define CODEPF 1
#define REFOFF 96.f
#define O_ORD 3
constexpr float QSCALE = 0.25f * 1.4426950408889634f;

DI unsigned pk_bf16(float lo, float hi) {
    f32x2 v = {lo, hi};
    bf16x2v r = __builtin_convertvector(v, bf16x2v);
    return __builtin_bit_cast(unsigned, r);
}
DI float bf2f(unsigned short u) { return __uint_as_float(((unsigned)u) << 16); }

DI void glds16(const void* g, unsigned lds_base) {
    unsigned sv;
    asm volatile("s_mov_b32 %0, m0\n\ts_mov_b32 m0, %2\n\ts_nop 0\n\tglobal_load_lds_dwordx4 %1, off\n\ts_mov_b32 m0, %0"
                 : "=&s"(sv) : "v"(g), "s"(lds_base) : "memory");
}
#define WAIT_BAR(N) asm volatile("s_waitcnt vmcnt(" #N ") lgkmcnt(0)\n\ts_barrier" ::: "memory")

#define STPOL "sc1"
DI void gst128(void* p, u32x4 v) { asm volatile("global_store_dwordx4 %0, %1, off " STPOL "\n\ts_nop 1" :: "v"(p), "v"(v) : "memory"); }
DI bf16x8 lds_b128(unsigned addr) { return *(const LDS_AS bf16x8*)(uintptr_t)addr; }
DI s16x4 lds_tr(unsigned addr) {
    return __builtin_bit_cast(s16x4, __builtin_amdgcn_ds_read_tr16_b64_v4i16((LDS_AS s16x4*)(uintptr_t)addr));
}
DI bf16x8 cat8(s16x4 a, s16x4 b) { return __builtin_shufflevector(a, b, 0, 1, 2, 3, 4, 5, 6, 7); }

DI u32x4 pack8(const f32x16& a, int base, float sc) {
    u32x4 r;
    r[0] = pk_bf16(a[base + 0] * sc, a[base + 1] * sc);
    r[1] = pk_bf16(a[base + 2] * sc, a[base + 3] * sc);
    r[2] = pk_bf16(a[base + 4] * sc, a[base + 5] * sc);
    r[3] = pk_bf16(a[base + 6] * sc, a[base + 7] * sc);
    return r;
}

constexpr int P_XS = 0, P_WS = 16384, P_STAGE = 32768, P_WX = 4 * P_STAGE, P_GT = P_WX + 16384, P_WO = P_GT + 9216, P_CNT = P_WO + 4096, P_LDS = P_CNT + 16;
#define SBAR() __builtin_amdgcn_sched_barrier(0)

struct PSrc { const unsigned short *xs0, *xs1, *wsr; int ldw; };
DI PSrc proj_src(const unsigned short* x, const unsigned short* wsrc, int ldw, int mt, int nt, int l, int lane) {
    PSrc s;
    const int row = 32 * l + (lane >> 3);
    s.xs0 = x + (size_t)(mt * 128 + row) * 512 + ((lane & 7) ^ ((row >> 1) & 7)) * 8;
    s.xs1 = x + (size_t)(mt * 128 + row + 8) * 512 + ((lane & 7) ^ (((row + 8) >> 1) & 7)) * 8;
    const int krow = 16 * l + (lane >> 4);
    s.wsr = wsrc + (size_t)krow * ldw + ((lane & 15) ^ (((lane >> 4) & 3) << 2)) * 8;
    s.ldw = ldw;
    return s;
}
DI void proj_issue(const PSrc& s, int c, int l, unsigned lds0) {
    const unsigned st = lds0 + (c & 3) * P_STAGE + l * 4096;
    glds16(s.xs0 + c * 64, st + P_XS);
    glds16(s.xs1 + c * 64, st + P_XS + 1024);
    glds16(s.xs0 + c * 64 + 16 * 512, st + P_XS + 2048);
    glds16(s.xs1 + c * 64 + 16 * 512, st + P_XS + 3072);
#pragma unroll
    for (int i = 0; i < 4; ++i) glds16(s.wsr + (size_t)(c * 64 + 4 * i) * s.ldw, st + P_WS + i * 1024);
}
DI float wave_sum63(float v) {
#define DPP_ADD(ctrl, rm) v += __int_as_float(__builtin_amdgcn_update_dpp(0, __float_as_int(v), ctrl, rm, 0xf, false))
    DPP_ADD(0x111, 0xf); DPP_ADD(0x112, 0xf); DPP_ADD(0x114, 0xf); DPP_ADD(0x118, 0xf);
    DPP_ADD(0x142, 0xa); DPP_ADD(0x143, 0xc);
#undef DPP_ADD
    return v;
}
DI void lds_spin(const char* smem, int off, unsigned target) {
    const volatile LDS_AS unsigned* p = (const volatile LDS_AS unsigned*)(uintptr_t)((unsigned)(uintptr_t)smem + off);
    while ((unsigned)__builtin_amdgcn_readfirstlane(*p) < target) __builtin_amdgcn_s_sleep(1);
    asm volatile("" ::: "memory");
}

template <bool ISV>
DI void proj_compute(const PSrc& src, const unsigned short* __restrict__ x, const unsigned short* __restrict__ wx,
                     const unsigned short* __restrict__ gw, const unsigned short* __restrict__ wo, char* __restrict__ ws,
                     int mt, int nt, int w, unsigned lds0, char* smem) {
    const int lane = threadIdx.x & 63;
    const int wm = w >> 1, wn = w & 1;
    const int r = lane & 31, h = lane >> 5;
    unsigned xo[4][2];
#pragma unroll
    for (int tt = 0; tt < 2; ++tt) {
        const int row = wm * 64 + tt * 32 + r;
#pragma unroll
        for (int ks = 0; ks < 4; ++ks) xo[ks][tt] = lds0 + P_XS + row * 128 + (((2 * ks + h) ^ ((row >> 1) & 7)) << 4);
    }
    const int q4 = (lane & 15) >> 2, p4 = lane & 3, g1 = (lane >> 4) & 1;
    unsigned wa[2];
#pragma unroll
    for (int ct = 0; ct < 2; ++ct)
        wa[ct] = lds0 + P_WS + (8 * h + q4) * 256 + ((4 * ((2 * wn + ct) ^ q4) + 2 * g1 + (p4 >> 1)) << 4) + (p4 & 1) * 8;
    f32x16 acc[2][2];
#pragma unroll
    for (int i = 0; i < 16; ++i) { acc[0][0][i] = 0.f; acc[0][1][i] = 0.f; acc[1][0][i] = 0.f; acc[1][1][i] = 0.f; }
    bf16x8 fx[2][4][2], fw[2][4][2];
    auto mm = [&](int bf, int ks) {
#pragma unroll
        for (int ct = 0; ct < 2; ++ct)
#pragma unroll
            for (int tt = 0; tt < 2; ++tt)
                acc[ct][tt] = ISV ? MFMA32(fx[bf][ks][tt], fw[bf][ks][ct], acc[ct][tt]) : MFMA32(fw[bf][ks][ct], fx[bf][ks][tt], acc[ct][tt]);
    };
    auto rd = [&](int bf, int ks, unsigned so) {
#pragma unroll
        for (int tt = 0; tt < 2; ++tt) fx[bf][ks][tt] = lds_b128(xo[ks][tt] + so);
#pragma unroll
        for (int ct = 0; ct < 2; ++ct) fw[bf][ks][ct] = cat8(lds_tr(wa[ct] + so + ks * 4096), lds_tr(wa[ct] + so + ks * 4096 + 1024));
    };
    auto step = [&](int bf, int c) {
        __builtin_amdgcn_s_waitcnt(0xC07F);
        asm volatile("s_barrier" ::: "memory");
        SBAR();
        const unsigned so = (c & 3) * P_STAGE;
#pragma unroll
        for (int ks = 0; ks < 4; ++ks) { rd(bf, ks, so); mm(bf ^ 1, ks); }
#pragma unroll
        for (int i = 0; i < 12; ++i) {
            __builtin_amdgcn_sched_group_barrier(0x008, 1, 0);
            __builtin_amdgcn_sched_group_barrier(0x002, 1, 0);
            __builtin_amdgcn_sched_group_barrier(0x100, 2, 0);
        }
        __builtin_amdgcn_sched_group_barrier(0x008, 4, 0);
        SBAR();
    };
    asm volatile("" : "+v"(xo[0][0]), "+v"(xo[0][1]), "+v"(xo[1][0]), "+v"(xo[1][1]), "+v"(xo[2][0]), "+v"(xo[2][1]),
                      "+v"(xo[3][0]), "+v"(xo[3][1]), "+v"(wa[0]), "+v"(wa[1]),
                      "+v"(acc[0][0]), "+v"(acc[0][1]), "+v"(acc[1][0]), "+v"(acc[1][1]));
    asm volatile("s_waitcnt vmcnt(0)" ::: "memory");
    if (lane == 0) atomicAdd((unsigned*)(smem + P_CNT), 1u);
    lds_spin(smem, P_CNT, 4u);
    ST(0, 1);
    rd(0, 0, 0); rd(0, 1, 0); rd(0, 2, 0); rd(0, 3, 0);
    SBAR();
    {
        if (nt >= 8) {
            const unsigned short* es = wx + (size_t)((lane >> 5) * 64 + 16 * w) * 16 + (lane & 31) * 8;
#pragma unroll
            for (int i = 0; i < 4; ++i) glds16(es + i * 2048, lds0 + P_WX + w * 4096 + i * 1024);
        }
        const unsigned short* px = x + (size_t)(mt * 128 + nt * 8 + 2 * w) * 512 + lane * 8;
        glds16(px, lds0 + P_GT + w * 2048);
        glds16(px + 512, lds0 + P_GT + w * 2048 + 1024);
        glds16(gw + lane * 8, lds0 + P_GT + 8192);
        glds16(wo + (size_t)(4 * w + (lane >> 4)) * 512 + ((lane & 15) ^ (((lane >> 4) & 3) << 2)) * 8, lds0 + P_WO + w * 1024);
    }

#pragma unroll 1
    for (int c = 1; c < 7; c += 2) { step(1, c); step(0, c + 1); }
    asm volatile("s_waitcnt vmcnt(0)" ::: "memory");
    step(1, 7);
    ST(0, 2);
#pragma unroll
    for (int ct = 0; ct < 2; ++ct)
#pragma unroll
        for (int tt = 0; tt < 2; ++tt)
#pragma unroll
            for (int ks = 0; ks < 4; ++ks)
                acc[ct][tt] = ISV ? MFMA32(fx[1][ks][tt], fw[1][ks][ct], acc[ct][tt]) : MFMA32(fw[1][ks][ct], fx[1][ks][tt], acc[ct][tt]);
    ST(0, 3);
#pragma unroll
    for (int ct = 0; ct < 2; ++ct)
#pragma unroll
        for (int tt = 0; tt < 2; ++tt) {
            const int hd0 = (nt & 3) * 8 + wn * 4 + ct * 2;
            const int tile = mt * 4 + wm * 2 + tt;
            if (ISV) {
                char* dst = ws + WS_KV + (size_t)(hd0 + (r >> 4)) * 131072 + tile * 2048 + 1024 + h * 256 + (r & 15) * 16;
                gst128(dst, pack8(acc[ct][tt], 0, 1.f));
                gst128(dst + 512, pack8(acc[ct][tt], 8, 1.f));
            } else {
                const bool isq = nt < 8;
                const float sc = isq ? QSCALE : 1.f;
                const size_t hs = isq ? 65536 : 131072;
                char* dst = isq ? (ws + WS_Q + (size_t)((nt >> 2) * 32 + hd0) * 65536 + tile * 1024 + lane * 16)
                                : (ws + WS_KV + (size_t)hd0 * 131072 + tile * 2048 + lane * 16);
                gst128(dst, pack8(acc[ct][tt], 0, sc));
                gst128(dst + hs, pack8(acc[ct][tt], 8, sc));
            }
        }
}

template <int XM>
DI void proj_loader(const PSrc& src, const unsigned short* __restrict__ x, const unsigned short* __restrict__ wk,
                    const unsigned short* __restrict__ wv, const unsigned short* __restrict__ gw, char* __restrict__ ws,
                    int mt, int nt, int l, unsigned lds0, char* smem) {
    const int lane = threadIdx.x & 63;
    const int r = lane & 31, h = lane >> 5;
    constexpr bool isx = XM != 0, isv = XM == 2;
    const int xtt = nt & 3;
    proj_issue(src, 2, l, lds0);
    const int q4 = (lane & 15) >> 2, p4 = lane & 3;
    const unsigned ea = lds0 + P_WX + l * 4096 + (8 * h + q4) * 32 + p4 * 8;
    const int xrow = xtt * 32 + r;
    const unsigned exa = lds0 + P_XS + xrow * 128 + (((2 * l + h) ^ ((xrow >> 1) & 7)) << 4);
    const int gtok = mt * 128 + nt * 8 + 2 * l;
    u32x4 gx0, gx1, gv;
    bf16x8 xe[8];
    if (isx) { lds_spin(smem, P_CNT, 4u); xe[0] = lds_b128(exa); }
#pragma unroll
    for (int c = 1; c < 8; ++c) {
        if (c < 7) WAIT_BAR(8); else WAIT_BAR(0);
        if (isx) xe[c] = lds_b128(exa + (c & 3) * P_STAGE);
        if (c < 6) proj_issue(src, c + 2, l, lds0);
    }
    gx0 = *(const u32x4*)(smem + P_GT + l * 2048 + lane * 16);
    gx1 = *(const u32x4*)(smem + P_GT + l * 2048 + 1024 + lane * 16);
    gv  = *(const u32x4*)(smem + P_GT + 8192 + lane * 16);
    {
        const int g1 = (lane >> 4) & 1;
        const unsigned woa = lds0 + P_WO + (4 * h + q4) * 256 + ((4 * (l ^ q4) + 2 * g1 + (p4 >> 1)) << 4) + (p4 & 1) * 8;
        const bf16x8 f = cat8(lds_tr(woa), lds_tr(woa + 2048));
        gst128(ws + WS_WO + (size_t)(blockIdx.x * 4 + l) * 1024 + lane * 16, __builtin_bit_cast(u32x4, f));
    }
    {
        float s0 = 0.f, s1 = 0.f;
#pragma unroll
        for (int i = 0; i < 4; ++i) {
            const float wl = __uint_as_float(gv[i] << 16), wh = __uint_as_float(gv[i] & 0xffff0000u);
            s0 += __uint_as_float(gx0[i] << 16) * wl + __uint_as_float(gx0[i] & 0xffff0000u) * wh;
            s1 += __uint_as_float(gx1[i] << 16) * wl + __uint_as_float(gx1[i] & 0xffff0000u) * wh;
        }
        s0 = wave_sum63(s0); s1 = wave_sum63(s1);
        if (lane == 63) {
            f32x2 g = {__builtin_amdgcn_rcpf(1.f + __expf(-s0)), __builtin_amdgcn_rcpf(1.f + __expf(-s1))};
            *(f32x2*)((float*)(ws + WS_G) + gtok) = g;
        }
    }
    f32x16 acce;
#pragma unroll
    for (int i = 0; i < 16; ++i) acce[i] = 0.f;
    if (isx) {
        __builtin_amdgcn_s_setprio(3);
#pragma unroll
        for (int c = 0; c < 8; ++c) {
            const bf16x8 we = cat8(lds_tr(ea + c * 512), lds_tr(ea + c * 512 + 128));
            acce = isv ? MFMA32(xe[c], we, acce) : MFMA32(we, xe[c], acce);
        }
    }
    if (isx) {
        f32x4* red = (f32x4*)smem;
#pragma unroll
        for (int g = 0; g < 4; ++g) {
            f32x4 v = {acce[4 * g], acce[4 * g + 1], acce[4 * g + 2], acce[4 * g + 3]};
            red[(l * 4 + g) * 64 + lane] = v;
        }
        unsigned old = 0;
        asm volatile("s_waitcnt lgkmcnt(0)" ::: "memory");
        if (lane == 0) old = atomicAdd((unsigned*)(smem + P_CNT), 1u);
        old = __builtin_amdgcn_readfirstlane(old);
        if (old == 7) {
            asm volatile("" ::: "memory");
#pragma unroll
            for (int g = 0; g < 4; ++g) {
                const f32x4 v = red[g * 64 + lane] + red[(4 + g) * 64 + lane] + red[(8 + g) * 64 + lane] + red[(12 + g) * 64 + lane];
#pragma unroll
                for (int i = 0; i < 4; ++i) acce[4 * g + i] = v[i];
            }
            char* base = ws + WS_KV + (size_t)32 * 131072 + (mt * 4 + xtt) * 2048;
            if (isv) {
                if (r < 16) {
                    gst128(base + 1024 + h * 256 + r * 16, pack8(acce, 0, 1.f));
                    gst128(base + 1536 + h * 256 + r * 16, pack8(acce, 8, 1.f));
                }
            } else {
                gst128(base + lane * 16, pack8(acce, 0, 1.f));
            }
        }
    }
}

__global__ __launch_bounds__(512, 2) void proj_kernel(const unsigned short* __restrict__ x, const unsigned short* __restrict__ wq,
                                                      const unsigned short* __restrict__ wk, const unsigned short* __restrict__ wv,
                                                      const unsigned short* __restrict__ wqkv, const unsigned short* __restrict__ gw,
                                                      const unsigned short* __restrict__ woA, const unsigned short* __restrict__ woB,
                                                      char* __restrict__ ws) {
    extern __shared__ __attribute__((aligned(16))) char smem[];
    const unsigned lds0 = (unsigned)(uintptr_t)smem;
    const int w = __builtin_amdgcn_readfirstlane(threadIdx.x >> 6);
    const int b = blockIdx.x, xcd = b & 7, slot = b >> 3;
    const int mt = (xcd & 3) * 4 + (slot & 3), nt = (xcd >> 2) * 8 + (slot >> 2);
    ST(0, 0);
    unsigned cpf = 0;
    if (w == 7) {
        unsigned long long pc; int len;
        asm volatile("s_getpc_b64 %0\n\tpf_text_begin:\n\ts_mov_b32 %1, pf_text_end-pf_text_begin" : "=s"(pc), "=s"(len));
        int off = ((slot & (8 * CODEPF - 1)) * 32 + (int)(threadIdx.x & 31)) * 128;
        off = off < len ? off : 0;
        asm volatile("global_load_dword %0, %1, %2" : "=&v"(cpf) : "v"(off), "s"(pc) : "memory");
    }
    if (threadIdx.x == 0) *(unsigned*)(smem + P_CNT) = 0u;
    const unsigned short* wsrc = (nt < 4) ? (wq + nt * 128) : (wqkv + (nt - 4) * 128);
    const PSrc src = proj_src(x, wsrc, (nt < 4) ? 512 : 1536, mt, nt, w & 3, threadIdx.x & 63);
    const unsigned short* wo = ((b < 128) ? woA : woB) + (size_t)((b >> 2) & 31) * 16 * 512 + (b & 3) * 128;
    proj_issue(src, w >> 2, w & 3, lds0);
    asm volatile("s_waitcnt lgkmcnt(0)\n\ts_barrier" ::: "memory");
    if (w >= 4) {
        if (nt >= 12)     proj_loader<2>(src, x, wk, wv, gw, ws, mt, nt, w - 4, lds0, smem);
        else if (nt >= 8) proj_loader<1>(src, x, wk, wv, gw, ws, mt, nt, w - 4, lds0, smem);
        else              proj_loader<0>(src, x, wk, wv, gw, ws, mt, nt, w - 4, lds0, smem);
        asm volatile("" :: "v"(cpf));
        ST(0, 1); ST(0, 2); ST(0, 3);
    } else if (nt >= 12) proj_compute<true>(src, x, wv, gw, wo, ws, mt, nt, w, lds0, smem);
    else                 proj_compute<false>(src, x, wk, gw, wo, ws, mt, nt, w, lds0, smem);
    ST(0, 4);
}

constexpr int A_KV = 0, A_CONST = 131072, A_LDS = A_CONST + 8192;

DI float max16(const f32x16& s) {
    float a = fmaxf(fmaxf(s[0], s[1]), s[2]), b = fmaxf(fmaxf(s[3], s[4]), s[5]);
    a = fmaxf(fmaxf(a, s[6]), s[7]);   b = fmaxf(fmaxf(b, s[8]), s[9]);
    a = fmaxf(fmaxf(a, s[10]), s[11]); b = fmaxf(fmaxf(b, s[12]), s[13]);
    a = fmaxf(fmaxf(a, s[14]), s[15]);
    return fmaxf(a, b);
}
DI float half_max(float m) {
    auto rr = __builtin_amdgcn_permlane32_swap(__float_as_uint(m), __float_as_uint(m), false, false);
    return fmaxf(__uint_as_float(rr[0]), __uint_as_float(rr[1]));
}

#define R_SA "192"
#define R_SB "208"
#define EX(X, i) "v_exp_f32 v[" X "+" #i "], v[" X "+" #i "]\n\t"
#define CV(X, j, a, b) "v_cvt_pk_bf16_f32 v[224+" #j "], v[" X "+" #a "], v[" X "+" #b "]\n\t"
#define HALF(X, Y, OX, OY, NY, QY, WA, RA, WB, RB, WC, RC)                                        \
    EX(X, 0) EX(X, 1) WA                                                                          \
    "v_mfma_f32_32x32x16_bf16 " OY ", %[v1], v[228:231], " OY "\n\t" RA                           \
    EX(X, 2) EX(X, 3) CV(X, 0, 0, 1) EX(X, 4) EX(X, 5) CV(X, 1, 2, 3) WB                          \
    "v_mfma_f32_32x32x16_bf16 v[" Y ":" Y "+15], %[kf], " QY ", " NY "\n\t" RB                    \
    EX(X, 6) EX(X, 7) CV(X, 2, 4, 5) EX(X, 8) EX(X, 9) CV(X, 3, 6, 7) EX(X, 10) EX(X, 11) WC      \
    "v_mfma_f32_32x32x16_bf16 " OX ", %[v0], v[224:227], " OX "\n\t" RC                           \
    CV(X, 4, 8, 9) EX(X, 12) EX(X, 13) CV(X, 5, 10, 11) EX(X, 14) EX(X, 15) CV(X, 6, 12, 13) CV(X, 7, 14, 15)
#define TILE(OFF_V1, OFF_KN, OFF_V0N, P0, P1)                                                     \
    HALF(R_SA, R_SB, "%[oa]", "%[ob]", "%[nb]", "%[qb]",                                          \
         "", "ds_read_b128 %[v1], %[va] offset:" OFF_V1 "\n\t" P0,                                \
         "", "ds_read_b128 %[kf], %[ka] offset:" OFF_KN "\n\t",                                   \
         "s_waitcnt lgkmcnt(2)\n\t", "")                                                          \
    HALF(R_SB, R_SA, "%[ob]", "%[oa]", "%[na]", "%[qa]",                                          \
         "s_waitcnt lgkmcnt(1)\n\t", P1,                                                          \
         "s_waitcnt lgkmcnt(0)\n\t", "",                                                          \
         "", "ds_read_b128 %[v0], %[va] offset:" OFF_V0N "\n\t")
#define KVDMA                                                                                     \
    "global_load_lds_dwordx4 %[lo], s[40:41]\n\t"                                                 \
    "s_add_u32 s40, s40, 0x2000\n\ts_addc_u32 s41, s41, 0\n\ts_add_u32 m0, m0, 0x2000\n\ts_nop 0\n\t"
#define ATTN_ASM(PY, PO)                                                                          \
    asm volatile(                                                                                 \
        "s_mov_b32 s44, m0\n\ts_mov_b32 m0, %[kd]\n\ts_mov_b64 s[40:41], %[kb]\n\ts_cmp_lg_u32 %[yg], 0\n\ts_cselect_b64 vcc, -1, 0\n\t"               \
        "ds_read_b128 %[kf], %[ka]\n\t"                                                           \
        "ds_read_b128 %[v1], %[va]\n\t"                                                           \
        "ds_read_b128 %[v0], %[va]\n\t"                                                           \
        "v_mov_b32 v228, 0\n\tv_mov_b32 v229, 0\n\tv_mov_b32 v230, 0\n\tv_mov_b32 v231, 0\n\t"    \
        "s_waitcnt lgkmcnt(0)\n\t"                                                                \
        "v_mfma_f32_32x32x16_bf16 v[192:207], %[kf], %[qa], %[na]\n\t"                            \
        "s_nop 7\n\ts_nop 7\n\t"                                                                  \
        "L_loop_%=:\n\t"                                                                          \
        "s_cmp_le_u32 %[cnt], 3\n\t"                                                              \
        "s_cbranch_scc1 L_sp_%=\n\t"                                                              \
        "L_go_%=:\n\t"                                                                            \
        TILE("512", "2048", "2048", "s_setprio 0\n\t", PY)                                        \
        TILE("2560", "4096", "4096", PO, "")                                                      \
        TILE("4608", "6144", "6144", "", "")                                                      \
        HALF(R_SA, R_SB, "%[oa]", "%[ob]", "%[nb]", "%[qb]",                                      \
             "", "ds_read_b128 %[v1], %[va] offset:6656\n\t",                                     \
             "", "ds_read_b128 %[kf], %[ka] offset:8192\n\tv_add_u32 %[va], %[va], %[vs]\n\t",    \
             "s_waitcnt lgkmcnt(2)\n\t", "v_add_u32 %[ka], 0x2000, %[ka]\n\t")                    \
        HALF(R_SB, R_SA, "%[ob]", "%[oa]", "%[na]", "%[qa]",                                      \
             "s_waitcnt lgkmcnt(1)\n\t", "s_add_u32 %[cnt], %[cnt], 1\n\t",                       \
             "s_waitcnt lgkmcnt(0)\n\t", "s_and_b32 s45, %[cnt], 15\n\t",                           \
             "", "ds_read_b128 %[v0], %[va]\n\t")                                                 \
        "s_cbranch_scc1 L_loop_%=\n\t"                                                            \
        "s_branch L_end_%=\n\t"                                                                   \
        "L_sp_%=:\n\t"                                                                            \
        "s_cmp_eq_u32 %[cnt], 3\n\t"                                                              \
        "s_cbranch_scc1 L_sy_%=\n\t"                                                              \
        KVDMA KVDMA KVDMA KVDMA                                                                   \
        "s_branch L_go_%=\n\t"                                                                    \
        "L_sy_%=:\n\t"                                                                            \
        "s_waitcnt vmcnt(0)\n\t"                                                                  \
        "s_barrier\n\t"                                                                           \
        "global_load_dword v232, %[pf], off\n\t"                                                  \
        "s_branch L_go_%=\n\t"                                                                    \
        "L_end_%=:\n\t"                                                                           \
        "s_nop 1\n\t"                                                                             \
        "v_mfma_f32_32x32x16_bf16 %[ob], %[v1], v[228:231], %[ob]\n\t"                            \
        "s_waitcnt vmcnt(0) lgkmcnt(0)\n\t"                                                       \
        "s_mov_b32 m0, s44\n\t"                                                                \
        "s_nop 7\n\ts_nop 7\n\t"                                                                  \
        : [oa] "+v"(oa), [ob] "+v"(ob), [ka] "+v"(ka), [va] "+v"(va), [cnt] "+s"(cnt),            \
          [kf] "=&v"(kf), [v0] "=&v"(v0), [v1] "=&v"(v1)                                          \
        : [qa] "v"(qa), [qb] "v"(qb), [na] "v"(na), [nb] "v"(nb), [vs] "v"(vstep), [yg] "s"(yg), [pf] "v"(pf), \
          [lo] "v"(lo), [kb] "s"(kb), [kd] "s"(kd)                                                \
        : "memory", "scc", "vcc", "s40", "s41", "s44", "s45",                                                                        \
          "v192", "v193", "v194", "v195", "v196", "v197", "v198", "v199", "v200", "v201", "v202", "v203", "v204", "v205", "v206", "v207", \
          "v208", "v209", "v210", "v211", "v212", "v213", "v214", "v215", "v216", "v217", "v218", "v219", "v220", "v221", "v222", "v223", \
          "v224", "v225", "v226", "v227", "v228", "v229", "v230", "v231", "v232")

DI void attn_main(bool young, bool first, unsigned ka, unsigned va, unsigned vstep, const bf16x8& qa, const bf16x8& qb, const f32x16& na,
                  const f32x16& nb, f32x16& oa, f32x16& ob, const char* pf, unsigned lo, const char* kb, unsigned kd) {
    bf16x8 kf, v0, v1;
    int cnt = __builtin_amdgcn_readfirstlane(first ? 0 : 16);
    const int yg = __builtin_amdgcn_readfirstlane(young ? 1 : 0);
    ATTN_ASM("s_cbranch_vccz L_py_%=\n\ts_setprio 1\n\tL_py_%=:\n\t", "s_setprio 1\n\t");
}

__global__ __launch_bounds__(512, 2) void attn_kernel(char* __restrict__ ws) {
    extern __shared__ __attribute__((aligned(16))) char smem[];
    const unsigned lds0 = (unsigned)(uintptr_t)smem;
    const int tid = threadIdx.x, lane = tid & 63;
    const int w = __builtin_amdgcn_readfirstlane(tid >> 6);
    const int r = lane & 31, h = lane >> 5;
    const int b = blockIdx.x, xcd = b & 7, slot = b >> 3;
    const int isB = (slot < 16);
    const int hd = xcd * 4 + ((slot & 15) >> 2);
    const int hh = isB ? 32 + hd : hd;
    const int kvh = isB ? hd : 32;
    const int tile = (slot & 3) * 16 + 2 * w;

    ST(1, 0);
    bf16x8 qa, qb;
    {
        const char* qsrc = ws + WS_Q + (size_t)hh * 65536 + tile * 1024 + lane * 16;
        asm volatile("global_load_dwordx4 %0, %2, off\n\tglobal_load_dwordx4 %1, %2, off offset:1024"
                     : "=&v"(qa), "=&v"(qb) : "v"(qsrc) : "memory");
    }
    const char* kvsrc = ws + WS_KV + (size_t)kvh * 131072 + w * 1024 + lane * 16;
#pragma unroll
    for (int i = 0; i < 4; ++i) glds16(kvsrc + i * 8192, lds0 + A_KV + i * 8192 + w * 1024);
    {
        const unsigned one = ((tid & 15) == 0 || (tid & 15) == 4) ? 0x3f803f80u : 0u;
        u32x4 c = {one, one, one, one};
        *(u32x4*)(smem + A_CONST + tid * 16) = c;
    }
    const unsigned kaddr = lds0 + A_KV + lane * 16;
    const unsigned vaddr = (r < 16) ? (lds0 + A_KV + 1024 + h * 256 + r * 16) : (lds0 + A_CONST + (r - 16) * 16);
    const unsigned vstep = (r < 16) ? 8192u : 0u;

    asm volatile("s_waitcnt vmcnt(0) lgkmcnt(0)\n\ts_barrier" : "+v"(qa), "+v"(qb) :: "memory");
    ST(1, 1);
    const char* kb = ws + WS_KV + (size_t)kvh * 131072 + 32768 + w * 1024;
    const unsigned kd = lds0 + A_KV + 32768 + w * 1024;
    float ga, gb;
    {
        const float* gsrc = (const float*)(ws + WS_G) + tile * 32 + r;
        asm volatile("global_load_dword %0, %2, off\n\tglobal_load_dword %1, %2, off offset:128" : "=&v"(ga), "=&v"(gb) : "v"(gsrc) : "memory");
    }
    const char* wpf = ws + WS_WO + (size_t)slot * 32768 + w * 4096 + r * 128;
    f32x16 zero;
#pragma unroll
    for (int i = 0; i < 16; ++i) zero[i] = 0.f;
    f32x16 na, nb, oa = zero, ob = zero;
    {
        const bf16x8 k0 = lds_b128(kaddr);
        const f32x16 sa = MFMA32(k0, qa, zero);
        const f32x16 sb = MFMA32(k0, qb, zero);
        const float ma = -half_max(max16(sa)) - REFOFF, mb = -half_max(max16(sb)) - REFOFF;
#pragma unroll
        for (int i = 0; i < 16; ++i) { na[i] = ma; nb[i] = mb; }
    }
    ST(1, 2);
    attn_main(w >= 4, true, kaddr, vaddr, vstep, qa, qb, na, nb, oa, ob, wpf, lane * 16, kb, kd);
    ST(1, 3);
    asm volatile("" : "+v"(ga), "+v"(gb));

    {
        float xa = fabsf(oa[0]), xb = fabsf(ob[0]), ta = oa[0], tb = ob[0];
#pragma unroll
        for (int i = 1; i < 8; ++i) { xa = fmaxf(xa, fabsf(oa[i])); xb = fmaxf(xb, fabsf(ob[i])); ta += oa[i]; tb += ob[i]; }
        const bool bad = !(oa[8] < 1.329228e36f) || !(ob[8] < 1.329228e36f) || !(fabsf(ta) <= 3.0e38f) || !(fabsf(tb) <= 3.0e38f) ||
                         !(xa > 7.7e-34f) || !(xb > 7.7e-34f);
        if (__any(bad)) {
            float ma = -INFINITY, mb = -INFINITY;
#pragma unroll 1
            for (int t = 0; t < 64; ++t) {
                const bf16x8 kf = lds_b128(kaddr + t * 2048);
                ma = fmaxf(ma, max16(MFMA32(kf, qa, zero)));
                mb = fmaxf(mb, max16(MFMA32(kf, qb, zero)));
            }
            ma = -half_max(ma); mb = -half_max(mb);
#pragma unroll
            for (int i = 0; i < 16; ++i) { na[i] = ma; nb[i] = mb; }
            oa = zero; ob = zero;
            attn_main(false, false, kaddr, vaddr, vstep, qa, qb, na, nb, oa, ob, wpf, lane * 16, kb, kd);
        }
    }
    const float sa = (isB ? 1.f - ga : ga) * __builtin_amdgcn_rcpf(oa[8]), sb = (isB ? 1.f - gb : gb) * __builtin_amdgcn_rcpf(ob[8]);
    char* odst = ws + WS_O + (size_t)tile * 65536 + hh * 1024 + lane * 16;
    gst128(odst, pack8(oa, 0, sa));
    gst128(odst + 65536, pack8(ob, 0, sb));
    ST(1, 4);
}

constexpr int O_LDS = 4 * 3 * 4096;

__global__ __launch_bounds__(256, 1) void out_kernel(const char* __restrict__ ws, unsigned short* __restrict__ out) {
    extern __shared__ __attribute__((aligned(16))) char smem[];
    const int tid = threadIdx.x, lane = tid & 63;
    const int w = __builtin_amdgcn_readfirstlane(tid >> 6);
    const int r = lane & 31, h = lane >> 5;
    const int b = blockIdx.x, xcd = b & 7, slot = b >> 3;
    const int mt = xcd * 4 + (slot & 3), nt = slot >> 2;

    ST(2, 0);
    const char* osrc = ws + WS_O + (size_t)(mt * 2) * 65536 + (w * 16) * 1024 + lane * 16;
    const char* wsrc = ws + WS_WO + (size_t)(w * 16) * 16384 + (nt * 2) * 1024 + lane * 16;
    bf16x8 of[16][2], wf[16][2];
    auto ldo = [&]() {
#pragma unroll
        for (int k = 0; k < 16; ++k) { of[k][0] = *(const bf16x8*)(osrc + k * 1024); of[k][1] = *(const bf16x8*)(osrc + 65536 + k * 1024); }
        SBAR();
    };
    auto ldw = [&]() {
#pragma unroll
        for (int k = 0; k < 16; ++k) { wf[k][0] = *(const bf16x8*)(wsrc + k * 16384); wf[k][1] = *(const bf16x8*)(wsrc + k * 16384 + 1024); }
        SBAR();
    };
    ldw(); ldo();
    SBAR();
    f32x16 acc[4];
#pragma unroll
    for (int i = 0; i < 16; ++i) { acc[0][i] = 0.f; acc[1][i] = 0.f; acc[2][i] = 0.f; acc[3][i] = 0.f; }
#pragma unroll
    for (int k = 0; k < 16; ++k)
#pragma unroll
        for (int q = 0; q < 4; ++q) acc[q] = MFMA32(wf[k][q >> 1], of[k][q & 1], acc[q]);
    ST(2, 1);
    f32x16 fin;
#pragma unroll
    for (int q = 0; q < 4; ++q) {
        if (q == w) { fin = acc[q]; }
        else {
            char* dst = smem + (q * 3 + ((w - q - 1) & 3)) * 4096 + lane * 16;
#pragma unroll
            for (int g = 0; g < 4; ++g) {
                f32x4 v = {acc[q][4 * g], acc[q][4 * g + 1], acc[q][4 * g + 2], acc[q][4 * g + 3]};
                *(f32x4*)(dst + g * 1024) = v;
            }
        }
    }
    __syncthreads();
    ST(2, 2);
#pragma unroll
    for (int p = 0; p < 3; ++p)
#pragma unroll
        for (int g = 0; g < 4; ++g) {
            const f32x4 v = *(const f32x4*)(smem + (w * 3 + p) * 4096 + g * 1024 + lane * 16);
#pragma unroll
            for (int i = 0; i < 4; ++i) fin[4 * g + i] += v[i];
        }
    unsigned short* drow = out + (size_t)(mt * 64 + (w & 1) * 32 + r) * 512 + nt * 64 + (w >> 1) * 32 + h * 8;
#pragma unroll
    for (int g = 0; g < 4; g += 2) {
        unsigned a0 = pk_bf16(fin[4 * g], fin[4 * g + 1]), a1 = pk_bf16(fin[4 * g + 2], fin[4 * g + 3]);
        unsigned b0 = pk_bf16(fin[4 * g + 4], fin[4 * g + 5]), b1 = pk_bf16(fin[4 * g + 6], fin[4 * g + 7]);
        auto r0 = __builtin_amdgcn_permlane32_swap(a0, b0, false, false);
        auto r1 = __builtin_amdgcn_permlane32_swap(a1, b1, false, false);
        u32x4 res = {r0[0], r1[0], r0[1], r1[1]};
        gst128(drow + g * 8, res);
    }
    ST(2, 3);
    asm volatile("pf_text_end:");
}

extern "C" void kernel_launch(void* const* d_in, const int* in_sizes, int n_in, void* d_out, int out_size, void* d_ws,
                              size_t ws_size, hipStream_t stream) {
    const unsigned short* x    = (const unsigned short*)d_in[0];
    const unsigned short* wq   = (const unsigned short*)d_in[1];
    const unsigned short* wk   = (const unsigned short*)d_in[2];
    const unsigned short* wv   = (const unsigned short*)d_in[3];
    const unsigned short* woA  = (const unsigned short*)d_in[4];
    const unsigned short* wqkv = (const unsigned short*)d_in[6];
    const unsigned short* woB  = (const unsigned short*)d_in[7];
    const unsigned short* gw   = (const unsigned short*)d_in[8];
    char* ws = (char*)d_ws;
    (void)hipFuncSetAttribute((const void*)proj_kernel, hipFuncAttributeMaxDynamicSharedMemorySize, P_LDS);
    (void)hipFuncSetAttribute((const void*)attn_kernel, hipFuncAttributeMaxDynamicSharedMemorySize, A_LDS);
    (void)hipFuncSetAttribute((const void*)out_kernel, hipFuncAttributeMaxDynamicSharedMemorySize, O_LDS);
    proj_kernel<<<256, 512, P_LDS, stream>>>(x, wq, wk, wv, wqkv, gw, woA, woB, ws);
    attn_kernel<<<256, 512, A_LDS, stream>>>(ws);
    out_kernel<<<256, 256, O_LDS, stream>>>(ws, (unsigned short*)d_out);
}
```

```cpp
#include <hip/hip_runtime.h>
#include <hip/hip_bf16.h>
#include <stdint.h>

typedef short    bf16x8 __attribute__((ext_vector_type(8)));
typedef short    s16x4  __attribute__((ext_vector_type(4)));
typedef float    f32x16 __attribute__((ext_vector_type(16)));
typedef float    f32x4  __attribute__((ext_vector_type(4)));
typedef float    f32x2  __attribute__((ext_vector_type(2)));
typedef unsigned u32x4  __attribute__((ext_vector_type(4)));
typedef unsigned u32x16 __attribute__((ext_vector_type(16)));
typedef __bf16   bf16x2v __attribute__((ext_vector_type(2)));

#define DI __device__ __forceinline__
#define MFMA32(a, b, c) __builtin_amdgcn_mfma_f32_32x32x16_bf16((a), (b), (c), 0, 0, 0)
#define LDS_AS __attribute__((address_space(3)))

constexpr size_t WS_Q  = 0;
constexpr size_t WS_KV = WS_Q + (size_t)64 * 65536;
constexpr size_t WS_O  = WS_KV + (size_t)34 * 131072;
constexpr size_t WS_WO = WS_O + (size_t)64 * 65536;
constexpr size_t WS_G  = WS_WO + (size_t)1024 * 1024;
constexpr size_t WS_C  = WS_G + 8192;

#define ST(k, i)
#define CODEPF 1
#define REFOFF 96.f
#define O_ORD 3
#define POLLSLEEP 2
constexpr float QSCALE = 0.25f * 1.4426950408889634f;

DI unsigned pk_bf16(float lo, float hi) {
    f32x2 v = {lo, hi};
    bf16x2v r = __builtin_convertvector(v, bf16x2v);
    return __builtin_bit_cast(unsigned, r);
}
DI float bf2f(unsigned short u) { return __uint_as_float(((unsigned)u) << 16); }

DI void glds16(const void* g, unsigned lds_base) {
    unsigned sv;
    asm volatile("s_mov_b32 %0, m0\n\ts_mov_b32 m0, %2\n\ts_nop 0\n\tglobal_load_lds_dwordx4 %1, off\n\ts_mov_b32 m0, %0"
                 : "=&s"(sv) : "v"(g), "s"(lds_base) : "memory");
}
#define WAIT_BAR(N) asm volatile("s_waitcnt vmcnt(" #N ") lgkmcnt(0)\n\ts_barrier" ::: "memory")

#define STPOL "sc1"
DI void gst128(void* p, u32x4 v) { asm volatile("global_store_dwordx4 %0, %1, off " STPOL "\n\ts_nop 1" :: "v"(p), "v"(v) : "memory"); }
DI bf16x8 lds_b128(unsigned addr) { return *(const LDS_AS bf16x8*)(uintptr_t)addr; }
DI s16x4 lds_tr(unsigned addr) {
    return __builtin_bit_cast(s16x4, __builtin_amdgcn_ds_read_tr16_b64_v4i16((LDS_AS s16x4*)(uintptr_t)addr));
}
DI bf16x8 cat8(s16x4 a, s16x4 b) { return __builtin_shufflevector(a, b, 0, 1, 2, 3, 4, 5, 6, 7); }

DI u32x4 pack8(const f32x16& a, int base, float sc) {
    u32x4 r;
    r[0] = pk_bf16(a[base + 0] * sc, a[base + 1] * sc);
    r[1] = pk_bf16(a[base + 2] * sc, a[base + 3] * sc);
    r[2] = pk_bf16(a[base + 4] * sc, a[base + 5] * sc);
    r[3] = pk_bf16(a[base + 6] * sc, a[base + 7] * sc);
    return r;
}

constexpr int P_XS = 0, P_WS = 16384, P_STAGE = 32768, P_WX = 4 * P_STAGE, P_GT = P_WX + 16384, P_WO = P_GT + 9216, P_CNT = P_WO + 4096, P_LDS = P_CNT + 16;
#define SBAR() __builtin_amdgcn_sched_barrier(0)

struct PSrc { const unsigned short *xs0, *xs1, *wsr; int ldw; };
DI PSrc proj_src(const unsigned short* x, const unsigned short* wsrc, int ldw, int mt, int nt, int l, int lane) {
    PSrc s;
    const int row = 32 * l + (lane >> 3);
    s.xs0 = x + (size_t)(mt * 128 + row) * 512 + ((lane & 7) ^ ((row >> 1) & 7)) * 8;
    s.xs1 = x + (size_t)(mt * 128 + row + 8) * 512 + ((lane & 7) ^ (((row + 8) >> 1) & 7)) * 8;
    const int krow = 16 * l + (lane >> 4);
    s.wsr = wsrc + (size_t)krow * ldw + ((lane & 15) ^ (((lane >> 4) & 3) << 2)) * 8;
    s.ldw = ldw;
    return s;
}
DI void proj_issue(const PSrc& s, int c, int l, unsigned lds0) {
    const unsigned st = lds0 + (c & 3) * P_STAGE + l * 4096;
    glds16(s.xs0 + c * 64, st + P_XS);
    glds16(s.xs1 + c * 64, st + P_XS + 1024);
    glds16(s.xs0 + c * 64 + 16 * 512, st + P_XS + 2048);
    glds16(s.xs1 + c * 64 + 16 * 512, st + P_XS + 3072);
#pragma unroll
    for (int i = 0; i < 4; ++i) glds16(s.wsr + (size_t)(c * 64 + 4 * i) * s.ldw, st + P_WS + i * 1024);
}
DI float wave_sum63(float v) {
#define DPP_ADD(ctrl, rm) v += __int_as_float(__builtin_amdgcn_update_dpp(0, __float_as_int(v), ctrl, rm, 0xf, false))
    DPP_ADD(0x111, 0xf); DPP_ADD(0x112, 0xf); DPP_ADD(0x114, 0xf); DPP_ADD(0x118, 0xf);
    DPP_ADD(0x142, 0xa); DPP_ADD(0x143, 0xc);
#undef DPP_ADD
    return v;
}
DI void lds_spin(const char* smem, int off, unsigned target) {
    const volatile LDS_AS unsigned* p = (const volatile LDS_AS unsigned*)(uintptr_t)((unsigned)(uintptr_t)smem + off);
    while ((unsigned)__builtin_amdgcn_readfirstlane(*p) < target) __builtin_amdgcn_s_sleep(1);
    asm volatile("" ::: "memory");
}

template <bool ISV>
DI void proj_compute(const PSrc& src, const unsigned short* __restrict__ x, const unsigned short* __restrict__ wx,
                     const unsigned short* __restrict__ gw, const unsigned short* __restrict__ wo, char* __restrict__ ws,
                     int mt, int nt, int w, unsigned lds0, char* smem) {
    const int lane = threadIdx.x & 63;
    const int wm = w >> 1, wn = w & 1;
    const int r = lane & 31, h = lane >> 5;
    unsigned xo[4][2];
#pragma unroll
    for (int tt = 0; tt < 2; ++tt) {
        const int row = wm * 64 + tt * 32 + r;
#pragma unroll
        for (int ks = 0; ks < 4; ++ks) xo[ks][tt] = lds0 + P_XS + row * 128 + (((2 * ks + h) ^ ((row >> 1) & 7)) << 4);
    }
    const int q4 = (lane & 15) >> 2, p4 = lane & 3, g1 = (lane >> 4) & 1;
    unsigned wa[2];
#pragma unroll
    for (int ct = 0; ct < 2; ++ct)
        wa[ct] = lds0 + P_WS + (8 * h + q4) * 256 + ((4 * ((2 * wn + ct) ^ q4) + 2 * g1 + (p4 >> 1)) << 4) + (p4 & 1) * 8;
    f32x16 acc[2][2];
#pragma unroll
    for (int i = 0; i < 16; ++i) { acc[0][0][i] = 0.f; acc[0][1][i] = 0.f; acc[1][0][i] = 0.f; acc[1][1][i] = 0.f; }
    bf16x8 fx[2][4][2], fw[2][4][2];
    auto mm = [&](int bf, int ks) {
#pragma unroll
        for (int ct = 0; ct < 2; ++ct)
#pragma unroll
            for (int tt = 0; tt < 2; ++tt)
                acc[ct][tt] = ISV ? MFMA32(fx[bf][ks][tt], fw[bf][ks][ct], acc[ct][tt]) : MFMA32(fw[bf][ks][ct], fx[bf][ks][tt], acc[ct][tt]);
    };
    auto rd = [&](int bf, int ks, unsigned so) {
#pragma unroll
        for (int tt = 0; tt < 2; ++tt) fx[bf][ks][tt] = lds_b128(xo[ks][tt] + so);
#pragma unroll
        for (int ct = 0; ct < 2; ++ct) fw[bf][ks][ct] = cat8(lds_tr(wa[ct] + so + ks * 4096), lds_tr(wa[ct] + so + ks * 4096 + 1024));
    };
    auto step = [&](int bf, int c) {
        __builtin_amdgcn_s_waitcnt(0xC07F);
        asm volatile("s_barrier" ::: "memory");
        SBAR();
        const unsigned so = (c & 3) * P_STAGE;
#pragma unroll
        for (int ks = 0; ks < 4; ++ks) { rd(bf, ks, so); mm(bf ^ 1, ks); }
#pragma unroll
        for (int i = 0; i < 12; ++i) {
            __builtin_amdgcn_sched_group_barrier(0x008, 1, 0);
            __builtin_amdgcn_sched_group_barrier(0x002, 1, 0);
            __builtin_amdgcn_sched_group_barrier(0x100, 2, 0);
        }
        __builtin_amdgcn_sched_group_barrier(0x008, 4, 0);
        SBAR();
    };
    asm volatile("" : "+v"(xo[0][0]), "+v"(xo[0][1]), "+v"(xo[1][0]), "+v"(xo[1][1]), "+v"(xo[2][0]), "+v"(xo[2][1]),
                      "+v"(xo[3][0]), "+v"(xo[3][1]), "+v"(wa[0]), "+v"(wa[1]),
                      "+v"(acc[0][0]), "+v"(acc[0][1]), "+v"(acc[1][0]), "+v"(acc[1][1]));
    asm volatile("s_waitcnt vmcnt(0)" ::: "memory");
    if (lane == 0) atomicAdd((unsigned*)(smem + P_CNT), 1u);
    lds_spin(smem, P_CNT, 4u);
    ST(0, 1);
    rd(0, 0, 0); rd(0, 1, 0); rd(0, 2, 0); rd(0, 3, 0);
    SBAR();
    {
        if (nt >= 8) {
            const unsigned short* es = wx + (size_t)((lane >> 5) * 64 + 16 * w) * 16 + (lane & 31) * 8;
#pragma unroll
            for (int i = 0; i < 4; ++i) glds16(es + i * 2048, lds0 + P_WX + w * 4096 + i * 1024);
        }
        const unsigned short* px = x + (size_t)(mt * 128 + nt * 8 + 2 * w) * 512 + lane * 8;
        glds16(px, lds0 + P_GT + w * 2048);
        glds16(px + 512, lds0 + P_GT + w * 2048 + 1024);
        glds16(gw + lane * 8, lds0 + P_GT + 8192);
        glds16(wo + (size_t)(4 * w + (lane >> 4)) * 512 + ((lane & 15) ^ (((lane >> 4) & 3) << 2)) * 8, lds0 + P_WO + w * 1024);
    }

#pragma unroll 1
    for (int c = 1; c < 7; c += 2) { step(1, c); step(0, c + 1); }
    asm volatile("s_waitcnt vmcnt(0)" ::: "memory");
    step(1, 7);
    ST(0, 2);
#pragma unroll
    for (int ct = 0; ct < 2; ++ct)
#pragma unroll
        for (int tt = 0; tt < 2; ++tt)
#pragma unroll
            for (int ks = 0; ks < 4; ++ks)
                acc[ct][tt] = ISV ? MFMA32(fx[1][ks][tt], fw[1][ks][ct], acc[ct][tt]) : MFMA32(fw[1][ks][ct], fx[1][ks][tt], acc[ct][tt]);
    ST(0, 3);
#pragma unroll
    for (int ct = 0; ct < 2; ++ct)
#pragma unroll
        for (int tt = 0; tt < 2; ++tt) {
            const int hd0 = (nt & 3) * 8 + wn * 4 + ct * 2;
            const int tile = mt * 4 + wm * 2 + tt;
            if (ISV) {
                char* dst = ws + WS_KV + (size_t)(hd0 + (r >> 4)) * 131072 + tile * 2048 + 1024 + h * 256 + (r & 15) * 16;
                gst128(dst, pack8(acc[ct][tt], 0, 1.f));
                gst128(dst + 512, pack8(acc[ct][tt], 8, 1.f));
            } else {
                const bool isq = nt < 8;
                const float sc = isq ? QSCALE : 1.f;
                const size_t hs = isq ? 65536 : 131072;
                char* dst = isq ? (ws + WS_Q + (size_t)((nt >> 2) * 32 + hd0) * 65536 + tile * 1024 + lane * 16)
                                : (ws + WS_KV + (size_t)hd0 * 131072 + tile * 2048 + lane * 16);
                gst128(dst, pack8(acc[ct][tt], 0, sc));
                gst128(dst + hs, pack8(acc[ct][tt], 8, sc));
            }
        }
}

template <int XM>
DI void proj_loader(const PSrc& src, const unsigned short* __restrict__ x, const unsigned short* __restrict__ wk,
                    const unsigned short* __restrict__ wv, const unsigned short* __restrict__ gw, char* __restrict__ ws,
                    int mt, int nt, int l, unsigned lds0, char* smem) {
    const int lane = threadIdx.x & 63;
    const int r = lane & 31, h = lane >> 5;
    constexpr bool isx = XM != 0, isv = XM == 2;
    const int xtt = nt & 3;
    proj_issue(src, 2, l, lds0);
    const int q4 = (lane & 15) >> 2, p4 = lane & 3;
    const unsigned ea = lds0 + P_WX + l * 4096 + (8 * h + q4) * 32 + p4 * 8;
    const int xrow = xtt * 32 + r;
    const unsigned exa = lds0 + P_XS + xrow * 128 + (((2 * l + h) ^ ((xrow >> 1) & 7)) << 4);
    const int gtok = mt * 128 + nt * 8 + 2 * l;
    u32x4 gx0, gx1, gv;
    bf16x8 xe[8];
    if (isx) { lds_spin(smem, P_CNT, 4u); xe[0] = lds_b128(exa); }
#pragma unroll
    for (int c = 1; c < 8; ++c) {
        if (c < 7) WAIT_BAR(8); else WAIT_BAR(0);
        if (isx) xe[c] = lds_b128(exa + (c & 3) * P_STAGE);
        if (c < 6) proj_issue(src, c + 2, l, lds0);
    }
    gx0 = *(const u32x4*)(smem + P_GT + l * 2048 + lane * 16);
    gx1 = *(const u32x4*)(smem + P_GT + l * 2048 + 1024 + lane * 16);
    gv  = *(const u32x4*)(smem + P_GT + 8192 + lane * 16);
    {
        const int g1 = (lane >> 4) & 1;
        const unsigned woa = lds0 + P_WO + (4 * h + q4) * 256 + ((4 * (l ^ q4) + 2 * g1 + (p4 >> 1)) << 4) + (p4 & 1) * 8;
        const bf16x8 f = cat8(lds_tr(woa), lds_tr(woa + 2048));
        gst128(ws + WS_WO + (size_t)(blockIdx.x * 4 + l) * 1024 + lane * 16, __builtin_bit_cast(u32x4, f));
    }
    {
        float s0 = 0.f, s1 = 0.f;
#pragma unroll
        for (int i = 0; i < 4; ++i) {
            const float wl = __uint_as_float(gv[i] << 16), wh = __uint_as_float(gv[i] & 0xffff0000u);
            s0 += __uint_as_float(gx0[i] << 16) * wl + __uint_as_float(gx0[i] & 0xffff0000u) * wh;
            s1 += __uint_as_float(gx1[i] << 16) * wl + __uint_as_float(gx1[i] & 0xffff0000u) * wh;
        }
        s0 = wave_sum63(s0); s1 = wave_sum63(s1);
        if (lane == 63) {
            f32x2 g = {__builtin_amdgcn_rcpf(1.f + __expf(-s0)), __builtin_amdgcn_rcpf(1.f + __expf(-s1))};
            *(f32x2*)((float*)(ws + WS_G) + gtok) = g;
        }
    }
    f32x16 acce;
#pragma unroll
    for (int i = 0; i < 16; ++i) acce[i] = 0.f;
    if (isx) {
        __builtin_amdgcn_s_setprio(3);
#pragma unroll
        for (int c = 0; c < 8; ++c) {
            const bf16x8 we = cat8(lds_tr(ea + c * 512), lds_tr(ea + c * 512 + 128));
            acce = isv ? MFMA32(xe[c], we, acce) : MFMA32(we, xe[c], acce);
        }
    }
    if (isx) {
        f32x4* red = (f32x4*)smem;
#pragma unroll
        for (int g = 0; g < 4; ++g) {
            f32x4 v = {acce[4 * g], acce[4 * g + 1], acce[4 * g + 2], acce[4 * g + 3]};
            red[(l * 4 + g) * 64 + lane] = v;
        }
        unsigned old = 0;
        asm volatile("s_waitcnt lgkmcnt(0)" ::: "memory");
        if (lane == 0) old = atomicAdd((unsigned*)(smem + P_CNT), 1u);
        old = __builtin_amdgcn_readfirstlane(old);
        if (old == 7) {
            asm volatile("" ::: "memory");
#pragma unroll
            for (int g = 0; g < 4; ++g) {
                const f32x4 v = red[g * 64 + lane] + red[(4 + g) * 64 + lane] + red[(8 + g) * 64 + lane] + red[(12 + g) * 64 + lane];
#pragma unroll
                for (int i = 0; i < 4; ++i) acce[4 * g + i] = v[i];
            }
            char* base = ws + WS_KV + (size_t)32 * 131072 + (mt * 4 + xtt) * 2048;
            if (isv) {
                if (r < 16) {
                    gst128(base + 1024 + h * 256 + r * 16, pack8(acce, 0, 1.f));
                    gst128(base + 1536 + h * 256 + r * 16, pack8(acce, 8, 1.f));
                }
            } else {
                gst128(base + lane * 16, pack8(acce, 0, 1.f));
            }
        }
    }
}

__global__ __launch_bounds__(512, 2) void proj_kernel(const unsigned short* __restrict__ x, const unsigned short* __restrict__ wq,
                                                      const unsigned short* __restrict__ wk, const unsigned short* __restrict__ wv,
                                                      const unsigned short* __restrict__ wqkv, const unsigned short* __restrict__ gw,
                                                      const unsigned short* __restrict__ woA, const unsigned short* __restrict__ woB,
                                                      char* __restrict__ ws) {
    extern __shared__ __attribute__((aligned(16))) char smem[];
    const unsigned lds0 = (unsigned)(uintptr_t)smem;
    const int w = __builtin_amdgcn_readfirstlane(threadIdx.x >> 6);
    const int b = blockIdx.x, xcd = b & 7, slot = b >> 3;
    const int mt = (xcd & 3) * 4 + (slot & 3), nt = (xcd >> 2) * 8 + (slot >> 2);
    ST(0, 0);
    unsigned cpf = 0;
    if (w == 7) {
        unsigned long long pc; int len;
        asm volatile("s_getpc_b64 %0\n\tpf_text_begin:\n\ts_mov_b32 %1, pf_text_end-pf_text_begin" : "=s"(pc), "=s"(len));
        int off = ((slot & (8 * CODEPF - 1)) * 32 + (int)(threadIdx.x & 31)) * 128;
        off = off < len ? off : 0;
        asm volatile("global_load_dword %0, %1, %2" : "=&v"(cpf) : "v"(off), "s"(pc) : "memory");
    }
    if (threadIdx.x == 0) *(unsigned*)(smem + P_CNT) = 0u;
    const unsigned short* wsrc = (nt < 4) ? (wq + nt * 128) : (wqkv + (nt - 4) * 128);
    const PSrc src = proj_src(x, wsrc, (nt < 4) ? 512 : 1536, mt, nt, w & 3, threadIdx.x & 63);
    const unsigned short* wo = ((b < 128) ? woA : woB) + (size_t)((b >> 2) & 31) * 16 * 512 + (b & 3) * 128;
    proj_issue(src, w >> 2, w & 3, lds0);
    asm volatile("s_waitcnt lgkmcnt(0)\n\ts_barrier" ::: "memory");
    if (w >= 4) {
        if (nt >= 12)     proj_loader<2>(src, x, wk, wv, gw, ws, mt, nt, w - 4, lds0, smem);
        else if (nt >= 8) proj_loader<1>(src, x, wk, wv, gw, ws, mt, nt, w - 4, lds0, smem);
        else              proj_loader<0>(src, x, wk, wv, gw, ws, mt, nt, w - 4, lds0, smem);
        asm volatile("" :: "v"(cpf));
        if (b == 0 && w == 4 && (threadIdx.x & 63) < 4) *(unsigned*)(ws + WS_C + (threadIdx.x & 63) * 256) = 0u;
        ST(0, 1); ST(0, 2); ST(0, 3);
    } else if (nt >= 12) proj_compute<true>(src, x, wv, gw, wo, ws, mt, nt, w, lds0, smem);
    else                 proj_compute<false>(src, x, wk, gw, wo, ws, mt, nt, w, lds0, smem);
    ST(0, 4);
}

constexpr int A_KV = 0, A_CONST = 131072, A_LDS = A_CONST + 8192;

DI float max16(const f32x16& s) {
    float a = fmaxf(fmaxf(s[0], s[1]), s[2]), b = fmaxf(fmaxf(s[3], s[4]), s[5]);
    a = fmaxf(fmaxf(a, s[6]), s[7]);   b = fmaxf(fmaxf(b, s[8]), s[9]);
    a = fmaxf(fmaxf(a, s[10]), s[11]); b = fmaxf(fmaxf(b, s[12]), s[13]);
    a = fmaxf(fmaxf(a, s[14]), s[15]);
    return fmaxf(a, b);
}
DI float half_max(float m) {
    auto rr = __builtin_amdgcn_permlane32_swap(__float_as_uint(m), __float_as_uint(m), false, false);
    return fmaxf(__uint_as_float(rr[0]), __uint_as_float(rr[1]));
}

#define R_SA "192"
#define R_SB "208"
#define EX(X, i) "v_exp_f32 v[" X "+" #i "], v[" X "+" #i "]\n\t"
#define CV(X, j, a, b) "v_cvt_pk_bf16_f32 v[224+" #j "], v[" X "+" #a "], v[" X "+" #b "]\n\t"
#define HALF(X, Y, OX, OY, NY, QY, WA, RA, WB, RB, WC, RC)                                        \
    EX(X, 0) EX(X, 1) WA                                                                          \
    "v_mfma_f32_32x32x16_bf16 " OY ", %[v1], v[228:231], " OY "\n\t" RA                           \
    EX(X, 2) EX(X, 3) CV(X, 0, 0, 1) EX(X, 4) EX(X, 5) CV(X, 1, 2, 3) WB                          \
    "v_mfma_f32_32x32x16_bf16 v[" Y ":" Y "+15], %[kf], " QY ", " NY "\n\t" RB                    \
    EX(X, 6) EX(X, 7) CV(X, 2, 4, 5) EX(X, 8) EX(X, 9) CV(X, 3, 6, 7) EX(X, 10) EX(X, 11) WC      \
    "v_mfma_f32_32x32x16_bf16 " OX ", %[v0], v[224:227], " OX "\n\t" RC                           \
    CV(X, 4, 8, 9) EX(X, 12) EX(X, 13) CV(X, 5, 10, 11) EX(X, 14) EX(X, 15) CV(X, 6, 12, 13) CV(X, 7, 14, 15)
#define TILE(OFF_V1, OFF_KN, OFF_V0N, P0, P1)                                                     \
    HALF(R_SA, R_SB, "%[oa]", "%[ob]", "%[nb]", "%[qb]",                                          \
         "", "ds_read_b128 %[v1], %[va] offset:" OFF_V1 "\n\t" P0,                                \
         "", "ds_read_b128 %[kf], %[ka] offset:" OFF_KN "\n\t",                                   \
         "s_waitcnt lgkmcnt(2)\n\t", "")                                                          \
    HALF(R_SB, R_SA, "%[ob]", "%[oa]", "%[na]", "%[qa]",                                          \
         "s_waitcnt lgkmcnt(1)\n\t", P1,                                                          \
         "s_waitcnt lgkmcnt(0)\n\t", "",                                                          \
         "", "ds_read_b128 %[v0], %[va] offset:" OFF_V0N "\n\t")
#define KVDMA                                                                                     \
    "global_load_lds_dwordx4 %[lo], s[40:41]\n\t"                                                 \
    "s_add_u32 s40, s40, 0x2000\n\ts_addc_u32 s41, s41, 0\n\ts_add_u32 m0, m0, 0x2000\n\ts_nop 0\n\t"
#define WOLD(R)                                                                                   \
    "global_load_dwordx4 v[" #R ":" #R "+3], %[lo], s[42:43]\n\t"                                 \
    "global_load_dwordx4 v[" #R "+4:" #R "+7], %[lo], s[42:43] offset:1024\n\t"                   \
    "s_add_u32 s42, s42, 0x4000\n\ts_addc_u32 s43, s43, 0\n\t"
#define ATTN_ASM(PY, PO)                                                                          \
    asm volatile(                                                                                 \
        "s_mov_b32 s44, m0\n\ts_mov_b32 m0, %[kd]\n\ts_mov_b64 s[40:41], %[kb]\n\ts_mov_b64 s[42:43], %[wb]\n\ts_cmp_lg_u32 %[yg], 0\n\ts_cselect_b64 vcc, -1, 0\n\t"               \
        "ds_read_b128 %[kf], %[ka]\n\t"                                                           \
        "ds_read_b128 %[v1], %[va]\n\t"                                                           \
        "ds_read_b128 %[v0], %[va]\n\t"                                                           \
        "v_mov_b32 v228, 0\n\tv_mov_b32 v229, 0\n\tv_mov_b32 v230, 0\n\tv_mov_b32 v231, 0\n\t"    \
        "s_waitcnt lgkmcnt(0)\n\t"                                                                \
        "v_mfma_f32_32x32x16_bf16 v[192:207], %[kf], %[qa], %[na]\n\t"                            \
        "s_nop 7\n\ts_nop 7\n\t"                                                                  \
        "L_loop_%=:\n\t"                                                                          \
        "s_bitcmp1_b32 %[mk], %[cnt]\n\t"                                                         \
        "s_cbranch_scc1 L_sp_%=\n\t"                                                              \
        "L_go_%=:\n\t"                                                                            \
        TILE("512", "2048", "2048", "s_setprio 0\n\t", PY)                                        \
        TILE("2560", "4096", "4096", PO, "")                                                      \
        TILE("4608", "6144", "6144", "", "")                                                      \
        HALF(R_SA, R_SB, "%[oa]", "%[ob]", "%[nb]", "%[qb]",                                      \
             "", "ds_read_b128 %[v1], %[va] offset:6656\n\t",                                     \
             "", "ds_read_b128 %[kf], %[ka] offset:8192\n\tv_add_u32 %[va], %[va], %[vs]\n\t",    \
             "s_waitcnt lgkmcnt(2)\n\t", "v_add_u32 %[ka], 0x2000, %[ka]\n\t")                    \
        HALF(R_SB, R_SA, "%[ob]", "%[oa]", "%[na]", "%[qa]",                                      \
             "s_waitcnt lgkmcnt(1)\n\t", "s_add_u32 %[cnt], %[cnt], 1\n\t",                       \
             "s_waitcnt lgkmcnt(0)\n\t", "s_and_b32 s45, %[cnt], 15\n\t",                           \
             "", "ds_read_b128 %[v0], %[va]\n\t")                                                 \
        "s_cbranch_scc1 L_loop_%=\n\t"                                                            \
        "s_branch L_end_%=\n\t"                                                                   \
        "L_sp_%=:\n\t"                                                                            \
        "s_cmp_eq_u32 %[cnt], 3\n\t"                                                              \
        "s_cbranch_scc1 L_sy_%=\n\t"                                                              \
        "s_cmp_lg_u32 %[cnt], 13\n\t"                                                             \
        "s_cbranch_scc1 L_kv_%=\n\t"                                                              \
        "global_load_dword %[cv], %[pf], off sc1\n\t"                                             \
        "s_branch L_go_%=\n\t"                                                                    \
        "L_kv_%=:\n\t"                                                                            \
        "s_cmp_lt_u32 %[cnt], 3\n\t"                                                              \
        "s_cbranch_scc1 L_k2_%=\n\t"                                                              \
        WOLD(128) WOLD(136) WOLD(144) WOLD(152) WOLD(160) WOLD(168) WOLD(176) WOLD(184)           \
        "s_branch L_go_%=\n\t"                                                                    \
        "L_k2_%=:\n\t"                                                                            \
        KVDMA KVDMA KVDMA KVDMA                                                                   \
        "s_branch L_go_%=\n\t"                                                                    \
        "L_sy_%=:\n\t"                                                                            \
        "s_waitcnt vmcnt(0)\n\t"                                                                  \
        "s_barrier\n\t"                                                                           \
        "s_cbranch_execz L_na_%=\n\t"                                                             \
        "s_cmp_lg_u32 %[w0], 0\n\t"                                                               \
        "s_cbranch_scc1 L_na_%=\n\t"                                                              \
        "s_mov_b64 exec, 1\n\t"                                                                   \
        "v_mov_b32 v232, 1\n\t"                                                                   \
        "global_atomic_add %[pf], v232, off sc1\n\t"                                              \
        "s_mov_b64 exec, -1\n\t"                                                                  \
        "L_na_%=:\n\t"                                                                            \
        "s_branch L_go_%=\n\t"                                                                    \
        "L_end_%=:\n\t"                                                                           \
        "s_nop 1\n\t"                                                                             \
        "v_mfma_f32_32x32x16_bf16 %[ob], %[v1], v[228:231], %[ob]\n\t"                            \
        "s_waitcnt vmcnt(0) lgkmcnt(0)\n\t"                                                       \
        "s_mov_b32 m0, s44\n\t"                                                                \
        "s_nop 7\n\ts_nop 7\n\t"                                                                  \
        : [oa] "+v"(oa), [ob] "+v"(ob), [ka] "+v"(ka), [va] "+v"(va), [cnt] "+s"(cnt), [cv] "+v"(cv), \
          [kf] "=&v"(kf), [v0] "=&v"(v0), [v1] "=&v"(v1),                                         \
          "+{v[128:143]}"(w0), "+{v[144:159]}"(w1), "+{v[160:175]}"(w2), "+{v[176:191]}"(w3)      \
        : [qa] "v"(qa), [qb] "v"(qb), [na] "v"(na), [nb] "v"(nb), [vs] "v"(vstep), [yg] "s"(yg), [pf] "v"(pf), \
          [lo] "v"(lo), [kb] "s"(kb), [kd] "s"(kd), [wb] "s"(wb), [w0] "s"(wi), [mk] "s"(mk)                                                \
        : "memory", "scc", "vcc", "s40", "s41", "s42", "s43", "s44", "s45",                                                                        \
          "v192", "v193", "v194", "v195", "v196", "v197", "v198", "v199", "v200", "v201", "v202", "v203", "v204", "v205", "v206", "v207", \
          "v208", "v209", "v210", "v211", "v212", "v213", "v214", "v215", "v216", "v217", "v218", "v219", "v220", "v221", "v222", "v223", \
          "v224", "v225", "v226", "v227", "v228", "v229", "v230", "v231", "v232")

DI void attn_main(bool young, bool first, unsigned ka, unsigned va, unsigned vstep, const bf16x8& qa, const bf16x8& qb, const f32x16& na,
                  const f32x16& nb, f32x16& oa, f32x16& ob, const char* pf, unsigned lo, const char* kb, unsigned kd,
                  const char* wb, int wi, u32x16& w0, u32x16& w1, u32x16& w2, u32x16& w3, unsigned& cv) {
    bf16x8 kf, v0, v1;
    int cnt = __builtin_amdgcn_readfirstlane(first ? 0 : 16);
    const int yg = __builtin_amdgcn_readfirstlane(young ? 1 : 0);
    const int mk = __builtin_amdgcn_readfirstlane(young ? 0x202f : 0x208f);
    ATTN_ASM("s_cbranch_vccz L_py_%=\n\ts_setprio 1\n\tL_py_%=:\n\t", "s_setprio 1\n\t");
}

__global__ __launch_bounds__(512, 2) void attn_kernel(char* __restrict__ ws, unsigned short* __restrict__ out) {
    extern __shared__ __attribute__((aligned(16))) char smem[];
    const unsigned lds0 = (unsigned)(uintptr_t)smem;
    const int tid = threadIdx.x, lane = tid & 63;
    const int w = __builtin_amdgcn_readfirstlane(tid >> 6);
    const int r = lane & 31, h = lane >> 5;
    const int b = blockIdx.x, xcd = b & 7, slot = b >> 3;
    const int isB = (slot < 16);
    const int hd = xcd * 4 + ((slot & 15) >> 2);
    const int hh = isB ? 32 + hd : hd;
    const int kvh = isB ? hd : 32;
    const int tile = (slot & 3) * 16 + 2 * w;

    ST(1, 0);
    bf16x8 qa, qb;
    {
        const char* qsrc = ws + WS_Q + (size_t)hh * 65536 + tile * 1024 + lane * 16;
        asm volatile("global_load_dwordx4 %0, %2, off\n\tglobal_load_dwordx4 %1, %2, off offset:1024"
                     : "=&v"(qa), "=&v"(qb) : "v"(qsrc) : "memory");
    }
    const char* kvsrc = ws + WS_KV + (size_t)kvh * 131072 + w * 1024 + lane * 16;
#pragma unroll
    for (int i = 0; i < 4; ++i) glds16(kvsrc + i * 8192, lds0 + A_KV + i * 8192 + w * 1024);
    {
        const unsigned one = ((tid & 15) == 0 || (tid & 15) == 4) ? 0x3f803f80u : 0u;
        u32x4 c = {one, one, one, one};
        *(u32x4*)(smem + A_CONST + tid * 16) = c;
    }
    const unsigned kaddr = lds0 + A_KV + lane * 16;
    const unsigned vaddr = (r < 16) ? (lds0 + A_KV + 1024 + h * 256 + r * 16) : (lds0 + A_CONST + (r - 16) * 16);
    const unsigned vstep = (r < 16) ? 8192u : 0u;

    asm volatile("s_waitcnt vmcnt(0) lgkmcnt(0)\n\ts_barrier" : "+v"(qa), "+v"(qb) :: "memory");
    ST(1, 1);
    const char* kb = ws + WS_KV + (size_t)kvh * 131072 + 32768 + w * 1024;
    const unsigned kd = lds0 + A_KV + 32768 + w * 1024;
    float ga, gb;
    {
        const float* gsrc = (const float*)(ws + WS_G) + tile * 32 + r;
        asm volatile("global_load_dword %0, %2, off\n\tglobal_load_dword %1, %2, off offset:128" : "=&v"(ga), "=&v"(gb) : "v"(gsrc) : "memory");
    }
    const int qblk = slot & 3;
    const int mt = qblk * 8 + (hh >> 3), nt = hh & 7;
    char* odst = ws + WS_O + (size_t)tile * 65536 + hh * 1024 + lane * 16;
    {
        const u32x4 sent = {0xffffffffu, 0xffffffffu, 0xffffffffu, 0xffffffffu};
        gst128(odst, sent);
        gst128(odst + 65536, sent);
    }
    const char* cnt = ws + WS_C + qblk * 256;
    unsigned cv = 0u;
    const char* wb = ws + WS_WO + (size_t)(w * 8) * 16384 + (nt * 2) * 1024;
    u32x16 w0, w1, w2, w3;
#pragma unroll
    for (int i = 0; i < 16; ++i) { w0[i] = 0u; w1[i] = 0u; w2[i] = 0u; w3[i] = 0u; }
    f32x16 zero;
#pragma unroll
    for (int i = 0; i < 16; ++i) zero[i] = 0.f;
    f32x16 na, nb, oa = zero, ob = zero;
    {
        const bf16x8 k0 = lds_b128(kaddr);
        const f32x16 sa = MFMA32(k0, qa, zero);
        const f32x16 sb = MFMA32(k0, qb, zero);
        const float ma = -half_max(max16(sa)) - REFOFF, mb = -half_max(max16(sb)) - REFOFF;
#pragma unroll
        for (int i = 0; i < 16; ++i) { na[i] = ma; nb[i] = mb; }
    }
    ST(1, 2);
    attn_main(w >= 4, true, kaddr, vaddr, vstep, qa, qb, na, nb, oa, ob, cnt, lane * 16, kb, kd, wb, w, w0, w1, w2, w3, cv);
    ST(1, 3);
    asm volatile("" : "+v"(ga), "+v"(gb));

    {
        float xa = fabsf(oa[0]), xb = fabsf(ob[0]), ta = oa[0], tb = ob[0];
#pragma unroll
        for (int i = 1; i < 8; ++i) { xa = fmaxf(xa, fabsf(oa[i])); xb = fmaxf(xb, fabsf(ob[i])); ta += oa[i]; tb += ob[i]; }
        const bool bad = !(oa[8] < 1.329228e36f) || !(ob[8] < 1.329228e36f) || !(fabsf(ta) <= 3.0e38f) || !(fabsf(tb) <= 3.0e38f) ||
                         !(xa > 7.7e-34f) || !(xb > 7.7e-34f);
        if (__any(bad)) {
            float ma = -INFINITY, mb = -INFINITY;
#pragma unroll 1
            for (int t = 0; t < 64; ++t) {
                const bf16x8 kf = lds_b128(kaddr + t * 2048);
                ma = fmaxf(ma, max16(MFMA32(kf, qa, zero)));
                mb = fmaxf(mb, max16(MFMA32(kf, qb, zero)));
            }
            ma = -half_max(ma); mb = -half_max(mb);
#pragma unroll
            for (int i = 0; i < 16; ++i) { na[i] = ma; nb[i] = mb; }
            oa = zero; ob = zero;
            attn_main(false, false, kaddr, vaddr, vstep, qa, qb, na, nb, oa, ob, cnt, lane * 16, kb, kd, wb, w, w0, w1, w2, w3, cv);
        }
    }
    const float sa = (isB ? 1.f - ga : ga) * __builtin_amdgcn_rcpf(oa[8]), sb = (isB ? 1.f - gb : gb) * __builtin_amdgcn_rcpf(ob[8]);
    {
        u32x4 pa = pack8(oa, 0, sa), pb = pack8(ob, 0, sb);
#pragma unroll
        for (int i = 0; i < 4; ++i) { pa[i] = min(pa[i], 0xfffffffeu); pb[i] = min(pb[i], 0xfffffffeu); }
        gst128(odst, pa);
        gst128(odst + 65536, pb);
    }
    ST(1, 4);
    for (unsigned spins = 0; spins < (1u << 20) && (unsigned)__builtin_amdgcn_readfirstlane(cv) < 64u; ++spins) {
        if ((unsigned)__builtin_amdgcn_readfirstlane(__hip_atomic_load((const unsigned*)cnt, __ATOMIC_RELAXED, __HIP_MEMORY_SCOPE_AGENT)) >= 64u) break;
        __builtin_amdgcn_s_sleep(2);
    }
    asm volatile("s_barrier" ::: "memory");
    u32x4 of[8][2];
#pragma unroll
    for (int k = 0; k < 8; ++k)
#pragma unroll
        for (int i = 0; i < 4; ++i) { of[k][0][i] = 0xffffffffu; of[k][1][i] = 0xffffffffu; }
    {
        const char* osrc = ws + WS_O + (size_t)(mt * 2) * 65536 + (w * 8) * 1024 + lane * 16;
        unsigned pend = 0xffu;
        for (unsigned spins = 0; spins < (1u << 20); ++spins) {
#pragma unroll
            for (int k = 0; k < 8; ++k)
                if (pend & (1u << k))
                    asm volatile("global_load_dwordx4 %0, %2, off sc1\n\tglobal_load_dwordx4 %1, %3, off sc1"
                                 : "+v"(of[k][0]), "+v"(of[k][1]) : "v"(osrc + k * 1024), "v"(osrc + 65536 + k * 1024) : "memory");
            asm volatile("s_waitcnt vmcnt(0)"
                         : "+v"(of[0][0]), "+v"(of[0][1]), "+v"(of[1][0]), "+v"(of[1][1]), "+v"(of[2][0]), "+v"(of[2][1]), "+v"(of[3][0]), "+v"(of[3][1]),
                           "+v"(of[4][0]), "+v"(of[4][1]), "+v"(of[5][0]), "+v"(of[5][1]), "+v"(of[6][0]), "+v"(of[6][1]), "+v"(of[7][0]), "+v"(of[7][1])
                         :: "memory");
            pend = 0u;
#pragma unroll
            for (int k = 0; k < 8; ++k) {
                const unsigned m = max(max(max(of[k][0][0], of[k][0][1]), max(of[k][0][2], of[k][0][3])),
                                       max(max(of[k][1][0], of[k][1][1]), max(of[k][1][2], of[k][1][3])));
                if (__any(m == 0xffffffffu)) pend |= 1u << k;
            }
            if (pend == 0u) break;
            __builtin_amdgcn_s_sleep(POLLSLEEP);
        }
    }
    ST(2, 0);
    u32x4 wf[8][2];
#define WSUB(W, k) wf[k][0] = __builtin_shufflevector(W, W, 0, 1, 2, 3); wf[k][1] = __builtin_shufflevector(W, W, 4, 5, 6, 7); \
                   wf[k + 1][0] = __builtin_shufflevector(W, W, 8, 9, 10, 11); wf[k + 1][1] = __builtin_shufflevector(W, W, 12, 13, 14, 15);
    WSUB(w0, 0) WSUB(w1, 2) WSUB(w2, 4) WSUB(w3, 6)
    f32x16 acc[4];
#pragma unroll
    for (int q = 0; q < 4; ++q) {
        acc[q] = zero;
#pragma unroll
        for (int k = 0; k < 8; ++k) acc[q] = MFMA32(__builtin_bit_cast(bf16x8, wf[k][q >> 1]), __builtin_bit_cast(bf16x8, of[k][q & 1]), acc[q]);
    }
    ST(2, 1);
    float fin[8];
#pragma unroll
    for (int p = 0; p < 8; ++p) {
        if (p == w) {
#pragma unroll
            for (int i = 0; i < 8; ++i) fin[i] = acc[p >> 1][8 * (p & 1) + i];
        } else {
            char* dst = smem + (p * 7 + ((w - p - 1) & 7)) * 2048 + lane * 16;
#pragma unroll
            for (int g = 0; g < 2; ++g) {
                f32x4 v;
#pragma unroll
                for (int i = 0; i < 4; ++i) v[i] = acc[p >> 1][8 * (p & 1) + 4 * g + i];
                *(f32x4*)(dst + g * 1024) = v;
            }
        }
    }
    asm volatile("s_waitcnt lgkmcnt(0)\n\ts_barrier" ::: "memory");
    ST(2, 2);
#pragma unroll
    for (int p = 0; p < 7; ++p)
#pragma unroll
        for (int g = 0; g < 2; ++g) {
            const f32x4 v = *(const f32x4*)(smem + (w * 7 + p) * 2048 + g * 1024 + lane * 16);
#pragma unroll
            for (int i = 0; i < 4; ++i) fin[4 * g + i] += v[i];
        }
    const unsigned a0 = pk_bf16(fin[0], fin[1]), a1 = pk_bf16(fin[2], fin[3]);
    const unsigned b0 = pk_bf16(fin[4], fin[5]), b1 = pk_bf16(fin[6], fin[7]);
    auto r0 = __builtin_amdgcn_permlane32_swap(a0, b0, false, false);
    auto r1 = __builtin_amdgcn_permlane32_swap(a1, b1, false, false);
    u32x4 res = {r0[0], r1[0], r0[1], r1[1]};
    gst128(out + (size_t)(mt * 64 + ((w >> 1) & 1) * 32 + r) * 512 + nt * 64 + (w >> 2) * 32 + (w & 1) * 16 + h * 8, res);
    ST(2, 3);
    asm volatile("pf_text_end:");
}

extern "C" void kernel_launch(void* const* d_in, const int* in_sizes, int n_in, void* d_out, int out_size, void* d_ws,
                              size_t ws_size, hipStream_t stream) {
    const unsigned short* x    = (const unsigned short*)d_in[0];
    const unsigned short* wq   = (const unsigned short*)d_in[1];
    const unsigned short* wk   = (const unsigned short*)d_in[2];
    const unsigned short* wv   = (const unsigned short*)d_in[3];
    const unsigned short* woA  = (const unsigned short*)d_in[4];
    const unsigned short* wqkv = (const unsigned short*)d_in[6];
    const unsigned short* woB  = (const unsigned short*)d_in[7];
    const unsigned short* gw   = (const unsigned short*)d_in[8];
    char* ws = (char*)d_ws;
    (void)hipFuncSetAttribute((const void*)proj_kernel, hipFuncAttributeMaxDynamicSharedMemorySize, P_LDS);
    (void)hipFuncSetAttribute((const void*)attn_kernel, hipFuncAttributeMaxDynamicSharedMemorySize, A_LDS);
    proj_kernel<<<256, 512, P_LDS, stream>>>(x, wq, wk, wv, wqkv, gw, woA, woB, ws);
    attn_kernel<<<256, 512, A_LDS, stream>>>(ws, (unsigned short*)d_out);
}
```

```cpp
#include <hip/hip_runtime.h>
#include <hip/hip_bf16.h>
#include <stdint.h>

typedef short    bf16x8 __attribute__((ext_vector_type(8)));
typedef short    s16x4  __attribute__((ext_vector_type(4)));
typedef float    f32x16 __attribute__((ext_vector_type(16)));
typedef float    f32x4  __attribute__((ext_vector_type(4)));
typedef float    f32x2  __attribute__((ext_vector_type(2)));
typedef unsigned u32x4  __attribute__((ext_vector_type(4)));
typedef unsigned u32x16 __attribute__((ext_vector_type(16)));
typedef __bf16   bf16x2v __attribute__((ext_vector_type(2)));

#define DI __device__ __forceinline__
#define MFMA32(a, b, c) __builtin_amdgcn_mfma_f32_32x32x16_bf16((a), (b), (c), 0, 0, 0)
#define LDS_AS __attribute__((address_space(3)))

constexpr size_t WS_Q  = 0;
constexpr size_t WS_KV = WS_Q + (size_t)64 * 65536;
constexpr size_t WS_O  = WS_KV + (size_t)34 * 131072;
constexpr size_t WS_WO = WS_O + (size_t)64 * 65536;
constexpr size_t WS_G  = WS_WO + (size_t)1024 * 1024;
constexpr size_t WS_C  = WS_G + 8192;

#define ST(k, i)
#define CODEPF 1
#define REFOFF 96.f
#define O_ORD 3
#define POLLSLEEP 2
constexpr float QSCALE = 0.25f * 1.4426950408889634f;

DI unsigned pk_bf16(float lo, float hi) {
    f32x2 v = {lo, hi};
    bf16x2v r = __builtin_convertvector(v, bf16x2v);
    return __builtin_bit_cast(unsigned, r);
}
DI float bf2f(unsigned short u) { return __uint_as_float(((unsigned)u) << 16); }

DI void glds16(const void* g, unsigned lds_base) {
    unsigned sv;
    asm volatile("s_mov_b32 %0, m0\n\ts_mov_b32 m0, %2\n\ts_nop 0\n\tglobal_load_lds_dwordx4 %1, off\n\ts_mov_b32 m0, %0"
                 : "=&s"(sv) : "v"(g), "s"(lds_base) : "memory");
}
#define WAIT_BAR(N) asm volatile("s_waitcnt vmcnt(" #N ") lgkmcnt(0)\n\ts_barrier" ::: "memory")

#define STPOL "sc1"
DI void gst128(void* p, u32x4 v) { asm volatile("global_store_dwordx4 %0, %1, off " STPOL "\n\ts_nop 1" :: "v"(p), "v"(v) : "memory"); }
DI bf16x8 lds_b128(unsigned addr) { return *(const LDS_AS bf16x8*)(uintptr_t)addr; }
DI s16x4 lds_tr(unsigned addr) {
    return __builtin_bit_cast(s16x4, __builtin_amdgcn_ds_read_tr16_b64_v4i16((LDS_AS s16x4*)(uintptr_t)addr));
}
DI bf16x8 cat8(s16x4 a, s16x4 b) { return __builtin_shufflevector(a, b, 0, 1, 2, 3, 4, 5, 6, 7); }

DI u32x4 pack8(const f32x16& a, int base, float sc) {
    u32x4 r;
    r[0] = pk_bf16(a[base + 0] * sc, a[base + 1] * sc);
    r[1] = pk_bf16(a[base + 2] * sc, a[base + 3] * sc);
    r[2] = pk_bf16(a[base + 4] * sc, a[base + 5] * sc);
    r[3] = pk_bf16(a[base + 6] * sc, a[base + 7] * sc);
    return r;
}

constexpr int P_XS = 0, P_WS = 16384, P_STAGE = 32768, P_WX = 4 * P_STAGE, P_GT = P_WX + 16384, P_WO = P_GT + 9216, P_CNT = P_WO + 4096, P_LDS = P_CNT + 16;
#define SBAR() __builtin_amdgcn_sched_barrier(0)

struct PSrc { const unsigned short *xs0, *xs1, *wsr; int ldw; };
DI PSrc proj_src(const unsigned short* x, const unsigned short* wsrc, int ldw, int mt, int nt, int l, int lane) {
    PSrc s;
    const int row = 32 * l + (lane >> 3);
    s.xs0 = x + (size_t)(mt * 128 + row) * 512 + ((lane & 7) ^ ((row >> 1) & 7)) * 8;
    s.xs1 = x + (size_t)(mt * 128 + row + 8) * 512 + ((lane & 7) ^ (((row + 8) >> 1) & 7)) * 8;
    const int krow = 16 * l + (lane >> 4);
    s.wsr = wsrc + (size_t)krow * ldw + ((lane & 15) ^ (((lane >> 4) & 3) << 2)) * 8;
    s.ldw = ldw;
    return s;
}
DI void proj_issue(const PSrc& s, int c, int l, unsigned lds0) {
    const unsigned st = lds0 + (c & 3) * P_STAGE + l * 4096;
    glds16(s.xs0 + c * 64, st + P_XS);
    glds16(s.xs1 + c * 64, st + P_XS + 1024);
    glds16(s.xs0 + c * 64 + 16 * 512, st + P_XS + 2048);
    glds16(s.xs1 + c * 64 + 16 * 512, st + P_XS + 3072);
#pragma unroll
    for (int i = 0; i < 4; ++i) glds16(s.wsr + (size_t)(c * 64 + 4 * i) * s.ldw, st + P_WS + i * 1024);
}
DI float wave_sum63(float v) {
#define DPP_ADD(ctrl, rm) v += __int_as_float(__builtin_amdgcn_update_dpp(0, __float_as_int(v), ctrl, rm, 0xf, false))
    DPP_ADD(0x111, 0xf); DPP_ADD(0x112, 0xf); DPP_ADD(0x114, 0xf); DPP_ADD(0x118, 0xf);
    DPP_ADD(0x142, 0xa); DPP_ADD(0x143, 0xc);
#undef DPP_ADD
    return v;
}
DI void lds_spin(const char* smem, int off, unsigned target) {
    const volatile LDS_AS unsigned* p = (const volatile LDS_AS unsigned*)(uintptr_t)((unsigned)(uintptr_t)smem + off);
    while ((unsigned)__builtin_amdgcn_readfirstlane(*p) < target) __builtin_amdgcn_s_sleep(1);
    asm volatile("" ::: "memory");
}

template <bool ISV>
DI void proj_compute(const PSrc& src, const unsigned short* __restrict__ x, const unsigned short* __restrict__ wx,
                     const unsigned short* __restrict__ gw, const unsigned short* __restrict__ wo, char* __restrict__ ws,
                     int mt, int nt, int w, unsigned lds0, char* smem) {
    const int lane = threadIdx.x & 63;
    const int wm = w >> 1, wn = w & 1;
    const int r = lane & 31, h = lane >> 5;
    unsigned xo[4][2];
#pragma unroll
    for (int tt = 0; tt < 2; ++tt) {
        const int row = wm * 64 + tt * 32 + r;
#pragma unroll
        for (int ks = 0; ks < 4; ++ks) xo[ks][tt] = lds0 + P_XS + row * 128 + (((2 * ks + h) ^ ((row >> 1) & 7)) << 4);
    }
    const int q4 = (lane & 15) >> 2, p4 = lane & 3, g1 = (lane >> 4) & 1;
    unsigned wa[2];
#pragma unroll
    for (int ct = 0; ct < 2; ++ct)
        wa[ct] = lds0 + P_WS + (8 * h + q4) * 256 + ((4 * ((2 * wn + ct) ^ q4) + 2 * g1 + (p4 >> 1)) << 4) + (p4 & 1) * 8;
    f32x16 acc[2][2];
#pragma unroll
    for (int i = 0; i < 16; ++i) { acc[0][0][i] = 0.f; acc[0][1][i] = 0.f; acc[1][0][i] = 0.f; acc[1][1][i] = 0.f; }
    bf16x8 fx[2][4][2], fw[2][4][2];
    auto mm = [&](int bf, int ks) {
#pragma unroll
        for (int ct = 0; ct < 2; ++ct)
#pragma unroll
            for (int tt = 0; tt < 2; ++tt)
                acc[ct][tt] = ISV ? MFMA32(fx[bf][ks][tt], fw[bf][ks][ct], acc[ct][tt]) : MFMA32(fw[bf][ks][ct], fx[bf][ks][tt], acc[ct][tt]);
    };
    auto rd = [&](int bf, int ks, unsigned so) {
#pragma unroll
        for (int tt = 0; tt < 2; ++tt) fx[bf][ks][tt] = lds_b128(xo[ks][tt] + so);
#pragma unroll
        for (int ct = 0; ct < 2; ++ct) fw[bf][ks][ct] = cat8(lds_tr(wa[ct] + so + ks * 4096), lds_tr(wa[ct] + so + ks * 4096 + 1024));
    };
    auto step = [&](int bf, int c) {
        __builtin_amdgcn_s_waitcnt(0xC07F);
        asm volatile("s_barrier" ::: "memory");
        SBAR();
        const unsigned so = (c & 3) * P_STAGE;
#pragma unroll
        for (int ks = 0; ks < 4; ++ks) { rd(bf, ks, so); mm(bf ^ 1, ks); }
#pragma unroll
        for (int i = 0; i < 12; ++i) {
            __builtin_amdgcn_sched_group_barrier(0x008, 1, 0);
            __builtin_amdgcn_sched_group_barrier(0x002, 1, 0);
            __builtin_amdgcn_sched_group_barrier(0x100, 2, 0);
        }
        __builtin_amdgcn_sched_group_barrier(0x008, 4, 0);
        SBAR();
    };
    asm volatile("" : "+v"(xo[0][0]), "+v"(xo[0][1]), "+v"(xo[1][0]), "+v"(xo[1][1]), "+v"(xo[2][0]), "+v"(xo[2][1]),
                      "+v"(xo[3][0]), "+v"(xo[3][1]), "+v"(wa[0]), "+v"(wa[1]),
                      "+v"(acc[0][0]), "+v"(acc[0][1]), "+v"(acc[1][0]), "+v"(acc[1][1]));
    asm volatile("s_waitcnt vmcnt(0)" ::: "memory");
    if (lane == 0) atomicAdd((unsigned*)(smem + P_CNT), 1u);
    lds_spin(smem, P_CNT, 4u);
    ST(0, 1);
    rd(0, 0, 0); rd(0, 1, 0); rd(0, 2, 0); rd(0, 3, 0);
    SBAR();
    {
        if (nt >= 8) {
            const unsigned short* es = wx + (size_t)((lane >> 5) * 64 + 16 * w) * 16 + (lane & 31) * 8;
#pragma unroll
            for (int i = 0; i < 4; ++i) glds16(es + i * 2048, lds0 + P_WX + w * 4096 + i * 1024);
        }
        const unsigned short* px = x + (size_t)(mt * 128 + nt * 8 + 2 * w) * 512 + lane * 8;
        glds16(px, lds0 + P_GT + w * 2048);
        glds16(px + 512, lds0 + P_GT + w * 2048 + 1024);
        glds16(gw + lane * 8, lds0 + P_GT + 8192);
        glds16(wo + (size_t)(4 * w + (lane >> 4)) * 512 + ((lane & 15) ^ (((lane >> 4) & 3) << 2)) * 8, lds0 + P_WO + w * 1024);
    }

#pragma unroll 1
    for (int c = 1; c < 7; c += 2) { step(1, c); step(0, c + 1); }
    asm volatile("s_waitcnt vmcnt(0)" ::: "memory");
    step(1, 7);
    ST(0, 2);
#pragma unroll
    for (int ct = 0; ct < 2; ++ct)
#pragma unroll
        for (int tt = 0; tt < 2; ++tt)
#pragma unroll
            for (int ks = 0; ks < 4; ++ks)
                acc[ct][tt] = ISV ? MFMA32(fx[1][ks][tt], fw[1][ks][ct], acc[ct][tt]) : MFMA32(fw[1][ks][ct], fx[1][ks][tt], acc[ct][tt]);
    ST(0, 3);
#pragma unroll
    for (int ct = 0; ct < 2; ++ct)
#pragma unroll
        for (int tt = 0; tt < 2; ++tt) {
            const int hd0 = (nt & 3) * 8 + wn * 4 + ct * 2;
            const int tile = mt * 4 + wm * 2 + tt;
            if (ISV) {
                char* dst = ws + WS_KV + (size_t)(hd0 + (r >> 4)) * 131072 + tile * 2048 + 1024 + h * 256 + (r & 15) * 16;
                gst128(dst, pack8(acc[ct][tt], 0, 1.f));
                gst128(dst + 512, pack8(acc[ct][tt], 8, 1.f));
            } else {
                const bool isq = nt < 8;
                const float sc = isq ? QSCALE : 1.f;
                const size_t hs = isq ? 65536 : 131072;
                char* dst = isq ? (ws + WS_Q + (size_t)((nt >> 2) * 32 + hd0) * 65536 + tile * 1024 + lane * 16)
                                : (ws + WS_KV + (size_t)hd0 * 131072 + tile * 2048 + lane * 16);
                gst128(dst, pack8(acc[ct][tt], 0, sc));
                gst128(dst + hs, pack8(acc[ct][tt], 8, sc));
            }
        }
}

template <int XM>
DI void proj_loader(const PSrc& src, const unsigned short* __restrict__ x, const unsigned short* __restrict__ wk,
                    const unsigned short* __restrict__ wv, const unsigned short* __restrict__ gw, char* __restrict__ ws,
                    int mt, int nt, int l, unsigned lds0, char* smem) {
    const int lane = threadIdx.x & 63;
    const int r = lane & 31, h = lane >> 5;
    constexpr bool isx = XM != 0, isv = XM == 2;
    const int xtt = nt & 3;
    proj_issue(src, 2, l, lds0);
    const int q4 = (lane & 15) >> 2, p4 = lane & 3;
    const unsigned ea = lds0 + P_WX + l * 4096 + (8 * h + q4) * 32 + p4 * 8;
    const int xrow = xtt * 32 + r;
    const unsigned exa = lds0 + P_XS + xrow * 128 + (((2 * l + h) ^ ((xrow >> 1) & 7)) << 4);
    const int gtok = mt * 128 + nt * 8 + 2 * l;
    u32x4 gx0, gx1, gv;
    bf16x8 xe[8];
    if (isx) { lds_spin(smem, P_CNT, 4u); xe[0] = lds_b128(exa); }
#pragma unroll
    for (int c = 1; c < 8; ++c) {
        if (c < 7) WAIT_BAR(8); else WAIT_BAR(0);
        if (isx) xe[c] = lds_b128(exa + (c & 3) * P_STAGE);
        if (c < 6) proj_issue(src, c + 2, l, lds0);
    }
    gx0 = *(const u32x4*)(smem + P_GT + l * 2048 + lane * 16);
    gx1 = *(const u32x4*)(smem + P_GT + l * 2048 + 1024 + lane * 16);
    gv  = *(const u32x4*)(smem + P_GT + 8192 + lane * 16);
    {
        const int g1 = (lane >> 4) & 1;
        const unsigned woa = lds0 + P_WO + (4 * h + q4) * 256 + ((4 * (l ^ q4) + 2 * g1 + (p4 >> 1)) << 4) + (p4 & 1) * 8;
        const bf16x8 f = cat8(lds_tr(woa), lds_tr(woa + 2048));
        gst128(ws + WS_WO + (size_t)(blockIdx.x * 4 + l) * 1024 + lane * 16, __builtin_bit_cast(u32x4, f));
    }
    {
        float s0 = 0.f, s1 = 0.f;
#pragma unroll
        for (int i = 0; i < 4; ++i) {
            const float wl = __uint_as_float(gv[i] << 16), wh = __uint_as_float(gv[i] & 0xffff0000u);
            s0 += __uint_as_float(gx0[i] << 16) * wl + __uint_as_float(gx0[i] & 0xffff0000u) * wh;
            s1 += __uint_as_float(gx1[i] << 16) * wl + __uint_as_float(gx1[i] & 0xffff0000u) * wh;
        }
        s0 = wave_sum63(s0); s1 = wave_sum63(s1);
        if (lane == 63) {
            f32x2 g = {__builtin_amdgcn_rcpf(1.f + __expf(-s0)), __builtin_amdgcn_rcpf(1.f + __expf(-s1))};
            *(f32x2*)((float*)(ws + WS_G) + gtok) = g;
        }
    }
    f32x16 acce;
#pragma unroll
    for (int i = 0; i < 16; ++i) acce[i] = 0.f;
    if (isx) {
        __builtin_amdgcn_s_setprio(3);
#pragma unroll
        for (int c = 0; c < 8; ++c) {
            const bf16x8 we = cat8(lds_tr(ea + c * 512), lds_tr(ea + c * 512 + 128));
            acce = isv ? MFMA32(xe[c], we, acce) : MFMA32(we, xe[c], acce);
        }
    }
    if (isx) {
        f32x4* red = (f32x4*)smem;
#pragma unroll
        for (int g = 0; g < 4; ++g) {
            f32x4 v = {acce[4 * g], acce[4 * g + 1], acce[4 * g + 2], acce[4 * g + 3]};
            red[(l * 4 + g) * 64 + lane] = v;
        }
        unsigned old = 0;
        asm volatile("s_waitcnt lgkmcnt(0)" ::: "memory");
        if (lane == 0) old = atomicAdd((unsigned*)(smem + P_CNT), 1u);
        old = __builtin_amdgcn_readfirstlane(old);
        if (old == 7) {
            asm volatile("" ::: "memory");
#pragma unroll
            for (int g = 0; g < 4; ++g) {
                const f32x4 v = red[g * 64 + lane] + red[(4 + g) * 64 + lane] + red[(8 + g) * 64 + lane] + red[(12 + g) * 64 + lane];
#pragma unroll
                for (int i = 0; i < 4; ++i) acce[4 * g + i] = v[i];
            }
            char* base = ws + WS_KV + (size_t)32 * 131072 + (mt * 4 + xtt) * 2048;
            if (isv) {
                if (r < 16) {
                    gst128(base + 1024 + h * 256 + r * 16, pack8(acce, 0, 1.f));
                    gst128(base + 1536 + h * 256 + r * 16, pack8(acce, 8, 1.f));
                }
            } else {
                gst128(base + lane * 16, pack8(acce, 0, 1.f));
            }
        }
    }
}

__global__ __launch_bounds__(512, 2) void proj_kernel(const unsigned short* __restrict__ x, const unsigned short* __restrict__ wq,
                                                      const unsigned short* __restrict__ wk, const unsigned short* __restrict__ wv,
                                                      const unsigned short* __restrict__ wqkv, const unsigned short* __restrict__ gw,
                                                      const unsigned short* __restrict__ woA, const unsigned short* __restrict__ woB,
                                                      char* __restrict__ ws) {
    extern __shared__ __attribute__((aligned(16))) char smem[];
    const unsigned lds0 = (unsigned)(uintptr_t)smem;
    const int w = __builtin_amdgcn_readfirstlane(threadIdx.x >> 6);
    const int b = blockIdx.x, xcd = b & 7, slot = b >> 3;
    const int mt = (xcd & 3) * 4 + (slot & 3), nt = (xcd >> 2) * 8 + (slot >> 2);
    ST(0, 0);
    unsigned cpf = 0;
    if (w == 7) {
        unsigned long long pc; int len;
        asm volatile("s_getpc_b64 %0\n\tpf_text_begin:\n\ts_mov_b32 %1, pf_text_end-pf_text_begin" : "=s"(pc), "=s"(len));
        int off = ((slot & (8 * CODEPF - 1)) * 32 + (int)(threadIdx.x & 31)) * 128;
        off = off < len ? off : 0;
        asm volatile("global_load_dword %0, %1, %2" : "=&v"(cpf) : "v"(off), "s"(pc) : "memory");
    }
    if (threadIdx.x == 0) *(unsigned*)(smem + P_CNT) = 0u;
    const unsigned short* wsrc = (nt < 4) ? (wq + nt * 128) : (wqkv + (nt - 4) * 128);
    const PSrc src = proj_src(x, wsrc, (nt < 4) ? 512 : 1536, mt, nt, w & 3, threadIdx.x & 63);
    const unsigned short* wo = ((b < 128) ? woA : woB) + (size_t)((b >> 2) & 31) * 16 * 512 + (b & 3) * 128;
    proj_issue(src, w >> 2, w & 3, lds0);
    asm volatile("s_waitcnt lgkmcnt(0)\n\ts_barrier" ::: "memory");
    if (w >= 4) {
        if (nt >= 12)     proj_loader<2>(src, x, wk, wv, gw, ws, mt, nt, w - 4, lds0, smem);
        else if (nt >= 8) proj_loader<1>(src, x, wk, wv, gw, ws, mt, nt, w - 4, lds0, smem);
        else              proj_loader<0>(src, x, wk, wv, gw, ws, mt, nt, w - 4, lds0, smem);
        asm volatile("" :: "v"(cpf));
        if (b == 0 && w == 4 && (threadIdx.x & 63) < 4) *(unsigned*)(ws + WS_C + (threadIdx.x & 63) * 256) = 0u;
        ST(0, 1); ST(0, 2); ST(0, 3);
    } else if (nt >= 12) proj_compute<true>(src, x, wv, gw, wo, ws, mt, nt, w, lds0, smem);
    else                 proj_compute<false>(src, x, wk, gw, wo, ws, mt, nt, w, lds0, smem);
    ST(0, 4);
}

constexpr int A_KV = 0, A_CONST = 131072, A_LDS = A_CONST + 8192;

DI float max16(const f32x16& s) {
    float a = fmaxf(fmaxf(s[0], s[1]), s[2]), b = fmaxf(fmaxf(s[3], s[4]), s[5]);
    a = fmaxf(fmaxf(a, s[6]), s[7]);   b = fmaxf(fmaxf(b, s[8]), s[9]);
    a = fmaxf(fmaxf(a, s[10]), s[11]); b = fmaxf(fmaxf(b, s[12]), s[13]);
    a = fmaxf(fmaxf(a, s[14]), s[15]);
    return fmaxf(a, b);
}
DI float half_max(float m) {
    auto rr = __builtin_amdgcn_permlane32_swap(__float_as_uint(m), __float_as_uint(m), false, false);
    return fmaxf(__uint_as_float(rr[0]), __uint_as_float(rr[1]));
}

#define R_SA "192"
#define R_SB "208"
#define EX(X, i) "v_exp_f32 v[" X "+" #i "], v[" X "+" #i "]\n\t"
#define CV(X, j, a, b) "v_cvt_pk_bf16_f32 v[224+" #j "], v[" X "+" #a "], v[" X "+" #b "]\n\t"
#define HALF(X, Y, OX, OY, NY, QY, WA, RA, WB, RB, WC, RC)                                        \
    EX(X, 0) EX(X, 1) WA                                                                          \
    "v_mfma_f32_32x32x16_bf16 " OY ", %[v1], v[228:231], " OY "\n\t" RA                           \
    EX(X, 2) EX(X, 3) CV(X, 0, 0, 1) EX(X, 4) EX(X, 5) CV(X, 1, 2, 3) WB                          \
    "v_mfma_f32_32x32x16_bf16 v[" Y ":" Y "+15], %[kf], " QY ", " NY "\n\t" RB                    \
    EX(X, 6) EX(X, 7) CV(X, 2, 4, 5) EX(X, 8) EX(X, 9) CV(X, 3, 6, 7) EX(X, 10) EX(X, 11) WC      \
    "v_mfma_f32_32x32x16_bf16 " OX ", %[v0], v[224:227], " OX "\n\t" RC                           \
    CV(X, 4, 8, 9) EX(X, 12) EX(X, 13) CV(X, 5, 10, 11) EX(X, 14) EX(X, 15) CV(X, 6, 12, 13) CV(X, 7, 14, 15)
#define TILE(OFF_V1, OFF_KN, OFF_V0N, P0, P1)                                                     \
    HALF(R_SA, R_SB, "%[oa]", "%[ob]", "%[nb]", "%[qb]",                                          \
         "", "ds_read_b128 %[v1], %[va] offset:" OFF_V1 "\n\t" P0,                                \
         "", "ds_read_b128 %[kf], %[ka] offset:" OFF_KN "\n\t",                                   \
         "s_waitcnt lgkmcnt(2)\n\t", "")                                                          \
    HALF(R_SB, R_SA, "%[ob]", "%[oa]", "%[na]", "%[qa]",                                          \
         "s_waitcnt lgkmcnt(1)\n\t", P1,                                                          \
         "s_waitcnt lgkmcnt(0)\n\t", "",                                                          \
         "", "ds_read_b128 %[v0], %[va] offset:" OFF_V0N "\n\t")
#define KVDMA                                                                                     \
    "global_load_lds_dwordx4 %[lo], s[40:41]\n\t"                                                 \
    "s_add_u32 s40, s40, 0x2000\n\ts_addc_u32 s41, s41, 0\n\ts_add_u32 m0, m0, 0x2000\n\ts_nop 0\n\t"
#define WOLD(R)                                                                                   \
    "global_load_dwordx4 v[" #R ":" #R "+3], %[lo], s[42:43]\n\t"                                 \
    "global_load_dwordx4 v[" #R "+4:" #R "+7], %[lo], s[42:43] offset:1024\n\t"                   \
    "s_add_u32 s42, s42, 0x4000\n\ts_addc_u32 s43, s43, 0\n\t"
#define ATTN_ASM(PY, PO)                                                                          \
    asm volatile(                                                                                 \
        "s_mov_b32 s44, m0\n\ts_mov_b32 m0, %[kd]\n\ts_mov_b64 s[40:41], %[kb]\n\ts_mov_b64 s[42:43], %[wb]\n\ts_cmp_lg_u32 %[yg], 0\n\ts_cselect_b64 vcc, -1, 0\n\t"               \
        "ds_read_b128 %[kf], %[ka]\n\t"                                                           \
        "ds_read_b128 %[v1], %[va]\n\t"                                                           \
        "ds_read_b128 %[v0], %[va]\n\t"                                                           \
        "v_mov_b32 v228, 0\n\tv_mov_b32 v229, 0\n\tv_mov_b32 v230, 0\n\tv_mov_b32 v231, 0\n\t"    \
        "s_waitcnt lgkmcnt(0)\n\t"                                                                \
        "v_mfma_f32_32x32x16_bf16 v[192:207], %[kf], %[qa], %[na]\n\t"                            \
        "s_nop 7\n\ts_nop 7\n\t"                                                                  \
        "L_loop_%=:\n\t"                                                                          \
        "s_bitcmp1_b32 %[mk], %[cnt]\n\t"                                                         \
        "s_cbranch_scc1 L_sp_%=\n\t"                                                              \
        "L_go_%=:\n\t"                                                                            \
        TILE("512", "2048", "2048", "s_setprio 0\n\t", PY)                                        \
        TILE("2560", "4096", "4096", PO, "")                                                      \
        TILE("4608", "6144", "6144", "", "")                                                      \
        HALF(R_SA, R_SB, "%[oa]", "%[ob]", "%[nb]", "%[qb]",                                      \
             "", "ds_read_b128 %[v1], %[va] offset:6656\n\t",                                     \
             "", "ds_read_b128 %[kf], %[ka] offset:8192\n\tv_add_u32 %[va], %[va], %[vs]\n\t",    \
             "s_waitcnt lgkmcnt(2)\n\t", "v_add_u32 %[ka], 0x2000, %[ka]\n\t")                    \
        HALF(R_SB, R_SA, "%[ob]", "%[oa]", "%[na]", "%[qa]",                                      \
             "s_waitcnt lgkmcnt(1)\n\t", "s_add_u32 %[cnt], %[cnt], 1\n\t",                       \
             "s_waitcnt lgkmcnt(0)\n\t", "s_and_b32 s45, %[cnt], 15\n\t",                           \
             "", "ds_read_b128 %[v0], %[va]\n\t")                                                 \
        "s_cbranch_scc1 L_loop_%=\n\t"                                                            \
        "s_branch L_end_%=\n\t"                                                                   \
        "L_sp_%=:\n\t"                                                                            \
        "s_cmp_gt_u32 %[cnt], 2\n\t"                                                              \
        "s_cbranch_scc1 L_s3_%=\n\t"                                                              \
        KVDMA KVDMA KVDMA KVDMA                                                                   \
        "s_branch L_go_%=\n\t"                                                                    \
        "L_s3_%=:\n\t"                                                                            \
        "s_cmp_gt_u32 %[cnt], 3\n\t"                                                              \
        "s_cbranch_scc1 L_s4_%=\n\t"                                                              \
        "s_waitcnt vmcnt(0)\n\t"                                                                  \
        "s_barrier\n\t"                                                                           \
        "s_branch L_go_%=\n\t"                                                                    \
        "L_s4_%=:\n\t"                                                                            \
        "s_cmp_gt_u32 %[cnt], 8\n\t"                                                              \
        "s_cbranch_scc1 L_s5_%=\n\t"                                                              \
        "v_mov_b32 v232, -1\n\tv_mov_b32 v233, -1\n\tv_mov_b32 v234, -1\n\tv_mov_b32 v235, -1\n\t" \
        "s_mov_b64 s[40:41], %[ob2]\n\t"                                                          \
        "global_store_dwordx4 %[lo], v[232:235], s[40:41] sc1\n\t"                                \
        "s_add_u32 s40, s40, 0x10000\n\ts_addc_u32 s41, s41, 0\n\t"                               \
        "global_store_dwordx4 %[lo], v[232:235], s[40:41] sc1\n\t"                                \
        WOLD(128) WOLD(136) WOLD(144) WOLD(152) WOLD(160) WOLD(168) WOLD(176) WOLD(184)           \
        "s_branch L_go_%=\n\t"                                                                    \
        "L_s5_%=:\n\t"                                                                            \
        "s_cmp_gt_u32 %[cnt], 11\n\t"                                                             \
        "s_cbranch_scc1 L_s6_%=\n\t"                                                              \
        "s_waitcnt vmcnt(0)\n\t"                                                                  \
        "s_mov_b64 exec, 1\n\t"                                                                   \
        "v_mov_b32 v232, 1\n\t"                                                                   \
        "global_atomic_add %[pf], v232, off sc1\n\t"                                              \
        "s_mov_b64 exec, -1\n\t"                                                                  \
        "s_branch L_go_%=\n\t"                                                                    \
        "L_s6_%=:\n\t"                                                                            \
        "global_load_dword %[cv], %[pf], off sc1\n\t"                                             \
        "s_branch L_go_%=\n\t"                                                                    \
        "L_end_%=:\n\t"                                                                           \
        "s_nop 1\n\t"                                                                             \
        "v_mfma_f32_32x32x16_bf16 %[ob], %[v1], v[228:231], %[ob]\n\t"                            \
        "s_waitcnt vmcnt(0) lgkmcnt(0)\n\t"                                                       \
        "s_mov_b32 m0, s44\n\t"                                                                \
        "s_nop 7\n\ts_nop 7\n\t"                                                                  \
        : [oa] "+v"(oa), [ob] "+v"(ob), [ka] "+v"(ka), [va] "+v"(va), [cnt] "+s"(cnt), [cv] "+v"(cv), \
          [kf] "=&v"(kf), [v0] "=&v"(v0), [v1] "=&v"(v1),                                         \
          "+{v[128:143]}"(w0), "+{v[144:159]}"(w1), "+{v[160:175]}"(w2), "+{v[176:191]}"(w3)      \
        : [qa] "v"(qa), [qb] "v"(qb), [na] "v"(na), [nb] "v"(nb), [vs] "v"(vstep), [yg] "s"(yg), [pf] "v"(pf), \
          [lo] "v"(lo), [kb] "s"(kb), [kd] "s"(kd), [wb] "s"(wb), [ob2] "s"(ob2), [mk] "s"(mk)                                                \
        : "memory", "scc", "vcc", "s40", "s41", "s42", "s43", "s44", "s45",                                                                        \
          "v192", "v193", "v194", "v195", "v196", "v197", "v198", "v199", "v200", "v201", "v202", "v203", "v204", "v205", "v206", "v207", \
          "v208", "v209", "v210", "v211", "v212", "v213", "v214", "v215", "v216", "v217", "v218", "v219", "v220", "v221", "v222", "v223", \
          "v224", "v225", "v226", "v227", "v228", "v229", "v230", "v231", "v232", "v233", "v234", "v235")

DI void attn_main(bool young, bool first, unsigned ka, unsigned va, unsigned vstep, const bf16x8& qa, const bf16x8& qb, const f32x16& na,
                  const f32x16& nb, f32x16& oa, f32x16& ob, const char* pf, unsigned lo, const char* kb, unsigned kd,
                  const char* wb, const char* ob2, u32x16& w0, u32x16& w1, u32x16& w2, u32x16& w3, unsigned& cv) {
    bf16x8 kf, v0, v1;
    int cnt = __builtin_amdgcn_readfirstlane(first ? 0 : 16);
    const int yg = __builtin_amdgcn_readfirstlane(young ? 1 : 0);
    const int mk = __builtin_amdgcn_readfirstlane(young ? 0x422f : 0x448f);
    ATTN_ASM("s_cbranch_vccz L_py_%=\n\ts_setprio 1\n\tL_py_%=:\n\t", "s_setprio 1\n\t");
}

__global__ __launch_bounds__(512, 2) void attn_kernel(char* __restrict__ ws, unsigned short* __restrict__ out) {
    extern __shared__ __attribute__((aligned(16))) char smem[];
    const unsigned lds0 = (unsigned)(uintptr_t)smem;
    const int tid = threadIdx.x, lane = tid & 63;
    const int w = __builtin_amdgcn_readfirstlane(tid >> 6);
    const int r = lane & 31, h = lane >> 5;
    const int b = blockIdx.x, xcd = b & 7, slot = b >> 3;
    const int isB = (slot < 16);
    const int hd = xcd * 4 + ((slot & 15) >> 2);
    const int hh = isB ? 32 + hd : hd;
    const int kvh = isB ? hd : 32;
    const int tile = (slot & 3) * 16 + 2 * w;

    ST(1, 0);
    bf16x8 qa, qb;
    {
        const char* qsrc = ws + WS_Q + (size_t)hh * 65536 + tile * 1024 + lane * 16;
        asm volatile("global_load_dwordx4 %0, %2, off\n\tglobal_load_dwordx4 %1, %2, off offset:1024"
                     : "=&v"(qa), "=&v"(qb) : "v"(qsrc) : "memory");
    }
    const char* kvsrc = ws + WS_KV + (size_t)kvh * 131072 + w * 1024 + lane * 16;
#pragma unroll
    for (int i = 0; i < 4; ++i) glds16(kvsrc + i * 8192, lds0 + A_KV + i * 8192 + w * 1024);
    {
        const unsigned one = ((tid & 15) == 0 || (tid & 15) == 4) ? 0x3f803f80u : 0u;
        u32x4 c = {one, one, one, one};
        *(u32x4*)(smem + A_CONST + tid * 16) = c;
    }
    const unsigned kaddr = lds0 + A_KV + lane * 16;
    const unsigned vaddr = (r < 16) ? (lds0 + A_KV + 1024 + h * 256 + r * 16) : (lds0 + A_CONST + (r - 16) * 16);
    const unsigned vstep = (r < 16) ? 8192u : 0u;

    asm volatile("s_waitcnt vmcnt(0) lgkmcnt(0)\n\ts_barrier" : "+v"(qa), "+v"(qb) :: "memory");
    ST(1, 1);
    const char* kb = ws + WS_KV + (size_t)kvh * 131072 + 32768 + w * 1024;
    const unsigned kd = lds0 + A_KV + 32768 + w * 1024;
    float ga, gb;
    {
        const float* gsrc = (const float*)(ws + WS_G) + tile * 32 + r;
        asm volatile("global_load_dword %0, %2, off\n\tglobal_load_dword %1, %2, off offset:128" : "=&v"(ga), "=&v"(gb) : "v"(gsrc) : "memory");
    }
    const int qblk = slot & 3;
    const int mt = qblk * 8 + (hh >> 3), nt = hh & 7;
    char* obase = ws + WS_O + (size_t)tile * 65536 + hh * 1024;
    char* odst = obase + lane * 16;
    const char* cnt = ws + WS_C + qblk * 256;
    unsigned cv = 0u;
    const char* wb = ws + WS_WO + (size_t)(w * 8) * 16384 + (nt * 2) * 1024;
    u32x16 w0, w1, w2, w3;
#pragma unroll
    for (int i = 0; i < 16; ++i) { w0[i] = 0u; w1[i] = 0u; w2[i] = 0u; w3[i] = 0u; }
    f32x16 zero;
#pragma unroll
    for (int i = 0; i < 16; ++i) zero[i] = 0.f;
    f32x16 na, nb, oa = zero, ob = zero;
    {
        const bf16x8 k0 = lds_b128(kaddr);
        const f32x16 sa = MFMA32(k0, qa, zero);
        const f32x16 sb = MFMA32(k0, qb, zero);
        const float ma = -half_max(max16(sa)) - REFOFF, mb = -half_max(max16(sb)) - REFOFF;
#pragma unroll
        for (int i = 0; i < 16; ++i) { na[i] = ma; nb[i] = mb; }
    }
    ST(1, 2);
    attn_main(w >= 4, true, kaddr, vaddr, vstep, qa, qb, na, nb, oa, ob, cnt, lane * 16, kb, kd, wb, obase, w0, w1, w2, w3, cv);
    ST(1, 3);
    asm volatile("" : "+v"(ga), "+v"(gb));

    {
        float xa = fabsf(oa[0]), xb = fabsf(ob[0]), ta = oa[0], tb = ob[0];
#pragma unroll
        for (int i = 1; i < 8; ++i) { xa = fmaxf(xa, fabsf(oa[i])); xb = fmaxf(xb, fabsf(ob[i])); ta += oa[i]; tb += ob[i]; }
        const bool bad = !(oa[8] < 1.329228e36f) || !(ob[8] < 1.329228e36f) || !(fabsf(ta) <= 3.0e38f) || !(fabsf(tb) <= 3.0e38f) ||
                         !(xa > 7.7e-34f) || !(xb > 7.7e-34f);
        if (__any(bad)) {
            float ma = -INFINITY, mb = -INFINITY;
#pragma unroll 1
            for (int t = 0; t < 64; ++t) {
                const bf16x8 kf = lds_b128(kaddr + t * 2048);
                ma = fmaxf(ma, max16(MFMA32(kf, qa, zero)));
                mb = fmaxf(mb, max16(MFMA32(kf, qb, zero)));
            }
            ma = -half_max(ma); mb = -half_max(mb);
#pragma unroll
            for (int i = 0; i < 16; ++i) { na[i] = ma; nb[i] = mb; }
            oa = zero; ob = zero;
            attn_main(false, false, kaddr, vaddr, vstep, qa, qb, na, nb, oa, ob, cnt, lane * 16, kb, kd, wb, obase, w0, w1, w2, w3, cv);
        }
    }
    const float sa = (isB ? 1.f - ga : ga) * __builtin_amdgcn_rcpf(oa[8]), sb = (isB ? 1.f - gb : gb) * __builtin_amdgcn_rcpf(ob[8]);
    {
        u32x4 pa = pack8(oa, 0, sa), pb = pack8(ob, 0, sb);
#pragma unroll
        for (int i = 0; i < 4; ++i) { pa[i] = min(pa[i], 0xfffffffeu); pb[i] = min(pb[i], 0xfffffffeu); }
        gst128(odst, pa);
        gst128(odst + 65536, pb);
    }
    ST(1, 4);
    for (unsigned spins = 0; spins < (1u << 20) && (unsigned)__builtin_amdgcn_readfirstlane(cv) < 512u; ++spins) {
        if ((unsigned)__builtin_amdgcn_readfirstlane(__hip_atomic_load((const unsigned*)cnt, __ATOMIC_RELAXED, __HIP_MEMORY_SCOPE_AGENT)) >= 512u) break;
        __builtin_amdgcn_s_sleep(2);
    }
    asm volatile("s_barrier" ::: "memory");
    ST(2, 0);
    u32x4 wf[8][2];
#define WSUB(W, k) wf[k][0] = __builtin_shufflevector(W, W, 0, 1, 2, 3); wf[k][1] = __builtin_shufflevector(W, W, 4, 5, 6, 7); \
                   wf[k + 1][0] = __builtin_shufflevector(W, W, 8, 9, 10, 11); wf[k + 1][1] = __builtin_shufflevector(W, W, 12, 13, 14, 15);
    WSUB(w0, 0) WSUB(w1, 2) WSUB(w2, 4) WSUB(w3, 6)
    f32x16 acc[4] = {zero, zero, zero, zero};
    float fin[8];
    {
        const char* osrc = ws + WS_O + (size_t)(mt * 2) * 65536 + (w * 8) * 1024;
        const unsigned lo16 = lane * 16;
        u32x4 of[8][2];
        auto valid = [&](int k) -> bool {
            const unsigned m = max(max(max(of[k][0][0], of[k][0][1]), max(of[k][0][2], of[k][0][3])),
                                   max(max(of[k][1][0], of[k][1][1]), max(of[k][1][2], of[k][1][3])));
            return !__any(m == 0xffffffffu);
        };
        auto mul = [&](int k) {
#pragma unroll
            for (int q = 0; q < 4; ++q)
                acc[q] = MFMA32(__builtin_bit_cast(bf16x8, wf[k][q >> 1]), __builtin_bit_cast(bf16x8, of[k][q & 1]), acc[q]);
        };
#pragma unroll
        for (int k = 0; k < 8; ++k)
            asm volatile("global_load_dwordx4 %0, %2, %3 offset:%5 sc1\n\tglobal_load_dwordx4 %1, %2, %4 offset:%5 sc1"
                         : "=&v"(of[k][0]), "=&v"(of[k][1])
                         : "v"(lo16), "s"(osrc + (k >> 2) * 4096), "s"(osrc + 65536 + (k >> 2) * 4096), "i"((k & 3) * 1024) : "memory");
        auto settle = [&](int k0, int k1) {
            const unsigned need = ((1u << k1) - 1u) & ~((1u << k0) - 1u);
            for (unsigned spins = 0; spins < (1u << 20); ++spins) {
                asm volatile("s_waitcnt vmcnt(0)"
                             : "+v"(of[0][0]), "+v"(of[0][1]), "+v"(of[1][0]), "+v"(of[1][1]), "+v"(of[2][0]), "+v"(of[2][1]), "+v"(of[3][0]), "+v"(of[3][1]),
                               "+v"(of[4][0]), "+v"(of[4][1]), "+v"(of[5][0]), "+v"(of[5][1]), "+v"(of[6][0]), "+v"(of[6][1]), "+v"(of[7][0]), "+v"(of[7][1])
                             :: "memory");
                unsigned pend = 0u;
#pragma unroll
                for (int k = k0; k < 8; ++k) if (!valid(k)) pend |= 1u << k;
                if ((pend & need) == 0u) break;
                __builtin_amdgcn_s_sleep(POLLSLEEP);
#pragma unroll
                for (int k = k0; k < 8; ++k)
                    if (pend & (1u << k))
                        asm volatile("global_load_dwordx4 %0, %2, %3 offset:%5 sc1\n\tglobal_load_dwordx4 %1, %2, %4 offset:%5 sc1"
                                     : "+v"(of[k][0]), "+v"(of[k][1])
                                     : "v"(lo16), "s"(osrc + (k >> 2) * 4096), "s"(osrc + 65536 + (k >> 2) * 4096), "i"((k & 3) * 1024) : "memory");
            }
        };
        asm volatile("s_waitcnt vmcnt(8)" : "+v"(of[0][0]), "+v"(of[0][1]), "+v"(of[1][0]), "+v"(of[1][1]), "+v"(of[2][0]), "+v"(of[2][1]),
                                            "+v"(of[3][0]), "+v"(of[3][1]) :: "memory");
        if (!(valid(0) && valid(1) && valid(2) && valid(3))) settle(0, 4);
        mul(0); mul(1); mul(2); mul(3);
        SBAR();
        settle(4, 8);
        auto mulq = [&](int q) {
#pragma unroll
            for (int k = 4; k < 8; ++k)
                acc[q] = MFMA32(__builtin_bit_cast(bf16x8, wf[k][q >> 1]), __builtin_bit_cast(bf16x8, of[k][q & 1]), acc[q]);
            SBAR();
        };
        auto put = [&](int q) {
#pragma unroll
            for (int p = 2 * q; p < 2 * q + 2; ++p) {
                if (p == w) {
#pragma unroll
                    for (int i = 0; i < 8; ++i) fin[i] = acc[q][8 * (p & 1) + i];
                } else {
                    char* dst = smem + (p * 7 + ((w - p - 1) & 7)) * 2048 + lane * 16;
#pragma unroll
                    for (int g = 0; g < 2; ++g) {
                        f32x4 v;
#pragma unroll
                        for (int i = 0; i < 4; ++i) v[i] = acc[q][8 * (p & 1) + 4 * g + i];
                        *(f32x4*)(dst + g * 1024) = v;
                    }
                }
            }
            SBAR();
        };
        mulq(0); mulq(1); put(0); mulq(2); put(1); mulq(3); put(2); put(3);
    }
    ST(2, 1);
    asm volatile("s_waitcnt lgkmcnt(0)\n\ts_barrier" ::: "memory");
    ST(2, 2);
#pragma unroll
    for (int p = 0; p < 7; ++p)
#pragma unroll
        for (int g = 0; g < 2; ++g) {
            const f32x4 v = *(const f32x4*)(smem + (w * 7 + p) * 2048 + g * 1024 + lane * 16);
#pragma unroll
            for (int i = 0; i < 4; ++i) fin[4 * g + i] += v[i];
        }
    const unsigned a0 = pk_bf16(fin[0], fin[1]), a1 = pk_bf16(fin[2], fin[3]);
    const unsigned b0 = pk_bf16(fin[4], fin[5]), b1 = pk_bf16(fin[6], fin[7]);
    auto r0 = __builtin_amdgcn_permlane32_swap(a0, b0, false, false);
    auto r1 = __builtin_amdgcn_permlane32_swap(a1, b1, false, false);
    u32x4 res = {r0[0], r1[0], r0[1], r1[1]};
    gst128(out + (size_t)(mt * 64 + ((w >> 1) & 1) * 32 + r) * 512 + nt * 64 + (w >> 2) * 32 + (w & 1) * 16 + h * 8, res);
    ST(2, 3);
    asm volatile("pf_text_end:");
}

extern "C" void kernel_launch(void* const* d_in, const int* in_sizes, int n_in, void* d_out, int out_size, void* d_ws,
                              size_t ws_size, hipStream_t stream) {
    const unsigned short* x    = (const unsigned short*)d_in[0];
    const unsigned short* wq   = (const unsigned short*)d_in[1];
    const unsigned short* wk   = (const unsigned short*)d_in[2];
    const unsigned short* wv   = (const unsigned short*)d_in[3];
    const unsigned short* woA  = (const unsigned short*)d_in[4];
    const unsigned short* wqkv = (const unsigned short*)d_in[6];
    const unsigned short* woB  = (const unsigned short*)d_in[7];
    const unsigned short* gw   = (const unsigned short*)d_in[8];
    char* ws = (char*)d_ws;
    (void)hipFuncSetAttribute((const void*)proj_kernel, hipFuncAttributeMaxDynamicSharedMemorySize, P_LDS);
    (void)hipFuncSetAttribute((const void*)attn_kernel, hipFuncAttributeMaxDynamicSharedMemorySize, A_LDS);
    proj_kernel<<<256, 512, P_LDS, stream>>>(x, wq, wk, wv, wqkv, gw, woA, woB, ws);
    attn_kernel<<<256, 512, A_LDS, stream>>>(ws, (unsigned short*)d_out);
}
```

```cpp
#include <hip/hip_runtime.h>
#include <hip/hip_bf16.h>
#include <stdint.h>

typedef short    bf16x8 __attribute__((ext_vector_type(8)));
typedef short    s16x4  __attribute__((ext_vector_type(4)));
typedef float    f32x16 __attribute__((ext_vector_type(16)));
typedef float    f32x4  __attribute__((ext_vector_type(4)));
typedef float    f32x2  __attribute__((ext_vector_type(2)));
typedef unsigned u32x4  __attribute__((ext_vector_type(4)));
typedef unsigned u32x16 __attribute__((ext_vector_type(16)));
typedef __bf16   bf16x2v __attribute__((ext_vector_type(2)));

#define DI __device__ __forceinline__
#define MFMA32(a, b, c) __builtin_amdgcn_mfma_f32_32x32x16_bf16((a), (b), (c), 0, 0, 0)
#define LDS_AS __attribute__((address_space(3)))

constexpr size_t WS_Q  = 0;
constexpr size_t WS_KV = WS_Q + (size_t)64 * 65536;
constexpr size_t WS_O  = WS_KV + (size_t)34 * 131072;
constexpr size_t WS_WO = WS_O + (size_t)64 * 65536;
constexpr size_t WS_G  = WS_WO + (size_t)1024 * 1024;
constexpr size_t WS_C  = WS_G + 8192;

constexpr float QSCALE = 0.25f * 1.4426950408889634f;
constexpr float REFOFF = 96.f;
constexpr unsigned SPIN_CAP = 1u << 20;

DI unsigned pk_bf16(float lo, float hi) {
    f32x2 v = {lo, hi};
    bf16x2v r = __builtin_convertvector(v, bf16x2v);
    return __builtin_bit_cast(unsigned, r);
}

DI void glds16(const void* g, unsigned lds_base) {
    unsigned sv;
    asm volatile("s_mov_b32 %0, m0\n\ts_mov_b32 m0, %2\n\ts_nop 0\n\tglobal_load_lds_dwordx4 %1, off\n\ts_mov_b32 m0, %0"
                 : "=&s"(sv) : "v"(g), "s"(lds_base) : "memory");
}
#define WAIT_BAR(N) asm volatile("s_waitcnt vmcnt(" #N ") lgkmcnt(0)\n\ts_barrier" ::: "memory")

DI void gst128(void* p, u32x4 v) { asm volatile("global_store_dwordx4 %0, %1, off sc1\n\ts_nop 1" :: "v"(p), "v"(v) : "memory"); }
DI bf16x8 lds_b128(unsigned addr) { return *(const LDS_AS bf16x8*)(uintptr_t)addr; }
DI s16x4 lds_tr(unsigned addr) {
    return __builtin_bit_cast(s16x4, __builtin_amdgcn_ds_read_tr16_b64_v4i16((LDS_AS s16x4*)(uintptr_t)addr));
}
DI bf16x8 cat8(s16x4 a, s16x4 b) { return __builtin_shufflevector(a, b, 0, 1, 2, 3, 4, 5, 6, 7); }

DI u32x4 pack8(const f32x16& a, int base, float sc) {
    u32x4 r;
    r[0] = pk_bf16(a[base + 0] * sc, a[base + 1] * sc);
    r[1] = pk_bf16(a[base + 2] * sc, a[base + 3] * sc);
    r[2] = pk_bf16(a[base + 4] * sc, a[base + 5] * sc);
    r[3] = pk_bf16(a[base + 6] * sc, a[base + 7] * sc);
    return r;
}

constexpr int P_XS = 0, P_WS = 16384, P_STAGE = 32768, P_WX = 4 * P_STAGE, P_GT = P_WX + 16384, P_WO = P_GT + 9216, P_CNT = P_WO + 4096,
              P_DUMP = P_CNT + 16, P_LDS = P_DUMP + 256;
#define SBAR() __builtin_amdgcn_sched_barrier(0)

struct PSrc { const unsigned short *xs0, *xs1, *wsr; int ldw; };
DI PSrc proj_src(const unsigned short* x, const unsigned short* wsrc, int ldw, int mt, int nt, int l, int lane) {
    PSrc s;
    const int row = 32 * l + (lane >> 3);
    s.xs0 = x + (size_t)(mt * 128 + row) * 512 + ((lane & 7) ^ ((row >> 1) & 7)) * 8;
    s.xs1 = x + (size_t)(mt * 128 + row + 8) * 512 + ((lane & 7) ^ (((row + 8) >> 1) & 7)) * 8;
    const int krow = 16 * l + (lane >> 4);
    s.wsr = wsrc + (size_t)krow * ldw + ((lane & 15) ^ (((lane >> 4) & 3) << 2)) * 8;
    s.ldw = ldw;
    return s;
}
DI void proj_issue(const PSrc& s, int c, int l, unsigned lds0) {
    const unsigned st = lds0 + (c & 3) * P_STAGE + l * 4096;
    glds16(s.xs0 + c * 64, st + P_XS);
    glds16(s.xs1 + c * 64, st + P_XS + 1024);
    glds16(s.xs0 + c * 64 + 16 * 512, st + P_XS + 2048);
    glds16(s.xs1 + c * 64 + 16 * 512, st + P_XS + 3072);
#pragma unroll
    for (int i = 0; i < 4; ++i) glds16(s.wsr + (size_t)(c * 64 + 4 * i) * s.ldw, st + P_WS + i * 1024);
}
DI float wave_sum63(float v) {
#define DPP_ADD(ctrl, rm) v += __int_as_float(__builtin_amdgcn_update_dpp(0, __float_as_int(v), ctrl, rm, 0xf, false))
    DPP_ADD(0x111, 0xf); DPP_ADD(0x112, 0xf); DPP_ADD(0x114, 0xf); DPP_ADD(0x118, 0xf);
    DPP_ADD(0x142, 0xa); DPP_ADD(0x143, 0xc);
#undef DPP_ADD
    return v;
}
DI void lds_spin(const char* smem, int off, unsigned target) {
    const volatile LDS_AS unsigned* p = (const volatile LDS_AS unsigned*)(uintptr_t)((unsigned)(uintptr_t)smem + off);
    while ((unsigned)__builtin_amdgcn_readfirstlane(*p) < target) __builtin_amdgcn_s_sleep(1);
    asm volatile("" ::: "memory");
}

template <bool ISV>
DI void proj_compute(const PSrc& src, const unsigned short* __restrict__ x, const unsigned short* __restrict__ wx,
                     const unsigned short* __restrict__ gw, const unsigned short* __restrict__ wo, char* __restrict__ ws,
                     int mt, int nt, int w, unsigned lds0, char* smem) {
    const int lane = threadIdx.x & 63;
    const int wm = w >> 1, wn = w & 1;
    const int r = lane & 31, h = lane >> 5;
    unsigned xo[4][2];
#pragma unroll
    for (int tt = 0; tt < 2; ++tt) {
        const int row = wm * 64 + tt * 32 + r;
#pragma unroll
        for (int ks = 0; ks < 4; ++ks) xo[ks][tt] = lds0 + P_XS + row * 128 + (((2 * ks + h) ^ ((row >> 1) & 7)) << 4);
    }
    const int q4 = (lane & 15) >> 2, p4 = lane & 3, g1 = (lane >> 4) & 1;
    unsigned wa[2];
#pragma unroll
    for (int ct = 0; ct < 2; ++ct)
        wa[ct] = lds0 + P_WS + (8 * h + q4) * 256 + ((4 * ((2 * wn + ct) ^ q4) + 2 * g1 + (p4 >> 1)) << 4) + (p4 & 1) * 8;
    f32x16 acc[2][2];
#pragma unroll
    for (int i = 0; i < 16; ++i) { acc[0][0][i] = 0.f; acc[0][1][i] = 0.f; acc[1][0][i] = 0.f; acc[1][1][i] = 0.f; }
    bf16x8 fx[2][4][2], fw[2][4][2];
    auto mm = [&](int bf, int ks) {
#pragma unroll
        for (int ct = 0; ct < 2; ++ct)
#pragma unroll
            for (int tt = 0; tt < 2; ++tt)
                acc[ct][tt] = ISV ? MFMA32(fx[bf][ks][tt], fw[bf][ks][ct], acc[ct][tt]) : MFMA32(fw[bf][ks][ct], fx[bf][ks][tt], acc[ct][tt]);
    };
    auto rd = [&](int bf, int ks, unsigned so) {
#pragma unroll
        for (int tt = 0; tt < 2; ++tt) fx[bf][ks][tt] = lds_b128(xo[ks][tt] + so);
#pragma unroll
        for (int ct = 0; ct < 2; ++ct) fw[bf][ks][ct] = cat8(lds_tr(wa[ct] + so + ks * 4096), lds_tr(wa[ct] + so + ks * 4096 + 1024));
    };
    auto step = [&](int bf, int c) {
        __builtin_amdgcn_s_waitcnt(0xC07F);
        asm volatile("s_barrier" ::: "memory");
        SBAR();
        const unsigned so = (c & 3) * P_STAGE;
#pragma unroll
        for (int ks = 0; ks < 4; ++ks) { rd(bf, ks, so); mm(bf ^ 1, ks); }
#pragma unroll
        for (int i = 0; i < 12; ++i) {
            __builtin_amdgcn_sched_group_barrier(0x008, 1, 0);
            __builtin_amdgcn_sched_group_barrier(0x002, 1, 0);
            __builtin_amdgcn_sched_group_barrier(0x100, 2, 0);
        }
        __builtin_amdgcn_sched_group_barrier(0x008, 4, 0);
        SBAR();
    };
    asm volatile("" : "+v"(xo[0][0]), "+v"(xo[0][1]), "+v"(xo[1][0]), "+v"(xo[1][1]), "+v"(xo[2][0]), "+v"(xo[2][1]),
                      "+v"(xo[3][0]), "+v"(xo[3][1]), "+v"(wa[0]), "+v"(wa[1]),
                      "+v"(acc[0][0]), "+v"(acc[0][1]), "+v"(acc[1][0]), "+v"(acc[1][1]));
    asm volatile("s_waitcnt vmcnt(0)" ::: "memory");
    if (lane == 0) atomicAdd((unsigned*)(smem + P_CNT), 1u);
    lds_spin(smem, P_CNT, 4u);
    rd(0, 0, 0); rd(0, 1, 0); rd(0, 2, 0); rd(0, 3, 0);
    SBAR();
    {
        if (nt >= 8) {
            const unsigned short* es = wx + (size_t)((lane >> 5) * 64 + 16 * w) * 16 + (lane & 31) * 8;
#pragma unroll
            for (int i = 0; i < 4; ++i) glds16(es + i * 2048, lds0 + P_WX + w * 4096 + i * 1024);
        }
        const unsigned short* px = x + (size_t)(mt * 128 + nt * 8 + 2 * w) * 512 + lane * 8;
        glds16(px, lds0 + P_GT + w * 2048);
        glds16(px + 512, lds0 + P_GT + w * 2048 + 1024);
        glds16(gw + lane * 8, lds0 + P_GT + 8192);
        glds16(wo + (size_t)(4 * w + (lane >> 4)) * 512 + ((lane & 15) ^ (((lane >> 4) & 3) << 2)) * 8, lds0 + P_WO + w * 1024);
    }

#pragma unroll 1
    for (int c = 1; c < 7; c += 2) { step(1, c); step(0, c + 1); }
    asm volatile("s_waitcnt vmcnt(0)" ::: "memory");
    step(1, 7);
#pragma unroll
    for (int ct = 0; ct < 2; ++ct)
#pragma unroll
        for (int tt = 0; tt < 2; ++tt)
#pragma unroll
            for (int ks = 0; ks < 4; ++ks)
                acc[ct][tt] = ISV ? MFMA32(fx[1][ks][tt], fw[1][ks][ct], acc[ct][tt]) : MFMA32(fw[1][ks][ct], fx[1][ks][tt], acc[ct][tt]);
#pragma unroll
    for (int ct = 0; ct < 2; ++ct)
#pragma unroll
        for (int tt = 0; tt < 2; ++tt) {
            const int hd0 = (nt & 3) * 8 + wn * 4 + ct * 2;
            const int tile = mt * 4 + wm * 2 + tt;
            if (ISV) {
                char* dst = ws + WS_KV + (size_t)(hd0 + (r >> 4)) * 131072 + tile * 2048 + 1024 + h * 256 + (r & 15) * 16;
                gst128(dst, pack8(acc[ct][tt], 0, 1.f));
                gst128(dst + 512, pack8(acc[ct][tt], 8, 1.f));
            } else {
                const bool isq = nt < 8;
                const float sc = isq ? QSCALE : 1.f;
                const size_t hs = isq ? 65536 : 131072;
                char* dst = isq ? (ws + WS_Q + (size_t)((nt >> 2) * 32 + hd0) * 65536 + tile * 1024 + lane * 16)
                                : (ws + WS_KV + (size_t)hd0 * 131072 + tile * 2048 + lane * 16);
                gst128(dst, pack8(acc[ct][tt], 0, sc));
                gst128(dst + hs, pack8(acc[ct][tt], 8, sc));
            }
        }
}

template <int XM>
DI void proj_loader(const PSrc& src, const unsigned short* __restrict__ x, const unsigned short* __restrict__ wk,
                    const unsigned short* __restrict__ wv, const unsigned short* __restrict__ gw, char* __restrict__ ws,
                    int mt, int nt, int l, unsigned lds0, char* smem) {
    const int lane = threadIdx.x & 63;
    const int r = lane & 31, h = lane >> 5;
    constexpr bool isx = XM != 0, isv = XM == 2;
    const int xtt = nt & 3;
    proj_issue(src, 2, l, lds0);
    const int q4 = (lane & 15) >> 2, p4 = lane & 3;
    const unsigned ea = lds0 + P_WX + l * 4096 + (8 * h + q4) * 32 + p4 * 8;
    const int xrow = xtt * 32 + r;
    const unsigned exa = lds0 + P_XS + xrow * 128 + (((2 * l + h) ^ ((xrow >> 1) & 7)) << 4);
    const int gtok = mt * 128 + nt * 8 + 2 * l;
    u32x4 gx0, gx1, gv;
    bf16x8 xe[8];
    if (isx) { lds_spin(smem, P_CNT, 4u); xe[0] = lds_b128(exa); }
#pragma unroll
    for (int c = 1; c < 8; ++c) {
        if (c < 7) WAIT_BAR(8); else WAIT_BAR(0);
        if (isx) xe[c] = lds_b128(exa + (c & 3) * P_STAGE);
        if (c < 6) proj_issue(src, c + 2, l, lds0);
    }
    gx0 = *(const u32x4*)(smem + P_GT + l * 2048 + lane * 16);
    gx1 = *(const u32x4*)(smem + P_GT + l * 2048 + 1024 + lane * 16);
    gv  = *(const u32x4*)(smem + P_GT + 8192 + lane * 16);
    {
        const int g1 = (lane >> 4) & 1;
        const unsigned woa = lds0 + P_WO + (4 * h + q4) * 256 + ((4 * (l ^ q4) + 2 * g1 + (p4 >> 1)) << 4) + (p4 & 1) * 8;
        const bf16x8 f = cat8(lds_tr(woa), lds_tr(woa + 2048));
        gst128(ws + WS_WO + (size_t)(blockIdx.x * 4 + l) * 1024 + lane * 16, __builtin_bit_cast(u32x4, f));
    }
    {
        float s0 = 0.f, s1 = 0.f;
#pragma unroll
        for (int i = 0; i < 4; ++i) {
            const float wl = __uint_as_float(gv[i] << 16), wh = __uint_as_float(gv[i] & 0xffff0000u);
            s0 += __uint_as_float(gx0[i] << 16) * wl + __uint_as_float(gx0[i] & 0xffff0000u) * wh;
            s1 += __uint_as_float(gx1[i] << 16) * wl + __uint_as_float(gx1[i] & 0xffff0000u) * wh;
        }
        s0 = wave_sum63(s0); s1 = wave_sum63(s1);
        if (lane == 63) {
            f32x2 g = {__builtin_amdgcn_rcpf(1.f + __expf(-s0)), __builtin_amdgcn_rcpf(1.f + __expf(-s1))};
            *(f32x2*)((float*)(ws + WS_G) + gtok) = g;
        }
    }
    f32x16 acce;
#pragma unroll
    for (int i = 0; i < 16; ++i) acce[i] = 0.f;
    if (isx) {
        __builtin_amdgcn_s_setprio(3);
#pragma unroll
        for (int c = 0; c < 8; ++c) {
            const bf16x8 we = cat8(lds_tr(ea + c * 512), lds_tr(ea + c * 512 + 128));
            acce = isv ? MFMA32(xe[c], we, acce) : MFMA32(we, xe[c], acce);
        }
    }
    if (isx) {
        f32x4* red = (f32x4*)smem;
#pragma unroll
        for (int g = 0; g < 4; ++g) {
            f32x4 v = {acce[4 * g], acce[4 * g + 1], acce[4 * g + 2], acce[4 * g + 3]};
            red[(l * 4 + g) * 64 + lane] = v;
        }
        unsigned old = 0;
        asm volatile("s_waitcnt lgkmcnt(0)" ::: "memory");
        if (lane == 0) old = atomicAdd((unsigned*)(smem + P_CNT), 1u);
        old = __builtin_amdgcn_readfirstlane(old);
        if (old == 7) {
            asm volatile("" ::: "memory");
#pragma unroll
            for (int g = 0; g < 4; ++g) {
                const f32x4 v = red[g * 64 + lane] + red[(4 + g) * 64 + lane] + red[(8 + g) * 64 + lane] + red[(12 + g) * 64 + lane];
#pragma unroll
                for (int i = 0; i < 4; ++i) acce[4 * g + i] = v[i];
            }
            char* base = ws + WS_KV + (size_t)32 * 131072 + (mt * 4 + xtt) * 2048;
            if (isv) {
                if (r < 16) {
                    gst128(base + 1024 + h * 256 + r * 16, pack8(acce, 0, 1.f));
                    gst128(base + 1536 + h * 256 + r * 16, pack8(acce, 8, 1.f));
                }
            } else {
                gst128(base + lane * 16, pack8(acce, 0, 1.f));
            }
        }
    }
}

__global__ __launch_bounds__(512, 2) void proj_kernel(const unsigned short* __restrict__ x, const unsigned short* __restrict__ wq,
                                                      const unsigned short* __restrict__ wk, const unsigned short* __restrict__ wv,
                                                      const unsigned short* __restrict__ wqkv, const unsigned short* __restrict__ gw,
                                                      const unsigned short* __restrict__ woA, const unsigned short* __restrict__ woB,
                                                      char* __restrict__ ws) {
    extern __shared__ __attribute__((aligned(16))) char smem[];
    const unsigned lds0 = (unsigned)(uintptr_t)smem;
    const int w = __builtin_amdgcn_readfirstlane(threadIdx.x >> 6);
    const int b = blockIdx.x, xcd = b & 7, slot = b >> 3;
    const int mt = (xcd & 3) * 4 + (slot & 3), nt = (xcd >> 2) * 8 + (slot >> 2);
    if (w == 7) {
        unsigned long long pc; int len;
        asm volatile("s_getpc_b64 %0\n\tpf_text_begin:\n\ts_mov_b32 %1, pf_text_end-pf_text_begin" : "=s"(pc), "=s"(len));
        int off = ((slot & 7) * 32 + (int)(threadIdx.x & 31)) * 128;
        off = off < len ? off : 0;
        unsigned sv;
        asm volatile("s_mov_b32 %0, m0\n\ts_mov_b32 m0, %3\n\ts_nop 0\n\tglobal_load_lds_dword %1, %2\n\ts_mov_b32 m0, %0"
                     : "=&s"(sv) : "v"(off), "s"(pc), "s"(lds0 + P_DUMP) : "memory");
    }
    if (threadIdx.x == 0) *(unsigned*)(smem + P_CNT) = 0u;
    const unsigned short* wsrc = (nt < 4) ? (wq + nt * 128) : (wqkv + (nt - 4) * 128);
    const PSrc src = proj_src(x, wsrc, (nt < 4) ? 512 : 1536, mt, nt, w & 3, threadIdx.x & 63);
    const unsigned short* wo = ((b < 128) ? woA : woB) + (size_t)((b >> 2) & 31) * 16 * 512 + (b & 3) * 128;
    proj_issue(src, w >> 2, w & 3, lds0);
    asm volatile("s_waitcnt lgkmcnt(0)\n\ts_barrier" ::: "memory");
    if (w >= 4) {
        if (nt >= 12)     proj_loader<2>(src, x, wk, wv, gw, ws, mt, nt, w - 4, lds0, smem);
        else if (nt >= 8) proj_loader<1>(src, x, wk, wv, gw, ws, mt, nt, w - 4, lds0, smem);
        else              proj_loader<0>(src, x, wk, wv, gw, ws, mt, nt, w - 4, lds0, smem);
        if (b == 0 && w == 4 && (threadIdx.x & 63) < 4) *(unsigned*)(ws + WS_C + (threadIdx.x & 63) * 256) = 0u;
    } else if (nt >= 12) proj_compute<true>(src, x, wv, gw, wo, ws, mt, nt, w, lds0, smem);
    else                 proj_compute<false>(src, x, wk, gw, wo, ws, mt, nt, w, lds0, smem);
}

constexpr int A_KV = 0, A_CONST = 131072, A_LDS = A_CONST + 8192;

DI float max16(const f32x16& s) {
    float a = fmaxf(fmaxf(s[0], s[1]), s[2]), b = fmaxf(fmaxf(s[3], s[4]), s[5]);
    a = fmaxf(fmaxf(a, s[6]), s[7]);   b = fmaxf(fmaxf(b, s[8]), s[9]);
    a = fmaxf(fmaxf(a, s[10]), s[11]); b = fmaxf(fmaxf(b, s[12]), s[13]);
    a = fmaxf(fmaxf(a, s[14]), s[15]);
    return fmaxf(a, b);
}
DI float half_max(float m) {
    auto rr = __builtin_amdgcn_permlane32_swap(__float_as_uint(m), __float_as_uint(m), false, false);
    return fmaxf(__uint_as_float(rr[0]), __uint_as_float(rr[1]));
}

#define R_SA "192"
#define R_SB "208"
#define EX(X, i) "v_exp_f32 v[" X "+" #i "], v[" X "+" #i "]\n\t"
#define CV(X, j, a, b) "v_cvt_pk_bf16_f32 v[224+" #j "], v[" X "+" #a "], v[" X "+" #b "]\n\t"
#define HALF(X, Y, OX, OY, NY, QY, WA, RA, WB, RB, WC, RC)                                        \
    EX(X, 0) EX(X, 1) WA                                                                          \
    "v_mfma_f32_32x32x16_bf16 " OY ", %[v1], v[228:231], " OY "\n\t" RA                           \
    EX(X, 2) EX(X, 3) CV(X, 0, 0, 1) EX(X, 4) EX(X, 5) CV(X, 1, 2, 3) WB                          \
    "v_mfma_f32_32x32x16_bf16 v[" Y ":" Y "+15], %[kf], " QY ", " NY "\n\t" RB                    \
    EX(X, 6) EX(X, 7) CV(X, 2, 4, 5) EX(X, 8) EX(X, 9) CV(X, 3, 6, 7) EX(X, 10) EX(X, 11) WC      \
    "v_mfma_f32_32x32x16_bf16 " OX ", %[v0], v[224:227], " OX "\n\t" RC                           \
    CV(X, 4, 8, 9) EX(X, 12) EX(X, 13) CV(X, 5, 10, 11) EX(X, 14) EX(X, 15) CV(X, 6, 12, 13) CV(X, 7, 14, 15)
#define TILE(OFF_V1, OFF_KN, OFF_V0N, P0, P1)                                                     \
    HALF(R_SA, R_SB, "%[oa]", "%[ob]", "%[nb]", "%[qb]",                                          \
         "", "ds_read_b128 %[v1], %[va] offset:" OFF_V1 "\n\t" P0,                                \
         "", "ds_read_b128 %[kf], %[ka] offset:" OFF_KN "\n\t",                                   \
         "s_waitcnt lgkmcnt(2)\n\t", "")                                                          \
    HALF(R_SB, R_SA, "%[ob]", "%[oa]", "%[na]", "%[qa]",                                          \
         "s_waitcnt lgkmcnt(1)\n\t", P1,                                                          \
         "s_waitcnt lgkmcnt(0)\n\t", "",                                                          \
         "", "ds_read_b128 %[v0], %[va] offset:" OFF_V0N "\n\t")
#define KVDMA                                                                                     \
    "global_load_lds_dwordx4 %[lo], s[40:41]\n\t"                                                 \
    "s_add_u32 s40, s40, 0x2000\n\ts_addc_u32 s41, s41, 0\n\ts_add_u32 m0, m0, 0x2000\n\ts_nop 0\n\t"
#define WOLD(R)                                                                                   \
    "global_load_dwordx4 v[" #R ":" #R "+3], %[lo], s[42:43]\n\t"                                 \
    "global_load_dwordx4 v[" #R "+4:" #R "+7], %[lo], s[42:43] offset:1024\n\t"                   \
    "s_add_u32 s42, s42, 0x4000\n\ts_addc_u32 s43, s43, 0\n\t"
#define ATTN_ASM(PY, PO)                                                                          \
    asm volatile(                                                                                 \
        "s_mov_b32 s44, m0\n\ts_mov_b32 m0, %[kd]\n\ts_mov_b64 s[40:41], %[kb]\n\ts_mov_b64 s[42:43], %[wb]\n\ts_cmp_lg_u32 %[yg], 0\n\ts_cselect_b64 vcc, -1, 0\n\t"               \
        "ds_read_b128 %[kf], %[ka]\n\t"                                                           \
        "ds_read_b128 %[v1], %[va]\n\t"                                                           \
        "ds_read_b128 %[v0], %[va]\n\t"                                                           \
        "v_mov_b32 v228, 0\n\tv_mov_b32 v229, 0\n\tv_mov_b32 v230, 0\n\tv_mov_b32 v231, 0\n\t"    \
        "s_waitcnt lgkmcnt(0)\n\t"                                                                \
        "v_mfma_f32_32x32x16_bf16 v[192:207], %[kf], %[qa], %[na]\n\t"                            \
        "s_nop 7\n\ts_nop 7\n\t"                                                                  \
        "L_loop_%=:\n\t"                                                                          \
        "s_bitcmp1_b32 %[mk], %[cnt]\n\t"                                                         \
        "s_cbranch_scc1 L_sp_%=\n\t"                                                              \
        "L_go_%=:\n\t"                                                                            \
        TILE("512", "2048", "2048", "s_setprio 0\n\t", PY)                                        \
        TILE("2560", "4096", "4096", PO, "")                                                      \
        TILE("4608", "6144", "6144", "", "")                                                      \
        HALF(R_SA, R_SB, "%[oa]", "%[ob]", "%[nb]", "%[qb]",                                      \
             "", "ds_read_b128 %[v1], %[va] offset:6656\n\t",                                     \
             "", "ds_read_b128 %[kf], %[ka] offset:8192\n\tv_add_u32 %[va], %[va], %[vs]\n\t",    \
             "s_waitcnt lgkmcnt(2)\n\t", "v_add_u32 %[ka], 0x2000, %[ka]\n\t")                    \
        HALF(R_SB, R_SA, "%[ob]", "%[oa]", "%[na]", "%[qa]",                                      \
             "s_waitcnt lgkmcnt(1)\n\t", "s_add_u32 %[cnt], %[cnt], 1\n\t",                       \
             "s_waitcnt lgkmcnt(0)\n\t", "s_and_b32 s45, %[cnt], 15\n\t",                           \
             "", "ds_read_b128 %[v0], %[va]\n\t")                                                 \
        "s_cbranch_scc1 L_loop_%=\n\t"                                                            \
        "s_branch L_end_%=\n\t"                                                                   \
        "L_sp_%=:\n\t"                                                                            \
        "s_cmp_gt_u32 %[cnt], 2\n\t"                                                              \
        "s_cbranch_scc1 L_s3_%=\n\t"                                                              \
        KVDMA KVDMA KVDMA KVDMA                                                                   \
        "s_branch L_go_%=\n\t"                                                                    \
        "L_s3_%=:\n\t"                                                                            \
        "s_cmp_gt_u32 %[cnt], 3\n\t"                                                              \
        "s_cbranch_scc1 L_s4_%=\n\t"                                                              \
        "s_waitcnt vmcnt(0)\n\t"                                                                  \
        "s_barrier\n\t"                                                                           \
        "s_branch L_go_%=\n\t"                                                                    \
        "L_s4_%=:\n\t"                                                                            \
        "s_cmp_gt_u32 %[cnt], 8\n\t"                                                              \
        "s_cbranch_scc1 L_s5_%=\n\t"                                                              \
        "v_mov_b32 v232, -1\n\tv_mov_b32 v233, -1\n\tv_mov_b32 v234, -1\n\tv_mov_b32 v235, -1\n\t" \
        "s_mov_b64 s[40:41], %[ob2]\n\t"                                                          \
        "global_store_dwordx4 %[lo], v[232:235], s[40:41] sc1\n\t"                                \
        "s_add_u32 s40, s40, 0x10000\n\ts_addc_u32 s41, s41, 0\n\t"                               \
        "global_store_dwordx4 %[lo], v[232:235], s[40:41] sc1\n\t"                                \
        WOLD(128) WOLD(136) WOLD(144) WOLD(152) WOLD(160) WOLD(168) WOLD(176) WOLD(184)           \
        "s_branch L_go_%=\n\t"                                                                    \
        "L_s5_%=:\n\t"                                                                            \
        "s_cmp_gt_u32 %[cnt], 11\n\t"                                                             \
        "s_cbranch_scc1 L_s6_%=\n\t"                                                              \
        "s_waitcnt vmcnt(0)\n\t"                                                                  \
        "s_mov_b64 exec, 1\n\t"                                                                   \
        "v_mov_b32 v232, 1\n\t"                                                                   \
        "global_atomic_add %[pf], v232, off sc1\n\t"                                              \
        "s_mov_b64 exec, -1\n\t"                                                                  \
        "s_branch L_go_%=\n\t"                                                                    \
        "L_s6_%=:\n\t"                                                                            \
        "global_load_dword %[cv], %[pf], off sc1\n\t"                                             \
        "s_branch L_go_%=\n\t"                                                                    \
        "L_end_%=:\n\t"                                                                           \
        "s_nop 1\n\t"                                                                             \
        "v_mfma_f32_32x32x16_bf16 %[ob], %[v1], v[228:231], %[ob]\n\t"                            \
        "s_waitcnt vmcnt(0) lgkmcnt(0)\n\t"                                                       \
        "s_mov_b32 m0, s44\n\t"                                                                \
        "s_nop 7\n\ts_nop 7\n\t"                                                                  \
        : [oa] "+v"(oa), [ob] "+v"(ob), [ka] "+v"(ka), [va] "+v"(va), [cnt] "+s"(cnt), [cv] "+v"(cv), \
          [kf] "=&v"(kf), [v0] "=&v"(v0), [v1] "=&v"(v1),                                         \
          "+{v[128:143]}"(w0), "+{v[144:159]}"(w1), "+{v[160:175]}"(w2), "+{v[176:191]}"(w3)      \
        : [qa] "v"(qa), [qb] "v"(qb), [na] "v"(na), [nb] "v"(nb), [vs] "v"(vstep), [yg] "s"(yg), [pf] "v"(pf), \
          [lo] "v"(lo), [kb] "s"(kb), [kd] "s"(kd), [wb] "s"(wb), [ob2] "s"(ob2), [mk] "s"(mk)                                                \
        : "memory", "scc", "vcc", "s40", "s41", "s42", "s43", "s44", "s45",                                                                        \
          "v192", "v193", "v194", "v195", "v196", "v197", "v198", "v199", "v200", "v201", "v202", "v203", "v204", "v205", "v206", "v207", \
          "v208", "v209", "v210", "v211", "v212", "v213", "v214", "v215", "v216", "v217", "v218", "v219", "v220", "v221", "v222", "v223", \
          "v224", "v225", "v226", "v227", "v228", "v229", "v230", "v231", "v232", "v233", "v234", "v235")

DI void attn_main(bool young, bool first, unsigned ka, unsigned va, unsigned vstep, const bf16x8& qa, const bf16x8& qb, const f32x16& na,
                  const f32x16& nb, f32x16& oa, f32x16& ob, const char* pf, unsigned lo, const char* kb, unsigned kd,
                  const char* wb, const char* ob2, u32x16& w0, u32x16& w1, u32x16& w2, u32x16& w3, unsigned& cv) {
    bf16x8 kf, v0, v1;
    int cnt = __builtin_amdgcn_readfirstlane(first ? 0 : 16);
    const int yg = __builtin_amdgcn_readfirstlane(young ? 1 : 0);
    const int mk = __builtin_amdgcn_readfirstlane(young ? 0x422f : 0x448f);
    ATTN_ASM("s_cbranch_vccz L_py_%=\n\ts_setprio 1\n\tL_py_%=:\n\t", "s_setprio 1\n\t");
}

__global__ __launch_bounds__(512, 2) void attn_kernel(char* __restrict__ ws, unsigned short* __restrict__ out) {
    extern __shared__ __attribute__((aligned(16))) char smem[];
    const unsigned lds0 = (unsigned)(uintptr_t)smem;
    const int tid = threadIdx.x, lane = tid & 63;
    const int w = __builtin_amdgcn_readfirstlane(tid >> 6);
    const int r = lane & 31, h = lane >> 5;
    const int b = blockIdx.x, xcd = b & 7, slot = b >> 3;
    const int isB = (slot < 16);
    const int hd = xcd * 4 + ((slot & 15) >> 2);
    const int hh = isB ? 32 + hd : hd;
    const int kvh = isB ? hd : 32;
    const int tile = (slot & 3) * 16 + 2 * w;

    bf16x8 qa, qb;
    {
        const char* qsrc = ws + WS_Q + (size_t)hh * 65536 + tile * 1024 + lane * 16;
        asm volatile("global_load_dwordx4 %0, %2, off\n\tglobal_load_dwordx4 %1, %2, off offset:1024"
                     : "=&v"(qa), "=&v"(qb) : "v"(qsrc) : "memory");
    }
    const char* kvsrc = ws + WS_KV + (size_t)kvh * 131072 + w * 1024 + lane * 16;
#pragma unroll
    for (int i = 0; i < 4; ++i) glds16(kvsrc + i * 8192, lds0 + A_KV + i * 8192 + w * 1024);
    {
        const unsigned one = ((tid & 15) == 0 || (tid & 15) == 4) ? 0x3f803f80u : 0u;
        u32x4 c = {one, one, one, one};
        *(u32x4*)(smem + A_CONST + tid * 16) = c;
    }
    const unsigned kaddr = lds0 + A_KV + lane * 16;
    const unsigned vaddr = (r < 16) ? (lds0 + A_KV + 1024 + h * 256 + r * 16) : (lds0 + A_CONST + (r - 16) * 16);
    const unsigned vstep = (r < 16) ? 8192u : 0u;

    float ga, gb;
    {
        const float* gsrc = (const float*)(ws + WS_G) + tile * 32 + r;
        asm volatile("global_load_dword %0, %2, off\n\tglobal_load_dword %1, %2, off offset:128" : "=&v"(ga), "=&v"(gb) : "v"(gsrc) : "memory");
    }
    const char* kb = ws + WS_KV + (size_t)kvh * 131072 + 32768 + w * 1024;
    const unsigned kd = lds0 + A_KV + 32768 + w * 1024;
    const int qblk = slot & 3;
    const int mt = qblk * 8 + (hh >> 3), nt = hh & 7;
    char* obase = ws + WS_O + (size_t)tile * 65536 + hh * 1024;
    char* odst = obase + lane * 16;
    const char* cnt = ws + WS_C + qblk * 256;
    unsigned cv = 0u;
    const char* wb = ws + WS_WO + (size_t)(w * 8) * 16384 + (nt * 2) * 1024;
    u32x16 w0, w1, w2, w3;
    asm volatile("" : "={v[128:143]}"(w0), "={v[144:159]}"(w1), "={v[160:175]}"(w2), "={v[176:191]}"(w3));
    f32x16 zero;
#pragma unroll
    for (int i = 0; i < 16; ++i) zero[i] = 0.f;
    f32x16 na, nb, oa = zero, ob = zero;
    asm volatile("" : "+v"(oa), "+v"(ob), "+s"(kb), "+s"(wb), "+s"(obase), "+v"(cnt));
    asm volatile("s_waitcnt vmcnt(2) lgkmcnt(0)\n\ts_barrier" : "+v"(qa), "+v"(qb) :: "memory");
    {
        const bf16x8 k0 = lds_b128(kaddr);
        const f32x16 sa = MFMA32(k0, qa, zero);
        const f32x16 sb = MFMA32(k0, qb, zero);
        SBAR();
        const float ma = -half_max(max16(sa)) - REFOFF, mb = -half_max(max16(sb)) - REFOFF;
#pragma unroll
        for (int i = 0; i < 16; ++i) { na[i] = ma; nb[i] = mb; }
    }
    attn_main(w >= 4, true, kaddr, vaddr, vstep, qa, qb, na, nb, oa, ob, cnt, lane * 16, kb, kd, wb, obase, w0, w1, w2, w3, cv);
    asm volatile("" : "+v"(ga), "+v"(gb));

    {
        float xa = fabsf(oa[0]), xb = fabsf(ob[0]), ta = oa[0], tb = ob[0];
#pragma unroll
        for (int i = 1; i < 8; ++i) { xa = fmaxf(xa, fabsf(oa[i])); xb = fmaxf(xb, fabsf(ob[i])); ta += oa[i]; tb += ob[i]; }
        const bool bad = !(oa[8] < 1.329228e36f) || !(ob[8] < 1.329228e36f) || !(fabsf(ta) <= 3.0e38f) || !(fabsf(tb) <= 3.0e38f) ||
                         !(xa > 7.7e-34f) || !(xb > 7.7e-34f);
        if (__any(bad)) {
            float ma = -INFINITY, mb = -INFINITY;
#pragma unroll 1
            for (int t = 0; t < 64; ++t) {
                const bf16x8 kf = lds_b128(kaddr + t * 2048);
                ma = fmaxf(ma, max16(MFMA32(kf, qa, zero)));
                mb = fmaxf(mb, max16(MFMA32(kf, qb, zero)));
            }
            ma = -half_max(ma); mb = -half_max(mb);
#pragma unroll
            for (int i = 0; i < 16; ++i) { na[i] = ma; nb[i] = mb; }
            oa = zero; ob = zero;
            attn_main(false, false, kaddr, vaddr, vstep, qa, qb, na, nb, oa, ob, cnt, lane * 16, kb, kd, wb, obase, w0, w1, w2, w3, cv);
        }
    }
    const float sa = (isB ? 1.f - ga : ga) * __builtin_amdgcn_rcpf(oa[8]), sb = (isB ? 1.f - gb : gb) * __builtin_amdgcn_rcpf(ob[8]);
    {
        u32x4 pa = pack8(oa, 0, sa), pb = pack8(ob, 0, sb);
#pragma unroll
        for (int i = 0; i < 4; ++i) { pa[i] = min(pa[i], 0xfffffffeu); pb[i] = min(pb[i], 0xfffffffeu); }
        gst128(odst, pa);
        gst128(odst + 65536, pb);
    }
    for (unsigned spins = 0; spins < SPIN_CAP && (unsigned)__builtin_amdgcn_readfirstlane(cv) < 512u; ++spins) {
        if ((unsigned)__builtin_amdgcn_readfirstlane(__hip_atomic_load((const unsigned*)cnt, __ATOMIC_RELAXED, __HIP_MEMORY_SCOPE_AGENT)) >= 512u) break;
        __builtin_amdgcn_s_sleep(2);
    }
    asm volatile("s_barrier" ::: "memory");
    u32x4 wf[8][2];
#define WSUB(W, k) wf[k][0] = __builtin_shufflevector(W, W, 0, 1, 2, 3); wf[k][1] = __builtin_shufflevector(W, W, 4, 5, 6, 7); \
                   wf[k + 1][0] = __builtin_shufflevector(W, W, 8, 9, 10, 11); wf[k + 1][1] = __builtin_shufflevector(W, W, 12, 13, 14, 15);
    WSUB(w0, 0) WSUB(w1, 2) WSUB(w2, 4) WSUB(w3, 6)
    f32x16 acc[4] = {zero, zero, zero, zero};
    {
        const char* osrc = ws + WS_O + (size_t)(mt * 2) * 65536 + (w * 8) * 1024;
        const unsigned lo16 = lane * 16;
        u32x4 of[8][2];
        auto valid = [&](int k) -> bool {
            const unsigned m = max(max(max(of[k][0][0], of[k][0][1]), max(of[k][0][2], of[k][0][3])),
                                   max(max(of[k][1][0], of[k][1][1]), max(of[k][1][2], of[k][1][3])));
            return !__any(m == 0xffffffffu);
        };
        auto mul = [&](int k) {
#pragma unroll
            for (int q = 0; q < 4; ++q)
                acc[q] = MFMA32(__builtin_bit_cast(bf16x8, wf[k][q >> 1]), __builtin_bit_cast(bf16x8, of[k][q & 1]), acc[q]);
        };
#pragma unroll
        for (int k = 0; k < 8; ++k)
            asm volatile("global_load_dwordx4 %0, %2, %3 offset:%5 sc1\n\tglobal_load_dwordx4 %1, %2, %4 offset:%5 sc1"
                         : "=&v"(of[k][0]), "=&v"(of[k][1])
                         : "v"(lo16), "s"(osrc + (k >> 2) * 4096), "s"(osrc + 65536 + (k >> 2) * 4096), "i"((k & 3) * 1024) : "memory");
        auto settle = [&](int k0, int k1) {
            const unsigned need = ((1u << k1) - 1u) & ~((1u << k0) - 1u);
            for (unsigned spins = 0; spins < SPIN_CAP; ++spins) {
                asm volatile("s_waitcnt vmcnt(0)"
                             : "+v"(of[0][0]), "+v"(of[0][1]), "+v"(of[1][0]), "+v"(of[1][1]), "+v"(of[2][0]), "+v"(of[2][1]), "+v"(of[3][0]), "+v"(of[3][1]),
                               "+v"(of[4][0]), "+v"(of[4][1]), "+v"(of[5][0]), "+v"(of[5][1]), "+v"(of[6][0]), "+v"(of[6][1]), "+v"(of[7][0]), "+v"(of[7][1])
                             :: "memory");
                unsigned pend = 0u;
#pragma unroll
                for (int k = k0; k < 8; ++k) if (!valid(k)) pend |= 1u << k;
                if ((pend & need) == 0u) break;
                __builtin_amdgcn_s_sleep(2);
#pragma unroll
                for (int k = k0; k < 8; ++k)
                    if (pend & (1u << k))
                        asm volatile("global_load_dwordx4 %0, %2, %3 offset:%5 sc1\n\tglobal_load_dwordx4 %1, %2, %4 offset:%5 sc1"
                                     : "+v"(of[k][0]), "+v"(of[k][1])
                                     : "v"(lo16), "s"(osrc + (k >> 2) * 4096), "s"(osrc + 65536 + (k >> 2) * 4096), "i"((k & 3) * 1024) : "memory");
            }
        };
        asm volatile("s_waitcnt vmcnt(8)" : "+v"(of[0][0]), "+v"(of[0][1]), "+v"(of[1][0]), "+v"(of[1][1]), "+v"(of[2][0]), "+v"(of[2][1]),
                                            "+v"(of[3][0]), "+v"(of[3][1]) :: "memory");
        if (!(valid(0) && valid(1) && valid(2) && valid(3))) settle(0, 4);
        mul(0); mul(1); mul(2); mul(3);
        SBAR();
        settle(4, 8);
        mul(4); mul(5); mul(6); mul(7);
    }
    float fin[8];
#pragma unroll
    for (int p = 0; p < 8; ++p) {
        if (p == w) {
#pragma unroll
            for (int i = 0; i < 8; ++i) fin[i] = acc[p >> 1][8 * (p & 1) + i];
        } else {
            char* dst = smem + (p * 7 + ((w - p - 1) & 7)) * 2048 + lane * 16;
#pragma unroll
            for (int g = 0; g < 2; ++g) {
                f32x4 v;
#pragma unroll
                for (int i = 0; i < 4; ++i) v[i] = acc[p >> 1][8 * (p & 1) + 4 * g + i];
                *(f32x4*)(dst + g * 1024) = v;
            }
        }
    }
    asm volatile("s_waitcnt lgkmcnt(0)\n\ts_barrier" ::: "memory");
#pragma unroll
    for (int p = 0; p < 7; ++p)
#pragma unroll
        for (int g = 0; g < 2; ++g) {
            const f32x4 v = *(const f32x4*)(smem + (w * 7 + p) * 2048 + g * 1024 + lane * 16);
#pragma unroll
            for (int i = 0; i < 4; ++i) fin[4 * g + i] += v[i];
        }
    const unsigned a0 = pk_bf16(fin[0], fin[1]), a1 = pk_bf16(fin[2], fin[3]);
    const unsigned b0 = pk_bf16(fin[4], fin[5]), b1 = pk_bf16(fin[6], fin[7]);
    auto r0 = __builtin_amdgcn_permlane32_swap(a0, b0, false, false);
    auto r1 = __builtin_amdgcn_permlane32_swap(a1, b1, false, false);
    u32x4 res = {r0[0], r1[0], r0[1], r1[1]};
    gst128(out + (size_t)(mt * 64 + ((w >> 1) & 1) * 32 + r) * 512 + nt * 64 + (w >> 2) * 32 + (w & 1) * 16 + h * 8, res);
    asm volatile("pf_text_end:");
}

extern "C" void kernel_launch(void* const* d_in, const int* in_sizes, int n_in, void* d_out, int out_size, void* d_ws,
                              size_t ws_size, hipStream_t stream) {
    const unsigned short* x    = (const unsigned short*)d_in[0];
    const unsigned short* wq   = (const unsigned short*)d_in[1];
    const unsigned short* wk   = (const unsigned short*)d_in[2];
    const unsigned short* wv   = (const unsigned short*)d_in[3];
    const unsigned short* woA  = (const unsigned short*)d_in[4];
    const unsigned short* wqkv = (const unsigned short*)d_in[6];
    const unsigned short* woB  = (const unsigned short*)d_in[7];
    const unsigned short* gw   = (const unsigned short*)d_in[8];
    char* ws = (char*)d_ws;
    (void)hipFuncSetAttribute((const void*)proj_kernel, hipFuncAttributeMaxDynamicSharedMemorySize, P_LDS);
    (void)hipFuncSetAttribute((const void*)attn_kernel, hipFuncAttributeMaxDynamicSharedMemorySize, A_LDS);
    proj_kernel<<<256, 512, P_LDS, stream>>>(x, wq, wk, wv, wqkv, gw, woA, woB, ws);
    attn_kernel<<<256, 512, A_LDS, stream>>>(ws, (unsigned short*)d_out);
}
```

```cpp
#include <hip/hip_runtime.h>
#include <hip/hip_bf16.h>
#include <stdint.h>

typedef short    bf16x8 __attribute__((ext_vector_type(8)));
typedef short    s16x4  __attribute__((ext_vector_type(4)));
typedef float    f32x16 __attribute__((ext_vector_type(16)));
typedef float    f32x4  __attribute__((ext_vector_type(4)));
typedef float    f32x2  __attribute__((ext_vector_type(2)));
typedef unsigned u32x4  __attribute__((ext_vector_type(4)));
typedef unsigned u32x16 __attribute__((ext_vector_type(16)));
typedef __bf16   bf16x2v __attribute__((ext_vector_type(2)));

#define DI __device__ __forceinline__
#define MFMA32(a, b, c) __builtin_amdgcn_mfma_f32_32x32x16_bf16((a), (b), (c), 0, 0, 0)
#define LDS_AS __attribute__((address_space(3)))

constexpr size_t WS_Q  = 0;
constexpr size_t WS_KV = WS_Q + (size_t)64 * 65536;
constexpr size_t WS_O  = WS_KV + (size_t)34 * 131072;
constexpr size_t WS_WO = WS_O + (size_t)64 * 65536;
constexpr size_t WS_G  = WS_WO + (size_t)1024 * 1024;
constexpr size_t WS_C  = WS_G + 8192;

constexpr float QSCALE = 0.25f * 1.4426950408889634f;
constexpr float REFOFF = 96.f;
constexpr unsigned SPIN_CAP = 1u << 20;

DI unsigned pk_bf16(float lo, float hi) {
    f32x2 v = {lo, hi};
    bf16x2v r = __builtin_convertvector(v, bf16x2v);
    return __builtin_bit_cast(unsigned, r);
}

DI void glds16(const void* g, unsigned lds_base) {
    unsigned sv;
    asm volatile("s_mov_b32 %0, m0\n\ts_mov_b32 m0, %2\n\ts_nop 0\n\tglobal_load_lds_dwordx4 %1, off\n\ts_mov_b32 m0, %0"
                 : "=&s"(sv) : "v"(g), "s"(lds_base) : "memory");
}
#define WAIT_BAR(N) asm volatile("s_waitcnt vmcnt(" #N ") lgkmcnt(0)\n\ts_barrier" ::: "memory")

DI void gst128(void* p, u32x4 v) { asm volatile("global_store_dwordx4 %0, %1, off sc1\n\ts_nop 1" :: "v"(p), "v"(v) : "memory"); }
DI bf16x8 lds_b128(unsigned addr) { return *(const LDS_AS bf16x8*)(uintptr_t)addr; }
DI s16x4 lds_tr(unsigned addr) {
    return __builtin_bit_cast(s16x4, __builtin_amdgcn_ds_read_tr16_b64_v4i16((LDS_AS s16x4*)(uintptr_t)addr));
}
DI bf16x8 cat8(s16x4 a, s16x4 b) { return __builtin_shufflevector(a, b, 0, 1, 2, 3, 4, 5, 6, 7); }

DI u32x4 pack8(const f32x16& a, int base, float sc) {
    u32x4 r;
    r[0] = pk_bf16(a[base + 0] * sc, a[base + 1] * sc);
    r[1] = pk_bf16(a[base + 2] * sc, a[base + 3] * sc);
    r[2] = pk_bf16(a[base + 4] * sc, a[base + 5] * sc);
    r[3] = pk_bf16(a[base + 6] * sc, a[base + 7] * sc);
    return r;
}

constexpr int P_XS = 0, P_WS = 16384, P_STAGE = 32768, P_WX = 4 * P_STAGE, P_GT = P_WX + 16384, P_WO = P_GT + 9216, P_CNT = P_WO + 4096,
              P_DUMP = P_CNT + 16, P_LDS = P_DUMP + 256;
#define SBAR() __builtin_amdgcn_sched_barrier(0)

struct PSrc { const unsigned short *xs0, *xs1, *wsr; int ldw; };
DI PSrc proj_src(const unsigned short* x, const unsigned short* wsrc, int ldw, int mt, int nt, int l, int lane) {
    PSrc s;
    const int row = 32 * l + (lane >> 3);
    s.xs0 = x + (size_t)(mt * 128 + row) * 512 + ((lane & 7) ^ ((row >> 1) & 7)) * 8;
    s.xs1 = x + (size_t)(mt * 128 + row + 8) * 512 + ((lane & 7) ^ (((row + 8) >> 1) & 7)) * 8;
    const int krow = 16 * l + (lane >> 4);
    s.wsr = wsrc + (size_t)krow * ldw + ((lane & 15) ^ (((lane >> 4) & 3) << 2)) * 8;
    s.ldw = ldw;
    return s;
}
DI void proj_issue(const PSrc& s, int c, int l, unsigned lds0) {
    const unsigned st = lds0 + (c & 3) * P_STAGE + l * 4096;
    glds16(s.xs0 + c * 64, st + P_XS);
    glds16(s.xs1 + c * 64, st + P_XS + 1024);
    glds16(s.xs0 + c * 64 + 16 * 512, st + P_XS + 2048);
    glds16(s.xs1 + c * 64 + 16 * 512, st + P_XS + 3072);
#pragma unroll
    for (int i = 0; i < 4; ++i) glds16(s.wsr + (size_t)(c * 64 + 4 * i) * s.ldw, st + P_WS + i * 1024);
}
DI float wave_sum63(float v) {
#define DPP_ADD(ctrl, rm) v += __int_as_float(__builtin_amdgcn_update_dpp(0, __float_as_int(v), ctrl, rm, 0xf, false))
    DPP_ADD(0x111, 0xf); DPP_ADD(0x112, 0xf); DPP_ADD(0x114, 0xf); DPP_ADD(0x118, 0xf);
    DPP_ADD(0x142, 0xa); DPP_ADD(0x143, 0xc);
#undef DPP_ADD
    return v;
}
DI void lds_spin(const char* smem, int off, unsigned target) {
    const volatile LDS_AS unsigned* p = (const volatile LDS_AS unsigned*)(uintptr_t)((unsigned)(uintptr_t)smem + off);
    while ((unsigned)__builtin_amdgcn_readfirstlane(*p) < target) __builtin_amdgcn_s_sleep(1);
    asm volatile("" ::: "memory");
}

template <bool ISV>
DI void proj_compute(const PSrc& src, const unsigned short* __restrict__ x, const unsigned short* __restrict__ wx,
                     const unsigned short* __restrict__ gw, const unsigned short* __restrict__ wo, char* __restrict__ ws,
                     int mt, int nt, int w, unsigned lds0, char* smem) {
    const int lane = threadIdx.x & 63;
    const int wm = w >> 1, wn = w & 1;
    const int r = lane & 31, h = lane >> 5;
    unsigned xo[4][2];
#pragma unroll
    for (int tt = 0; tt < 2; ++tt) {
        const int row = wm * 64 + tt * 32 + r;
#pragma unroll
        for (int ks = 0; ks < 4; ++ks) xo[ks][tt] = lds0 + P_XS + row * 128 + (((2 * ks + h) ^ ((row >> 1) & 7)) << 4);
    }
    const int q4 = (lane & 15) >> 2, p4 = lane & 3, g1 = (lane >> 4) & 1;
    unsigned wa[2];
#pragma unroll
    for (int ct = 0; ct < 2; ++ct)
        wa[ct] = lds0 + P_WS + (8 * h + q4) * 256 + ((4 * ((2 * wn + ct) ^ q4) + 2 * g1 + (p4 >> 1)) << 4) + (p4 & 1) * 8;
    f32x16 acc[2][2];
#pragma unroll
    for (int i = 0; i < 16; ++i) { acc[0][0][i] = 0.f; acc[0][1][i] = 0.f; acc[1][0][i] = 0.f; acc[1][1][i] = 0.f; }
    bf16x8 fx[2][4][2], fw[2][4][2];
    auto mm = [&](int bf, int ks) {
#pragma unroll
        for (int ct = 0; ct < 2; ++ct)
#pragma unroll
            for (int tt = 0; tt < 2; ++tt)
                acc[ct][tt] = ISV ? MFMA32(fx[bf][ks][tt], fw[bf][ks][ct], acc[ct][tt]) : MFMA32(fw[bf][ks][ct], fx[bf][ks][tt], acc[ct][tt]);
    };
    auto rd = [&](int bf, int ks, unsigned so) {
#pragma unroll
        for (int tt = 0; tt < 2; ++tt) fx[bf][ks][tt] = lds_b128(xo[ks][tt] + so);
#pragma unroll
        for (int ct = 0; ct < 2; ++ct) fw[bf][ks][ct] = cat8(lds_tr(wa[ct] + so + ks * 4096), lds_tr(wa[ct] + so + ks * 4096 + 1024));
    };
    auto step = [&](int bf, int c) {
        __builtin_amdgcn_s_waitcnt(0xC07F);
        asm volatile("s_barrier" ::: "memory");
        SBAR();
        const unsigned so = (c & 3) * P_STAGE;
#pragma unroll
        for (int ks = 0; ks < 4; ++ks) { rd(bf, ks, so); mm(bf ^ 1, ks); }
#pragma unroll
        for (int i = 0; i < 12; ++i) {
            __builtin_amdgcn_sched_group_barrier(0x008, 1, 0);
            __builtin_amdgcn_sched_group_barrier(0x002, 1, 0);
            __builtin_amdgcn_sched_group_barrier(0x100, 2, 0);
        }
        __builtin_amdgcn_sched_group_barrier(0x008, 4, 0);
        SBAR();
    };
    asm volatile("" : "+v"(xo[0][0]), "+v"(xo[0][1]), "+v"(xo[1][0]), "+v"(xo[1][1]), "+v"(xo[2][0]), "+v"(xo[2][1]),
                      "+v"(xo[3][0]), "+v"(xo[3][1]), "+v"(wa[0]), "+v"(wa[1]),
                      "+v"(acc[0][0]), "+v"(acc[0][1]), "+v"(acc[1][0]), "+v"(acc[1][1]));
    asm volatile("s_waitcnt vmcnt(0)" ::: "memory");
    if (lane == 0) atomicAdd((unsigned*)(smem + P_CNT), 1u);
    lds_spin(smem, P_CNT, 4u);
    rd(0, 0, 0); rd(0, 1, 0); rd(0, 2, 0); rd(0, 3, 0);
    SBAR();
    {
        if (nt >= 8) {
            const unsigned short* es = wx + (size_t)((lane >> 5) * 64 + 16 * w) * 16 + (lane & 31) * 8;
#pragma unroll
            for (int i = 0; i < 4; ++i) glds16(es + i * 2048, lds0 + P_WX + w * 4096 + i * 1024);
        }
        const unsigned short* px = x + (size_t)(mt * 128 + nt * 8 + 2 * w) * 512 + lane * 8;
        glds16(px, lds0 + P_GT + w * 2048);
        glds16(px + 512, lds0 + P_GT + w * 2048 + 1024);
        glds16(gw + lane * 8, lds0 + P_GT + 8192);
        glds16(wo + (size_t)(4 * w + (lane >> 4)) * 512 + ((lane & 15) ^ (((lane >> 4) & 3) << 2)) * 8, lds0 + P_WO + w * 1024);
    }

#pragma unroll 1
    for (int c = 1; c < 7; c += 2) { step(1, c); step(0, c + 1); }
    asm volatile("s_waitcnt vmcnt(0)" ::: "memory");
    step(1, 7);
#pragma unroll
    for (int ct = 0; ct < 2; ++ct)
#pragma unroll
        for (int tt = 0; tt < 2; ++tt)
#pragma unroll
            for (int ks = 0; ks < 4; ++ks)
                acc[ct][tt] = ISV ? MFMA32(fx[1][ks][tt], fw[1][ks][ct], acc[ct][tt]) : MFMA32(fw[1][ks][ct], fx[1][ks][tt], acc[ct][tt]);
#pragma unroll
    for (int ct = 0; ct < 2; ++ct)
#pragma unroll
        for (int tt = 0; tt < 2; ++tt) {
            const int hd0 = (nt & 3) * 8 + wn * 4 + ct * 2;
            const int tile = mt * 4 + wm * 2 + tt;
            if (ISV) {
                char* dst = ws + WS_KV + (size_t)(hd0 + (r >> 4)) * 131072 + tile * 2048 + 1024 + h * 256 + (r & 15) * 16;
                gst128(dst, pack8(acc[ct][tt], 0, 1.f));
                gst128(dst + 512, pack8(acc[ct][tt], 8, 1.f));
            } else {
                const bool isq = nt < 8;
                const float sc = isq ? QSCALE : 1.f;
                const size_t hs = isq ? 65536 : 131072;
                char* dst = isq ? (ws + WS_Q + (size_t)((nt >> 2) * 32 + hd0) * 65536 + tile * 1024 + lane * 16)
                                : (ws + WS_KV + (size_t)hd0 * 131072 + tile * 2048 + lane * 16);
                gst128(dst, pack8(acc[ct][tt], 0, sc));
                gst128(dst + hs, pack8(acc[ct][tt], 8, sc));
            }
        }
}

template <int XM>
DI void proj_loader(const PSrc& src, const unsigned short* __restrict__ x, const unsigned short* __restrict__ wk,
                    const unsigned short* __restrict__ wv, const unsigned short* __restrict__ gw, char* __restrict__ ws,
                    int mt, int nt, int l, unsigned lds0, char* smem) {
    const int lane = threadIdx.x & 63;
    const int r = lane & 31, h = lane >> 5;
    constexpr bool isx = XM != 0, isv = XM == 2;
    const int xtt = nt & 3;
    proj_issue(src, 2, l, lds0);
    const int q4 = (lane & 15) >> 2, p4 = lane & 3;
    const unsigned ea = lds0 + P_WX + l * 4096 + (8 * h + q4) * 32 + p4 * 8;
    const int xrow = xtt * 32 + r;
    const unsigned exa = lds0 + P_XS + xrow * 128 + (((2 * l + h) ^ ((xrow >> 1) & 7)) << 4);
    const int gtok = mt * 128 + nt * 8 + 2 * l;
    u32x4 gx0, gx1, gv;
    bf16x8 xe[8];
    if (isx) { lds_spin(smem, P_CNT, 4u); xe[0] = lds_b128(exa); }
#pragma unroll
    for (int c = 1; c < 8; ++c) {
        if (c < 7) WAIT_BAR(8); else WAIT_BAR(0);
        if (isx) xe[c] = lds_b128(exa + (c & 3) * P_STAGE);
        if (c < 6) proj_issue(src, c + 2, l, lds0);
    }
    gx0 = *(const u32x4*)(smem + P_GT + l * 2048 + lane * 16);
    gx1 = *(const u32x4*)(smem + P_GT + l * 2048 + 1024 + lane * 16);
    gv  = *(const u32x4*)(smem + P_GT + 8192 + lane * 16);
    {
        const int g1 = (lane >> 4) & 1;
        const unsigned woa = lds0 + P_WO + (4 * h + q4) * 256 + ((4 * (l ^ q4) + 2 * g1 + (p4 >> 1)) << 4) + (p4 & 1) * 8;
        const bf16x8 f = cat8(lds_tr(woa), lds_tr(woa + 2048));
        gst128(ws + WS_WO + (size_t)(blockIdx.x * 4 + l) * 1024 + lane * 16, __builtin_bit_cast(u32x4, f));
    }
    {
        float s0 = 0.f, s1 = 0.f;
#pragma unroll
        for (int i = 0; i < 4; ++i) {
            const float wl = __uint_as_float(gv[i] << 16), wh = __uint_as_float(gv[i] & 0xffff0000u);
            s0 += __uint_as_float(gx0[i] << 16) * wl + __uint_as_float(gx0[i] & 0xffff0000u) * wh;
            s1 += __uint_as_float(gx1[i] << 16) * wl + __uint_as_float(gx1[i] & 0xffff0000u) * wh;
        }
        s0 = wave_sum63(s0); s1 = wave_sum63(s1);
        if (lane == 63) {
            f32x2 g = {__builtin_amdgcn_rcpf(1.f + __expf(-s0)), __builtin_amdgcn_rcpf(1.f + __expf(-s1))};
            *(f32x2*)((float*)(ws + WS_G) + gtok) = g;
        }
    }
    f32x16 acce;
#pragma unroll
    for (int i = 0; i < 16; ++i) acce[i] = 0.f;
    if (isx) {
        __builtin_amdgcn_s_setprio(3);
#pragma unroll
        for (int c = 0; c < 8; ++c) {
            const bf16x8 we = cat8(lds_tr(ea + c * 512), lds_tr(ea + c * 512 + 128));
            acce = isv ? MFMA32(xe[c], we, acce) : MFMA32(we, xe[c], acce);
        }
    }
    if (isx) {
        f32x4* red = (f32x4*)smem;
#pragma unroll
        for (int g = 0; g < 4; ++g) {
            f32x4 v = {acce[4 * g], acce[4 * g + 1], acce[4 * g + 2], acce[4 * g + 3]};
            red[(l * 4 + g) * 64 + lane] = v;
        }
        unsigned old = 0;
        asm volatile("s_waitcnt lgkmcnt(0)" ::: "memory");
        if (lane == 0) old = atomicAdd((unsigned*)(smem + P_CNT), 1u);
        old = __builtin_amdgcn_readfirstlane(old);
        if (old == 7) {
            asm volatile("" ::: "memory");
#pragma unroll
            for (int g = 0; g < 4; ++g) {
                const f32x4 v = red[g * 64 + lane] + red[(4 + g) * 64 + lane] + red[(8 + g) * 64 + lane] + red[(12 + g) * 64 + lane];
#pragma unroll
                for (int i = 0; i < 4; ++i) acce[4 * g + i] = v[i];
            }
            char* base = ws + WS_KV + (size_t)32 * 131072 + (mt * 4 + xtt) * 2048;
            if (isv) {
                if (r < 16) {
                    gst128(base + 1024 + h * 256 + r * 16, pack8(acce, 0, 1.f));
                    gst128(base + 1536 + h * 256 + r * 16, pack8(acce, 8, 1.f));
                }
            } else {
                gst128(base + lane * 16, pack8(acce, 0, 1.f));
            }
        }
    }
}

__global__ __launch_bounds__(512, 2) void proj_kernel(const unsigned short* __restrict__ x, const unsigned short* __restrict__ wq,
                                                      const unsigned short* __restrict__ wk, const unsigned short* __restrict__ wv,
                                                      const unsigned short* __restrict__ wqkv, const unsigned short* __restrict__ gw,
                                                      const unsigned short* __restrict__ woA, const unsigned short* __restrict__ woB,
                                                      char* __restrict__ ws) {
    extern __shared__ __attribute__((aligned(16))) char smem[];
    const unsigned lds0 = (unsigned)(uintptr_t)smem;
    const int w = __builtin_amdgcn_readfirstlane(threadIdx.x >> 6);
    const int b = blockIdx.x, xcd = b & 7, slot = b >> 3;
    const int mt = (xcd & 3) * 4 + (slot & 3), nt = (xcd >> 2) * 8 + (slot >> 2);
    if (w == 7) {
        unsigned long long pc; int len;
        asm volatile("s_getpc_b64 %0\n\tpf_text_begin:\n\ts_mov_b32 %1, pf_text_end-pf_text_begin" : "=s"(pc), "=s"(len));
        int off = ((slot & 7) * 32 + (int)(threadIdx.x & 31)) * 128;
        off = off < len ? off : 0;
        unsigned sv;
        asm volatile("s_mov_b32 %0, m0\n\ts_mov_b32 m0, %3\n\ts_nop 0\n\tglobal_load_lds_dword %1, %2\n\ts_mov_b32 m0, %0"
                     : "=&s"(sv) : "v"(off), "s"(pc), "s"(lds0 + P_DUMP) : "memory");
    }
    if (threadIdx.x == 0) *(unsigned*)(smem + P_CNT) = 0u;
    const unsigned short* wsrc = (nt < 4) ? (wq + nt * 128) : (wqkv + (nt - 4) * 128);
    const PSrc src = proj_src(x, wsrc, (nt < 4) ? 512 : 1536, mt, nt, w & 3, threadIdx.x & 63);
    const unsigned short* wo = ((b < 128) ? woA : woB) + (size_t)((b >> 2) & 31) * 16 * 512 + (b & 3) * 128;
    proj_issue(src, w >> 2, w & 3, lds0);
    asm volatile("s_waitcnt lgkmcnt(0)\n\ts_barrier" ::: "memory");
    if (w >= 4) {
        if (nt >= 12)     proj_loader<2>(src, x, wk, wv, gw, ws, mt, nt, w - 4, lds0, smem);
        else if (nt >= 8) proj_loader<1>(src, x, wk, wv, gw, ws, mt, nt, w - 4, lds0, smem);
        else              proj_loader<0>(src, x, wk, wv, gw, ws, mt, nt, w - 4, lds0, smem);
        if (b == 0 && w == 4 && (threadIdx.x & 63) < 4) *(unsigned*)(ws + WS_C + (threadIdx.x & 63) * 256) = 0u;
    } else if (nt >= 12) proj_compute<true>(src, x, wv, gw, wo, ws, mt, nt, w, lds0, smem);
    else                 proj_compute<false>(src, x, wk, gw, wo, ws, mt, nt, w, lds0, smem);
}

constexpr int A_KV = 0, A_CONST = 131072, A_LDS = A_CONST + 8192;

DI float max16(const f32x16& s) {
    float a = fmaxf(fmaxf(s[0], s[1]), s[2]), b = fmaxf(fmaxf(s[3], s[4]), s[5]);
    a = fmaxf(fmaxf(a, s[6]), s[7]);   b = fmaxf(fmaxf(b, s[8]), s[9]);
    a = fmaxf(fmaxf(a, s[10]), s[11]); b = fmaxf(fmaxf(b, s[12]), s[13]);
    a = fmaxf(fmaxf(a, s[14]), s[15]);
    return fmaxf(a, b);
}
DI float half_max(float m) {
    auto rr = __builtin_amdgcn_permlane32_swap(__float_as_uint(m), __float_as_uint(m), false, false);
    return fmaxf(__uint_as_float(rr[0]), __uint_as_float(rr[1]));
}

#define R_SA "192"
#define R_SB "208"
#define EX(X, i) "v_exp_f32 v[" X "+" #i "], v[" X "+" #i "]\n\t"
#define CV(X, j, a, b) "v_cvt_pk_bf16_f32 v[224+" #j "], v[" X "+" #a "], v[" X "+" #b "]\n\t"
#define HALF(X, Y, OX, OY, NY, QY, WA, RA, WB, RB, WC, RC)                                        \
    EX(X, 0) EX(X, 1) WA                                                                          \
    "v_mfma_f32_32x32x16_bf16 " OY ", %[v1], v[228:231], " OY "\n\t" RA                           \
    EX(X, 2) EX(X, 3) CV(X, 0, 0, 1) EX(X, 4) EX(X, 5) CV(X, 1, 2, 3) WB                          \
    "v_mfma_f32_32x32x16_bf16 v[" Y ":" Y "+15], %[kf], " QY ", " NY "\n\t" RB                    \
    EX(X, 6) EX(X, 7) CV(X, 2, 4, 5) EX(X, 8) EX(X, 9) CV(X, 3, 6, 7) EX(X, 10) EX(X, 11) WC      \
    "v_mfma_f32_32x32x16_bf16 " OX ", %[v0], v[224:227], " OX "\n\t" RC                           \
    CV(X, 4, 8, 9) EX(X, 12) EX(X, 13) CV(X, 5, 10, 11) EX(X, 14) EX(X, 15) CV(X, 6, 12, 13) CV(X, 7, 14, 15)
#define TILE(OFF_V1, OFF_KN, OFF_V0N, P0, P1)                                                     \
    HALF(R_SA, R_SB, "%[oa]", "%[ob]", "%[nb]", "%[qb]",                                          \
         "", "ds_read_b128 %[v1], %[va] offset:" OFF_V1 "\n\t" P0,                                \
         "", "ds_read_b128 %[kf], %[ka] offset:" OFF_KN "\n\t",                                   \
         "s_waitcnt lgkmcnt(2)\n\t", "")                                                          \
    HALF(R_SB, R_SA, "%[ob]", "%[oa]", "%[na]", "%[qa]",                                          \
         "s_waitcnt lgkmcnt(1)\n\t", P1,                                                          \
         "s_waitcnt lgkmcnt(0)\n\t", "",                                                          \
         "", "ds_read_b128 %[v0], %[va] offset:" OFF_V0N "\n\t")
#define KVDMA                                                                                     \
    "global_load_lds_dwordx4 %[lo], s[40:41]\n\t"                                                 \
    "s_add_u32 s40, s40, 0x2000\n\ts_addc_u32 s41, s41, 0\n\ts_add_u32 m0, m0, 0x2000\n\ts_nop 0\n\t"
#define WOLD(R)                                                                                   \
    "global_load_dwordx4 v[" #R ":" #R "+3], %[lo], s[42:43]\n\t"                                 \
    "global_load_dwordx4 v[" #R "+4:" #R "+7], %[lo], s[42:43] offset:1024\n\t"                   \
    "s_add_u32 s42, s42, 0x4000\n\ts_addc_u32 s43, s43, 0\n\t"
#define ATTN_ASM(PY, PO)                                                                          \
    asm volatile(                                                                                 \
        "s_mov_b32 s44, m0\n\ts_mov_b32 m0, %[kd]\n\ts_mov_b64 s[40:41], %[kb]\n\ts_mov_b64 s[42:43], %[wb]\n\ts_cmp_lg_u32 %[yg], 0\n\ts_cselect_b64 vcc, -1, 0\n\t"               \
        "ds_read_b128 %[kf], %[ka]\n\t"                                                           \
        "ds_read_b128 %[v1], %[va]\n\t"                                                           \
        "ds_read_b128 %[v0], %[va]\n\t"                                                           \
        "v_mov_b32 v228, 0\n\tv_mov_b32 v229, 0\n\tv_mov_b32 v230, 0\n\tv_mov_b32 v231, 0\n\t"    \
        "s_waitcnt lgkmcnt(0)\n\t"                                                                \
        "v_mfma_f32_32x32x16_bf16 v[192:207], %[kf], %[qa], %[na]\n\t"                            \
        "s_nop 7\n\ts_nop 7\n\t"                                                                  \
        "L_loop_%=:\n\t"                                                                          \
        "s_bitcmp1_b32 %[mk], %[cnt]\n\t"                                                         \
        "s_cbranch_scc1 L_sp_%=\n\t"                                                              \
        "L_go_%=:\n\t"                                                                            \
        TILE("512", "2048", "2048", "s_setprio 0\n\t", PY)                                        \
        TILE("2560", "4096", "4096", PO, "")                                                      \
        TILE("4608", "6144", "6144", "", "")                                                      \
        HALF(R_SA, R_SB, "%[oa]", "%[ob]", "%[nb]", "%[qb]",                                      \
             "", "ds_read_b128 %[v1], %[va] offset:6656\n\t",                                     \
             "", "ds_read_b128 %[kf], %[ka] offset:8192\n\tv_add_u32 %[va], %[va], %[vs]\n\t",    \
             "s_waitcnt lgkmcnt(2)\n\t", "v_add_u32 %[ka], 0x2000, %[ka]\n\t")                    \
        HALF(R_SB, R_SA, "%[ob]", "%[oa]", "%[na]", "%[qa]",                                      \
             "s_waitcnt lgkmcnt(1)\n\t", "s_add_u32 %[cnt], %[cnt], 1\n\t",                       \
             "s_waitcnt lgkmcnt(0)\n\t", "s_and_b32 s45, %[cnt], 15\n\t",                           \
             "", "ds_read_b128 %[v0], %[va]\n\t")                                                 \
        "s_cbranch_scc1 L_loop_%=\n\t"                                                            \
        "s_branch L_end_%=\n\t"                                                                   \
        "L_sp_%=:\n\t"                                                                            \
        "s_cmp_gt_u32 %[cnt], 2\n\t"                                                              \
        "s_cbranch_scc1 L_s3_%=\n\t"                                                              \
        "s_cmp_lg_u32 %[cnt], 1\n\t"                                                              \
        "s_cbranch_scc1 L_s2_%=\n\t"                                                              \
        "s_waitcnt vmcnt(4)\n\t"                                                                  \
        "s_barrier\n\t"                                                                           \
        "L_s2_%=:\n\t"                                                                            \
        KVDMA KVDMA KVDMA KVDMA                                                                   \
        "s_branch L_go_%=\n\t"                                                                    \
        "L_s3_%=:\n\t"                                                                            \
        "s_cmp_gt_u32 %[cnt], 3\n\t"                                                              \
        "s_cbranch_scc1 L_s4_%=\n\t"                                                              \
        "s_waitcnt vmcnt(0)\n\t"                                                                  \
        "s_barrier\n\t"                                                                           \
        "s_branch L_go_%=\n\t"                                                                    \
        "L_s4_%=:\n\t"                                                                            \
        "s_cmp_gt_u32 %[cnt], 8\n\t"                                                              \
        "s_cbranch_scc1 L_s5_%=\n\t"                                                              \
        "v_mov_b32 v232, -1\n\tv_mov_b32 v233, -1\n\tv_mov_b32 v234, -1\n\tv_mov_b32 v235, -1\n\t" \
        "s_mov_b64 s[40:41], %[ob2]\n\t"                                                          \
        "global_store_dwordx4 %[lo], v[232:235], s[40:41] sc1\n\t"                                \
        "s_add_u32 s40, s40, 0x10000\n\ts_addc_u32 s41, s41, 0\n\t"                               \
        "global_store_dwordx4 %[lo], v[232:235], s[40:41] sc1\n\t"                                \
        WOLD(128) WOLD(136) WOLD(144) WOLD(152) WOLD(160) WOLD(168) WOLD(176) WOLD(184)           \
        "s_branch L_go_%=\n\t"                                                                    \
        "L_s5_%=:\n\t"                                                                            \
        "s_cmp_gt_u32 %[cnt], 11\n\t"                                                             \
        "s_cbranch_scc1 L_s6_%=\n\t"                                                              \
        "s_waitcnt vmcnt(0)\n\t"                                                                  \
        "s_mov_b64 exec, 1\n\t"                                                                   \
        "v_mov_b32 v232, 1\n\t"                                                                   \
        "global_atomic_add %[pf], v232, off sc1\n\t"                                              \
        "s_mov_b64 exec, -1\n\t"                                                                  \
        "s_branch L_go_%=\n\t"                                                                    \
        "L_s6_%=:\n\t"                                                                            \
        "global_load_dword %[cv], %[pf], off sc1\n\t"                                             \
        "s_branch L_go_%=\n\t"                                                                    \
        "L_end_%=:\n\t"                                                                           \
        "s_nop 1\n\t"                                                                             \
        "v_mfma_f32_32x32x16_bf16 %[ob], %[v1], v[228:231], %[ob]\n\t"                            \
        "s_waitcnt vmcnt(0) lgkmcnt(0)\n\t"                                                       \
        "s_mov_b32 m0, s44\n\t"                                                                \
        "s_nop 7\n\ts_nop 7\n\t"                                                                  \
        : [oa] "+v"(oa), [ob] "+v"(ob), [ka] "+v"(ka), [va] "+v"(va), [cnt] "+s"(cnt), [cv] "+v"(cv), \
          [kf] "=&v"(kf), [v0] "=&v"(v0), [v1] "=&v"(v1),                                         \
          "+{v[128:143]}"(w0), "+{v[144:159]}"(w1), "+{v[160:175]}"(w2), "+{v[176:191]}"(w3)      \
        : [qa] "v"(qa), [qb] "v"(qb), [na] "v"(na), [nb] "v"(nb), [vs] "v"(vstep), [yg] "s"(yg), [pf] "v"(pf), \
          [lo] "v"(lo), [kb] "s"(kb), [kd] "s"(kd), [wb] "s"(wb), [ob2] "s"(ob2), [mk] "s"(mk)                                                \
        : "memory", "scc", "vcc", "s40", "s41", "s42", "s43", "s44", "s45",                                                                        \
          "v192", "v193", "v194", "v195", "v196", "v197", "v198", "v199", "v200", "v201", "v202", "v203", "v204", "v205", "v206", "v207", \
          "v208", "v209", "v210", "v211", "v212", "v213", "v214", "v215", "v216", "v217", "v218", "v219", "v220", "v221", "v222", "v223", \
          "v224", "v225", "v226", "v227", "v228", "v229", "v230", "v231", "v232", "v233", "v234", "v235")

DI void attn_main(bool young, bool first, unsigned ka, unsigned va, unsigned vstep, const bf16x8& qa, const bf16x8& qb, const f32x16& na,
                  const f32x16& nb, f32x16& oa, f32x16& ob, const char* pf, unsigned lo, const char* kb, unsigned kd,
                  const char* wb, const char* ob2, u32x16& w0, u32x16& w1, u32x16& w2, u32x16& w3, unsigned& cv) {
    bf16x8 kf, v0, v1;
    int cnt = __builtin_amdgcn_readfirstlane(first ? 0 : 16);
    const int yg = __builtin_amdgcn_readfirstlane(young ? 1 : 0);
    const int mk = __builtin_amdgcn_readfirstlane(young ? 0x422f : 0x448f);
    ATTN_ASM("s_cbranch_vccz L_py_%=\n\ts_setprio 1\n\tL_py_%=:\n\t", "s_setprio 1\n\t");
}

__global__ __launch_bounds__(512, 2) void attn_kernel(char* __restrict__ ws, unsigned short* __restrict__ out) {
    extern __shared__ __attribute__((aligned(16))) char smem[];
    const unsigned lds0 = (unsigned)(uintptr_t)smem;
    const int tid = threadIdx.x, lane = tid & 63;
    const int w = __builtin_amdgcn_readfirstlane(tid >> 6);
    const int r = lane & 31, h = lane >> 5;
    const int b = blockIdx.x, xcd = b & 7, slot = b >> 3;
    const int isB = (slot < 16);
    const int hd = xcd * 4 + ((slot & 15) >> 2);
    const int hh = isB ? 32 + hd : hd;
    const int kvh = isB ? hd : 32;
    const int tile = (slot & 3) * 16 + 2 * w;

    bf16x8 qa, qb;
    {
        const char* qsrc = ws + WS_Q + (size_t)hh * 65536 + tile * 1024 + lane * 16;
        asm volatile("global_load_dwordx4 %0, %2, off\n\tglobal_load_dwordx4 %1, %2, off offset:1024"
                     : "=&v"(qa), "=&v"(qb) : "v"(qsrc) : "memory");
    }
    const char* kvsrc = ws + WS_KV + (size_t)kvh * 131072 + w * 1024 + lane * 16;
#pragma unroll
    for (int i = 0; i < 4; ++i) glds16(kvsrc + i * 8192, lds0 + A_KV + i * 8192 + w * 1024);
    {
        const unsigned one = ((tid & 15) == 0 || (tid & 15) == 4) ? 0x3f803f80u : 0u;
        u32x4 c = {one, one, one, one};
        *(u32x4*)(smem + A_CONST + tid * 16) = c;
    }
    const unsigned kaddr = lds0 + A_KV + lane * 16;
    const unsigned vaddr = (r < 16) ? (lds0 + A_KV + 1024 + h * 256 + r * 16) : (lds0 + A_CONST + (r - 16) * 16);
    const unsigned vstep = (r < 16) ? 8192u : 0u;

    float ga, gb;
    {
        const float* gsrc = (const float*)(ws + WS_G) + tile * 32 + r;
        asm volatile("global_load_dword %0, %2, off\n\tglobal_load_dword %1, %2, off offset:128" : "=&v"(ga), "=&v"(gb) : "v"(gsrc) : "memory");
    }
    const char* kb = ws + WS_KV + (size_t)kvh * 131072 + 32768 + w * 1024;
    const unsigned kd = lds0 + A_KV + 32768 + w * 1024;
    const int qblk = slot & 3;
    const int mt = qblk * 8 + (hh >> 3), nt = hh & 7;
    char* obase = ws + WS_O + (size_t)tile * 65536 + hh * 1024;
    char* odst = obase + lane * 16;
    const char* cnt = ws + WS_C + qblk * 256;
    unsigned cv = 0u;
    const char* wb = ws + WS_WO + (size_t)(w * 8) * 16384 + (nt * 2) * 1024;
    u32x16 w0, w1, w2, w3;
    asm volatile("" : "={v[128:143]}"(w0), "={v[144:159]}"(w1), "={v[160:175]}"(w2), "={v[176:191]}"(w3));
    f32x16 zero;
#pragma unroll
    for (int i = 0; i < 16; ++i) zero[i] = 0.f;
    f32x16 na, nb, oa = zero, ob = zero;
    asm volatile("" : "+v"(oa), "+v"(ob), "+s"(kb), "+s"(wb), "+s"(obase), "+v"(cnt));
    asm volatile("s_waitcnt vmcnt(4) lgkmcnt(0)\n\ts_barrier" : "+v"(qa), "+v"(qb) :: "memory");
    {
        const bf16x8 k0 = lds_b128(kaddr);
        const f32x16 sa = MFMA32(k0, qa, zero);
        const f32x16 sb = MFMA32(k0, qb, zero);
        SBAR();
        const float ma = -half_max(max16(sa)) - REFOFF, mb = -half_max(max16(sb)) - REFOFF;
#pragma unroll
        for (int i = 0; i < 16; ++i) { na[i] = ma; nb[i] = mb; }
    }
    attn_main(w >= 4, true, kaddr, vaddr, vstep, qa, qb, na, nb, oa, ob, cnt, lane * 16, kb, kd, wb, obase, w0, w1, w2, w3, cv);
    asm volatile("" : "+v"(ga), "+v"(gb));

    {
        float xa = fabsf(oa[0]), xb = fabsf(ob[0]), ta = oa[0], tb = ob[0];
#pragma unroll
        for (int i = 1; i < 8; ++i) { xa = fmaxf(xa, fabsf(oa[i])); xb = fmaxf(xb, fabsf(ob[i])); ta += oa[i]; tb += ob[i]; }
        const bool bad = !(oa[8] < 1.329228e36f) || !(ob[8] < 1.329228e36f) || !(fabsf(ta) <= 3.0e38f) || !(fabsf(tb) <= 3.0e38f) ||
                         !(xa > 7.7e-34f) || !(xb > 7.7e-34f);
        if (__any(bad)) {
            float ma = -INFINITY, mb = -INFINITY;
#pragma unroll 1
            for (int t = 0; t < 64; ++t) {
                const bf16x8 kf = lds_b128(kaddr + t * 2048);
                ma = fmaxf(ma, max16(MFMA32(kf, qa, zero)));
                mb = fmaxf(mb, max16(MFMA32(kf, qb, zero)));
            }
            ma = -half_max(ma); mb = -half_max(mb);
#pragma unroll
            for (int i = 0; i < 16; ++i) { na[i] = ma; nb[i] = mb; }
            oa = zero; ob = zero;
            attn_main(false, false, kaddr, vaddr, vstep, qa, qb, na, nb, oa, ob, cnt, lane * 16, kb, kd, wb, obase, w0, w1, w2, w3, cv);
        }
    }
    const float sa = (isB ? 1.f - ga : ga) * __builtin_amdgcn_rcpf(oa[8]), sb = (isB ? 1.f - gb : gb) * __builtin_amdgcn_rcpf(ob[8]);
    {
        u32x4 pa = pack8(oa, 0, sa), pb = pack8(ob, 0, sb);
#pragma unroll
        for (int i = 0; i < 4; ++i) { pa[i] = min(pa[i], 0xfffffffeu); pb[i] = min(pb[i], 0xfffffffeu); }
        gst128(odst, pa);
        gst128(odst + 65536, pb);
    }
    for (unsigned spins = 0; spins < SPIN_CAP && (unsigned)__builtin_amdgcn_readfirstlane(cv) < 512u; ++spins) {
        if ((unsigned)__builtin_amdgcn_readfirstlane(__hip_atomic_load((const unsigned*)cnt, __ATOMIC_RELAXED, __HIP_MEMORY_SCOPE_AGENT)) >= 512u) break;
        __builtin_amdgcn_s_sleep(2);
    }
    asm volatile("s_barrier" ::: "memory");
    u32x4 wf[8][2];
#define WSUB(W, k) wf[k][0] = __builtin_shufflevector(W, W, 0, 1, 2, 3); wf[k][1] = __builtin_shufflevector(W, W, 4, 5, 6, 7); \
                   wf[k + 1][0] = __builtin_shufflevector(W, W, 8, 9, 10, 11); wf[k + 1][1] = __builtin_shufflevector(W, W, 12, 13, 14, 15);
    WSUB(w0, 0) WSUB(w1, 2) WSUB(w2, 4) WSUB(w3, 6)
    f32x16 acc[4] = {zero, zero, zero, zero};
    {
        const char* osrc = ws + WS_O + (size_t)(mt * 2) * 65536 + (w * 8) * 1024;
        const unsigned lo16 = lane * 16;
        u32x4 of[8][2];
        auto valid = [&](int k) -> bool {
            const unsigned m = max(max(max(of[k][0][0], of[k][0][1]), max(of[k][0][2], of[k][0][3])),
                                   max(max(of[k][1][0], of[k][1][1]), max(of[k][1][2], of[k][1][3])));
            return !__any(m == 0xffffffffu);
        };
        auto mul = [&](int k) {
#pragma unroll
            for (int q = 0; q < 4; ++q)
                acc[q] = MFMA32(__builtin_bit_cast(bf16x8, wf[k][q >> 1]), __builtin_bit_cast(bf16x8, of[k][q & 1]), acc[q]);
        };
#pragma unroll
        for (int k = 0; k < 8; ++k)
            asm volatile("global_load_dwordx4 %0, %2, %3 offset:%5 sc1\n\tglobal_load_dwordx4 %1, %2, %4 offset:%5 sc1"
                         : "=&v"(of[k][0]), "=&v"(of[k][1])
                         : "v"(lo16), "s"(osrc + (k >> 2) * 4096), "s"(osrc + 65536 + (k >> 2) * 4096), "i"((k & 3) * 1024) : "memory");
        auto settle = [&](int k0, int k1) {
            const unsigned need = ((1u << k1) - 1u) & ~((1u << k0) - 1u);
            for (unsigned spins = 0; spins < SPIN_CAP; ++spins) {
                asm volatile("s_waitcnt vmcnt(0)"
                             : "+v"(of[0][0]), "+v"(of[0][1]), "+v"(of[1][0]), "+v"(of[1][1]), "+v"(of[2][0]), "+v"(of[2][1]), "+v"(of[3][0]), "+v"(of[3][1]),
                               "+v"(of[4][0]), "+v"(of[4][1]), "+v"(of[5][0]), "+v"(of[5][1]), "+v"(of[6][0]), "+v"(of[6][1]), "+v"(of[7][0]), "+v"(of[7][1])
                             :: "memory");
                unsigned pend = 0u;
#pragma unroll
                for (int k = k0; k < 8; ++k) if (!valid(k)) pend |= 1u << k;
                if ((pend & need) == 0u) break;
                __builtin_amdgcn_s_sleep(2);
#pragma unroll
                for (int k = k0; k < 8; ++k)
                    if (pend & (1u << k))
                        asm volatile("global_load_dwordx4 %0, %2, %3 offset:%5 sc1\n\tglobal_load_dwordx4 %1, %2, %4 offset:%5 sc1"
                                     : "+v"(of[k][0]), "+v"(of[k][1])
                                     : "v"(lo16), "s"(osrc + (k >> 2) * 4096), "s"(osrc + 65536 + (k >> 2) * 4096), "i"((k & 3) * 1024) : "memory");
            }
        };
        asm volatile("s_waitcnt vmcnt(8)" : "+v"(of[0][0]), "+v"(of[0][1]), "+v"(of[1][0]), "+v"(of[1][1]), "+v"(of[2][0]), "+v"(of[2][1]),
                                            "+v"(of[3][0]), "+v"(of[3][1]) :: "memory");
        if (!(valid(0) && valid(1) && valid(2) && valid(3))) settle(0, 4);
        mul(0); mul(1); mul(2); mul(3);
        SBAR();
        settle(4, 8);
        mul(4); mul(5); mul(6); mul(7);
    }
    float fin[8];
#pragma unroll
    for (int p = 0; p < 8; ++p) {
        if (p == w) {
#pragma unroll
            for (int i = 0; i < 8; ++i) fin[i] = acc[p >> 1][8 * (p & 1) + i];
        } else {
            char* dst = smem + (p * 7 + ((w - p - 1) & 7)) * 2048 + lane * 16;
#pragma unroll
            for (int g = 0; g < 2; ++g) {
                f32x4 v;
#pragma unroll
                for (int i = 0; i < 4; ++i) v[i] = acc[p >> 1][8 * (p & 1) + 4 * g + i];
                *(f32x4*)(dst + g * 1024) = v;
            }
        }
    }
    asm volatile("s_waitcnt lgkmcnt(0)\n\ts_barrier" ::: "memory");
#pragma unroll
    for (int p = 0; p < 7; ++p)
#pragma unroll
        for (int g = 0; g < 2; ++g) {
            const f32x4 v = *(const f32x4*)(smem + (w * 7 + p) * 2048 + g * 1024 + lane * 16);
#pragma unroll
            for (int i = 0; i < 4; ++i) fin[4 * g + i] += v[i];
        }
    const unsigned a0 = pk_bf16(fin[0], fin[1]), a1 = pk_bf16(fin[2], fin[3]);
    const unsigned b0 = pk_bf16(fin[4], fin[5]), b1 = pk_bf16(fin[6], fin[7]);
    auto r0 = __builtin_amdgcn_permlane32_swap(a0, b0, false, false);
    auto r1 = __builtin_amdgcn_permlane32_swap(a1, b1, false, false);
    u32x4 res = {r0[0], r1[0], r0[1], r1[1]};
    gst128(out + (size_t)(mt * 64 + ((w >> 1) & 1) * 32 + r) * 512 + nt * 64 + (w >> 2) * 32 + (w & 1) * 16 + h * 8, res);
    asm volatile("pf_text_end:");
}

extern "C" void kernel_launch(void* const* d_in, const int* in_sizes, int n_in, void* d_out, int out_size, void* d_ws,
                              size_t ws_size, hipStream_t stream) {
    const unsigned short* x    = (const unsigned short*)d_in[0];
    const unsigned short* wq   = (const unsigned short*)d_in[1];
    const unsigned short* wk   = (const unsigned short*)d_in[2];
    const unsigned short* wv   = (const unsigned short*)d_in[3];
    const unsigned short* woA  = (const unsigned short*)d_in[4];
    const unsigned short* wqkv = (const unsigned short*)d_in[6];
    const unsigned short* woB  = (const unsigned short*)d_in[7];
    const unsigned short* gw   = (const unsigned short*)d_in[8];
    char* ws = (char*)d_ws;
    (void)hipFuncSetAttribute((const void*)proj_kernel, hipFuncAttributeMaxDynamicSharedMemorySize, P_LDS);
    (void)hipFuncSetAttribute((const void*)attn_kernel, hipFuncAttributeMaxDynamicSharedMemorySize, A_LDS);
    proj_kernel<<<256, 512, P_LDS, stream>>>(x, wq, wk, wv, wqkv, gw, woA, woB, ws);
    attn_kernel<<<256, 512, A_LDS, stream>>>(ws, (unsigned short*)d_out);
}
```

```cpp
#include <hip/hip_runtime.h>
#include <hip/hip_bf16.h>
#include <stdint.h>

typedef short    bf16x8 __attribute__((ext_vector_type(8)));
typedef short    s16x4  __attribute__((ext_vector_type(4)));
typedef float    f32x16 __attribute__((ext_vector_type(16)));
typedef float    f32x4  __attribute__((ext_vector_type(4)));
typedef float    f32x2  __attribute__((ext_vector_type(2)));
typedef unsigned u32x4  __attribute__((ext_vector_type(4)));
typedef unsigned u32x16 __attribute__((ext_vector_type(16)));
typedef __bf16   bf16x2v __attribute__((ext_vector_type(2)));

#define DI __device__ __forceinline__
#define MFMA32(a, b, c) __builtin_amdgcn_mfma_f32_32x32x16_bf16((a), (b), (c), 0, 0, 0)
#define LDS_AS __attribute__((address_space(3)))

constexpr size_t WS_Q  = 0;
constexpr size_t WS_KV = WS_Q + (size_t)64 * 65536;
constexpr size_t WS_O  = WS_KV + (size_t)34 * 131072;
constexpr size_t WS_WO = WS_O + (size_t)64 * 65536;
constexpr size_t WS_G  = WS_WO + (size_t)1024 * 1024;
constexpr size_t WS_C  = WS_G + 8192;

constexpr float QSCALE = 0.25f * 1.4426950408889634f;
constexpr float REFOFF = 96.f;
constexpr unsigned SPIN_CAP = 1u << 20;

DI unsigned pk_bf16(float lo, float hi) {
    f32x2 v = {lo, hi};
    bf16x2v r = __builtin_convertvector(v, bf16x2v);
    return __builtin_bit_cast(unsigned, r);
}

DI void glds16(const void* g, unsigned lds_base) {
    unsigned sv;
    asm volatile("s_mov_b32 %0, m0\n\ts_mov_b32 m0, %2\n\ts_nop 0\n\tglobal_load_lds_dwordx4 %1, off\n\ts_mov_b32 m0, %0"
                 : "=&s"(sv) : "v"(g), "s"(lds_base) : "memory");
}
#define WAIT_BAR(N) asm volatile("s_waitcnt vmcnt(" #N ") lgkmcnt(0)\n\ts_barrier" ::: "memory")

DI void gst128(void* p, u32x4 v) { asm volatile("global_store_dwordx4 %0, %1, off sc1\n\ts_nop 1" :: "v"(p), "v"(v) : "memory"); }
DI bf16x8 lds_b128(unsigned addr) { return *(const LDS_AS bf16x8*)(uintptr_t)addr; }
DI s16x4 lds_tr(unsigned addr) {
    return __builtin_bit_cast(s16x4, __builtin_amdgcn_ds_read_tr16_b64_v4i16((LDS_AS s16x4*)(uintptr_t)addr));
}
DI bf16x8 cat8(s16x4 a, s16x4 b) { return __builtin_shufflevector(a, b, 0, 1, 2, 3, 4, 5, 6, 7); }

DI u32x4 pack8(const f32x16& a, int base, float sc) {
    u32x4 r;
    r[0] = pk_bf16(a[base + 0] * sc, a[base + 1] * sc);
    r[1] = pk_bf16(a[base + 2] * sc, a[base + 3] * sc);
    r[2] = pk_bf16(a[base + 4] * sc, a[base + 5] * sc);
    r[3] = pk_bf16(a[base + 6] * sc, a[base + 7] * sc);
    return r;
}

constexpr int P_XS = 0, P_WS = 16384, P_STAGE = 32768, P_WX = 4 * P_STAGE, P_GT = P_WX + 16384, P_WO = P_GT + 9216, P_CNT = P_WO + 4096,
              P_DUMP = P_CNT + 16, P_LDS = P_DUMP + 256;
#define SBAR() __builtin_amdgcn_sched_barrier(0)

struct PSrc { const unsigned short *xs0, *xs1, *wsr; int ldw; };
DI PSrc proj_src(const unsigned short* x, const unsigned short* wsrc, int ldw, int mt, int nt, int l, int lane) {
    PSrc s;
    const int row = 32 * l + (lane >> 3);
    s.xs0 = x + (size_t)(mt * 128 + row) * 512 + ((lane & 7) ^ ((row >> 1) & 7)) * 8;
    s.xs1 = x + (size_t)(mt * 128 + row + 8) * 512 + ((lane & 7) ^ (((row + 8) >> 1) & 7)) * 8;
    const int krow = 16 * l + (lane >> 4);
    s.wsr = wsrc + (size_t)krow * ldw + ((lane & 15) ^ (((lane >> 4) & 3) << 2)) * 8;
    s.ldw = ldw;
    return s;
}
DI void proj_issue(const PSrc& s, int c, int l, unsigned lds0) {
    const unsigned st = lds0 + (c & 3) * P_STAGE + l * 4096;
    glds16(s.xs0 + c * 64, st + P_XS);
    glds16(s.xs1 + c * 64, st + P_XS + 1024);
    glds16(s.xs0 + c * 64 + 16 * 512, st + P_XS + 2048);
    glds16(s.xs1 + c * 64 + 16 * 512, st + P_XS + 3072);
#pragma unroll
    for (int i = 0; i < 4; ++i) glds16(s.wsr + (size_t)(c * 64 + 4 * i) * s.ldw, st + P_WS + i * 1024);
}
DI float wave_sum63(float v) {
#define DPP_ADD(ctrl, rm) v += __int_as_float(__builtin_amdgcn_update_dpp(0, __float_as_int(v), ctrl, rm, 0xf, false))
    DPP_ADD(0x111, 0xf); DPP_ADD(0x112, 0xf); DPP_ADD(0x114, 0xf); DPP_ADD(0x118, 0xf);
    DPP_ADD(0x142, 0xa); DPP_ADD(0x143, 0xc);
#undef DPP_ADD
    return v;
}
DI void lds_spin(const char* smem, int off, unsigned target) {
    const volatile LDS_AS unsigned* p = (const volatile LDS_AS unsigned*)(uintptr_t)((unsigned)(uintptr_t)smem + off);
    while ((unsigned)__builtin_amdgcn_readfirstlane(*p) < target) __builtin_amdgcn_s_sleep(1);
    asm volatile("" ::: "memory");
}

template <bool ISV>
DI void proj_compute(const PSrc& src, const unsigned short* __restrict__ x, const unsigned short* __restrict__ wx,
                     const unsigned short* __restrict__ gw, const unsigned short* __restrict__ wo, char* __restrict__ ws,
                     int mt, int nt, int w, unsigned lds0, char* smem) {
    const int lane = threadIdx.x & 63;
    const int wm = w >> 1, wn = w & 1;
    const int r = lane & 31, h = lane >> 5;
    unsigned xo[4][2];
#pragma unroll
    for (int tt = 0; tt < 2; ++tt) {
        const int row = wm * 64 + tt * 32 + r;
#pragma unroll
        for (int ks = 0; ks < 4; ++ks) xo[ks][tt] = lds0 + P_XS + row * 128 + (((2 * ks + h) ^ ((row >> 1) & 7)) << 4);
    }
    const int q4 = (lane & 15) >> 2, p4 = lane & 3, g1 = (lane >> 4) & 1;
    unsigned wa[2];
#pragma unroll
    for (int ct = 0; ct < 2; ++ct)
        wa[ct] = lds0 + P_WS + (8 * h + q4) * 256 + ((4 * ((2 * wn + ct) ^ q4) + 2 * g1 + (p4 >> 1)) << 4) + (p4 & 1) * 8;
    f32x16 acc[2][2];
#pragma unroll
    for (int i = 0; i < 16; ++i) { acc[0][0][i] = 0.f; acc[0][1][i] = 0.f; acc[1][0][i] = 0.f; acc[1][1][i] = 0.f; }
    bf16x8 fx[2][4][2], fw[2][4][2];
    auto mm = [&](int bf, int ks) {
#pragma unroll
        for (int ct = 0; ct < 2; ++ct)
#pragma unroll
            for (int tt = 0; tt < 2; ++tt)
                acc[ct][tt] = ISV ? MFMA32(fx[bf][ks][tt], fw[bf][ks][ct], acc[ct][tt]) : MFMA32(fw[bf][ks][ct], fx[bf][ks][tt], acc[ct][tt]);
    };
    auto rd = [&](int bf, int ks, unsigned so) {
#pragma unroll
        for (int tt = 0; tt < 2; ++tt) fx[bf][ks][tt] = lds_b128(xo[ks][tt] + so);
#pragma unroll
        for (int ct = 0; ct < 2; ++ct) fw[bf][ks][ct] = cat8(lds_tr(wa[ct] + so + ks * 4096), lds_tr(wa[ct] + so + ks * 4096 + 1024));
    };
    auto step = [&](int bf, int c) {
        __builtin_amdgcn_s_waitcnt(0xC07F);
        asm volatile("s_barrier" ::: "memory");
        SBAR();
        const unsigned so = (c & 3) * P_STAGE;
#pragma unroll
        for (int ks = 0; ks < 4; ++ks) { rd(bf, ks, so); mm(bf ^ 1, ks); }
#pragma unroll
        for (int i = 0; i < 12; ++i) {
            __builtin_amdgcn_sched_group_barrier(0x008, 1, 0);
            __builtin_amdgcn_sched_group_barrier(0x002, 1, 0);
            __builtin_amdgcn_sched_group_barrier(0x100, 2, 0);
        }
        __builtin_amdgcn_sched_group_barrier(0x008, 4, 0);
        SBAR();
    };
    asm volatile("" : "+v"(xo[0][0]), "+v"(xo[0][1]), "+v"(xo[1][0]), "+v"(xo[1][1]), "+v"(xo[2][0]), "+v"(xo[2][1]),
                      "+v"(xo[3][0]), "+v"(xo[3][1]), "+v"(wa[0]), "+v"(wa[1]),
                      "+v"(acc[0][0]), "+v"(acc[0][1]), "+v"(acc[1][0]), "+v"(acc[1][1]));
    asm volatile("s_waitcnt vmcnt(0)" ::: "memory");
    if (lane == 0) atomicAdd((unsigned*)(smem + P_CNT), 1u);
    lds_spin(smem, P_CNT, 4u);
    rd(0, 0, 0); rd(0, 1, 0); rd(0, 2, 0); rd(0, 3, 0);
    SBAR();
    {
        if (nt >= 8) {
            const unsigned short* es = wx + (size_t)((lane >> 5) * 64 + 16 * w) * 16 + (lane & 31) * 8;
#pragma unroll
            for (int i = 0; i < 4; ++i) glds16(es + i * 2048, lds0 + P_WX + w * 4096 + i * 1024);
        }
        const unsigned short* px = x + (size_t)(mt * 128 + nt * 8 + 2 * w) * 512 + lane * 8;
        glds16(px, lds0 + P_GT + w * 2048);
        glds16(px + 512, lds0 + P_GT + w * 2048 + 1024);
        glds16(gw + lane * 8, lds0 + P_GT + 8192);
        glds16(wo + (size_t)(4 * w + (lane >> 4)) * 512 + ((lane & 15) ^ (((lane >> 4) & 3) << 2)) * 8, lds0 + P_WO + w * 1024);
        const int hdt = (nt & 3) * 8 + wn * 4 + (lane & 3), tlt = mt * 4 + wm * 2;
        const char* td = (nt < 8) ? (ws + WS_Q + (size_t)((nt >> 2) * 32 + hdt) * 65536 + tlt * 1024) : (ws + WS_KV + (size_t)hdt * 131072 + tlt * 2048 + (ISV ? 1024 : 0));
        unsigned sv;
        asm volatile("s_mov_b32 %0, m0\n\ts_mov_b32 m0, %2\n\ts_nop 0\n\tglobal_load_lds_dword %1, off\n\ts_mov_b32 m0, %0"
                     : "=&s"(sv) : "v"(td), "s"(lds0 + P_DUMP) : "memory");
    }

#pragma unroll 1
    for (int c = 1; c < 7; c += 2) { step(1, c); step(0, c + 1); }
    asm volatile("s_waitcnt vmcnt(0)" ::: "memory");
    step(1, 7);
#pragma unroll
    for (int ct = 0; ct < 2; ++ct)
#pragma unroll
        for (int tt = 0; tt < 2; ++tt)
#pragma unroll
            for (int ks = 0; ks < 4; ++ks)
                acc[ct][tt] = ISV ? MFMA32(fx[1][ks][tt], fw[1][ks][ct], acc[ct][tt]) : MFMA32(fw[1][ks][ct], fx[1][ks][tt], acc[ct][tt]);
#pragma unroll
    for (int ct = 0; ct < 2; ++ct)
#pragma unroll
        for (int tt = 0; tt < 2; ++tt) {
            const int hd0 = (nt & 3) * 8 + wn * 4 + ct * 2;
            const int tile = mt * 4 + wm * 2 + tt;
            if (ISV) {
                char* dst = ws + WS_KV + (size_t)(hd0 + (r >> 4)) * 131072 + tile * 2048 + 1024 + h * 256 + (r & 15) * 16;
                gst128(dst, pack8(acc[ct][tt], 0, 1.f));
                gst128(dst + 512, pack8(acc[ct][tt], 8, 1.f));
            } else {
                const bool isq = nt < 8;
                const float sc = isq ? QSCALE : 1.f;
                const size_t hs = isq ? 65536 : 131072;
                char* dst = isq ? (ws + WS_Q + (size_t)((nt >> 2) * 32 + hd0) * 65536 + tile * 1024 + lane * 16)
                                : (ws + WS_KV + (size_t)hd0 * 131072 + tile * 2048 + lane * 16);
                gst128(dst, pack8(acc[ct][tt], 0, sc));
                gst128(dst + hs, pack8(acc[ct][tt], 8, sc));
            }
        }
}

template <int XM>
DI void proj_loader(const PSrc& src, const unsigned short* __restrict__ x, const unsigned short* __restrict__ wk,
                    const unsigned short* __restrict__ wv, const unsigned short* __restrict__ gw, char* __restrict__ ws,
                    int mt, int nt, int l, unsigned lds0, char* smem) {
    const int lane = threadIdx.x & 63;
    const int r = lane & 31, h = lane >> 5;
    constexpr bool isx = XM != 0, isv = XM == 2;
    const int xtt = nt & 3;
    proj_issue(src, 2, l, lds0);
    const int q4 = (lane & 15) >> 2, p4 = lane & 3;
    const unsigned ea = lds0 + P_WX + l * 4096 + (8 * h + q4) * 32 + p4 * 8;
    const int xrow = xtt * 32 + r;
    const unsigned exa = lds0 + P_XS + xrow * 128 + (((2 * l + h) ^ ((xrow >> 1) & 7)) << 4);
    const int gtok = mt * 128 + nt * 8 + 2 * l;
    u32x4 gx0, gx1, gv;
    bf16x8 xe[8];
    if (isx) { lds_spin(smem, P_CNT, 4u); xe[0] = lds_b128(exa); }
#pragma unroll
    for (int c = 1; c < 8; ++c) {
        if (c < 7) WAIT_BAR(8); else WAIT_BAR(0);
        if (isx) xe[c] = lds_b128(exa + (c & 3) * P_STAGE);
        if (c < 6) proj_issue(src, c + 2, l, lds0);
    }
    gx0 = *(const u32x4*)(smem + P_GT + l * 2048 + lane * 16);
    gx1 = *(const u32x4*)(smem + P_GT + l * 2048 + 1024 + lane * 16);
    gv  = *(const u32x4*)(smem + P_GT + 8192 + lane * 16);
    {
        const int g1 = (lane >> 4) & 1;
        const unsigned woa = lds0 + P_WO + (4 * h + q4) * 256 + ((4 * (l ^ q4) + 2 * g1 + (p4 >> 1)) << 4) + (p4 & 1) * 8;
        const bf16x8 f = cat8(lds_tr(woa), lds_tr(woa + 2048));
        gst128(ws + WS_WO + (size_t)(blockIdx.x * 4 + l) * 1024 + lane * 16, __builtin_bit_cast(u32x4, f));
    }
    {
        float s0 = 0.f, s1 = 0.f;
#pragma unroll
        for (int i = 0; i < 4; ++i) {
            const float wl = __uint_as_float(gv[i] << 16), wh = __uint_as_float(gv[i] & 0xffff0000u);
            s0 += __uint_as_float(gx0[i] << 16) * wl + __uint_as_float(gx0[i] & 0xffff0000u) * wh;
            s1 += __uint_as_float(gx1[i] << 16) * wl + __uint_as_float(gx1[i] & 0xffff0000u) * wh;
        }
        s0 = wave_sum63(s0); s1 = wave_sum63(s1);
        if (lane == 63) {
            f32x2 g = {__builtin_amdgcn_rcpf(1.f + __expf(-s0)), __builtin_amdgcn_rcpf(1.f + __expf(-s1))};
            *(f32x2*)((float*)(ws + WS_G) + gtok) = g;
        }
    }
    f32x16 acce;
#pragma unroll
    for (int i = 0; i < 16; ++i) acce[i] = 0.f;
    if (isx) {
        __builtin_amdgcn_s_setprio(3);
#pragma unroll
        for (int c = 0; c < 8; ++c) {
            const bf16x8 we = cat8(lds_tr(ea + c * 512), lds_tr(ea + c * 512 + 128));
            acce = isv ? MFMA32(xe[c], we, acce) : MFMA32(we, xe[c], acce);
        }
    }
    if (isx) {
        f32x4* red = (f32x4*)smem;
#pragma unroll
        for (int g = 0; g < 4; ++g) {
            f32x4 v = {acce[4 * g], acce[4 * g + 1], acce[4 * g + 2], acce[4 * g + 3]};
            red[(l * 4 + g) * 64 + lane] = v;
        }
        unsigned old = 0;
        asm volatile("s_waitcnt lgkmcnt(0)" ::: "memory");
        if (lane == 0) old = atomicAdd((unsigned*)(smem + P_CNT), 1u);
        old = __builtin_amdgcn_readfirstlane(old);
        if (old == 7) {
            asm volatile("" ::: "memory");
#pragma unroll
            for (int g = 0; g < 4; ++g) {
                const f32x4 v = red[g * 64 + lane] + red[(4 + g) * 64 + lane] + red[(8 + g) * 64 + lane] + red[(12 + g) * 64 + lane];
#pragma unroll
                for (int i = 0; i < 4; ++i) acce[4 * g + i] = v[i];
            }
            char* base = ws + WS_KV + (size_t)32 * 131072 + (mt * 4 + xtt) * 2048;
            if (isv) {
                if (r < 16) {
                    gst128(base + 1024 + h * 256 + r * 16, pack8(acce, 0, 1.f));
                    gst128(base + 1536 + h * 256 + r * 16, pack8(acce, 8, 1.f));
                }
            } else {
                gst128(base + lane * 16, pack8(acce, 0, 1.f));
            }
        }
    }
}

__global__ __launch_bounds__(512, 2) void proj_kernel(const unsigned short* __restrict__ x, const unsigned short* __restrict__ wq,
                                                      const unsigned short* __restrict__ wk, const unsigned short* __restrict__ wv,
                                                      const unsigned short* __restrict__ wqkv, const unsigned short* __restrict__ gw,
                                                      const unsigned short* __restrict__ woA, const unsigned short* __restrict__ woB,
                                                      char* __restrict__ ws) {
    extern __shared__ __attribute__((aligned(16))) char smem[];
    const unsigned lds0 = (unsigned)(uintptr_t)smem;
    const int w = __builtin_amdgcn_readfirstlane(threadIdx.x >> 6);
    const int b = blockIdx.x, xcd = b & 7, slot = b >> 3;
    const int mt = (xcd & 3) * 4 + (slot & 3), nt = (xcd >> 2) * 8 + (slot >> 2);
    if (w == 7) {
        unsigned long long pc; int len;
        asm volatile("s_getpc_b64 %0\n\tpf_text_begin:\n\ts_mov_b32 %1, pf_text_end-pf_text_begin" : "=s"(pc), "=s"(len));
        int off = ((slot & 7) * 32 + (int)(threadIdx.x & 31)) * 128;
        off = off < len ? off : 0;
        unsigned sv;
        asm volatile("s_mov_b32 %0, m0\n\ts_mov_b32 m0, %3\n\ts_nop 0\n\tglobal_load_lds_dword %1, %2\n\ts_mov_b32 m0, %0"
                     : "=&s"(sv) : "v"(off), "s"(pc), "s"(lds0 + P_DUMP) : "memory");
    }
    if (threadIdx.x == 0) *(unsigned*)(smem + P_CNT) = 0u;
    const unsigned short* wsrc = (nt < 4) ? (wq + nt * 128) : (wqkv + (nt - 4) * 128);
    const PSrc src = proj_src(x, wsrc, (nt < 4) ? 512 : 1536, mt, nt, w & 3, threadIdx.x & 63);
    const unsigned short* wo = ((b < 128) ? woA : woB) + (size_t)((b >> 2) & 31) * 16 * 512 + (b & 3) * 128;
    proj_issue(src, w >> 2, w & 3, lds0);
    asm volatile("s_waitcnt lgkmcnt(0)\n\ts_barrier" ::: "memory");
    if (w >= 4) {
        if (nt >= 12)     proj_loader<2>(src, x, wk, wv, gw, ws, mt, nt, w - 4, lds0, smem);
        else if (nt >= 8) proj_loader<1>(src, x, wk, wv, gw, ws, mt, nt, w - 4, lds0, smem);
        else              proj_loader<0>(src, x, wk, wv, gw, ws, mt, nt, w - 4, lds0, smem);
        if (b == 0 && w == 4 && (threadIdx.x & 63) < 4) *(unsigned*)(ws + WS_C + (threadIdx.x & 63) * 256) = 0u;
    } else if (nt >= 12) proj_compute<true>(src, x, wv, gw, wo, ws, mt, nt, w, lds0, smem);
    else                 proj_compute<false>(src, x, wk, gw, wo, ws, mt, nt, w, lds0, smem);
}

constexpr int A_KV = 0, A_CONST = 131072, A_LDS = A_CONST + 8192;

DI float max16(const f32x16& s) {
    float a = fmaxf(fmaxf(s[0], s[1]), s[2]), b = fmaxf(fmaxf(s[3], s[4]), s[5]);
    a = fmaxf(fmaxf(a, s[6]), s[7]);   b = fmaxf(fmaxf(b, s[8]), s[9]);
    a = fmaxf(fmaxf(a, s[10]), s[11]); b = fmaxf(fmaxf(b, s[12]), s[13]);
    a = fmaxf(fmaxf(a, s[14]), s[15]);
    return fmaxf(a, b);
}
DI float half_max(float m) {
    auto rr = __builtin_amdgcn_permlane32_swap(__float_as_uint(m), __float_as_uint(m), false, false);
    return fmaxf(__uint_as_float(rr[0]), __uint_as_float(rr[1]));
}

#define R_SA "192"
#define R_SB "208"
#define EX(X, i) "v_exp_f32 v[" X "+" #i "], v[" X "+" #i "]\n\t"
#define CV(X, j, a, b) "v_cvt_pk_bf16_f32 v[224+" #j "], v[" X "+" #a "], v[" X "+" #b "]\n\t"
#define HALF(X, Y, OX, OY, NY, QY, WA, RA, WB, RB, WC, RC)                                        \
    EX(X, 0) EX(X, 1) WA                                                                          \
    "v_mfma_f32_32x32x16_bf16 " OY ", %[v1], v[228:231], " OY "\n\t" RA                           \
    EX(X, 2) EX(X, 3) CV(X, 0, 0, 1) EX(X, 4) EX(X, 5) CV(X, 1, 2, 3) WB                          \
    "v_mfma_f32_32x32x16_bf16 v[" Y ":" Y "+15], %[kf], " QY ", " NY "\n\t" RB                    \
    EX(X, 6) EX(X, 7) CV(X, 2, 4, 5) EX(X, 8) EX(X, 9) CV(X, 3, 6, 7) EX(X, 10) EX(X, 11) WC      \
    "v_mfma_f32_32x32x16_bf16 " OX ", %[v0], v[224:227], " OX "\n\t" RC                           \
    CV(X, 4, 8, 9) EX(X, 12) EX(X, 13) CV(X, 5, 10, 11) EX(X, 14) EX(X, 15) CV(X, 6, 12, 13) CV(X, 7, 14, 15)
#define TILE(OFF_V1, OFF_KN, OFF_V0N, P0, P1)                                                     \
    HALF(R_SA, R_SB, "%[oa]", "%[ob]", "%[nb]", "%[qb]",                                          \
         "", "ds_read_b128 %[v1], %[va] offset:" OFF_V1 "\n\t" P0,                                \
         "", "ds_read_b128 %[kf], %[ka] offset:" OFF_KN "\n\t",                                   \
         "s_waitcnt lgkmcnt(2)\n\t", "")                                                          \
    HALF(R_SB, R_SA, "%[ob]", "%[oa]", "%[na]", "%[qa]",                                          \
         "s_waitcnt lgkmcnt(1)\n\t", P1,                                                          \
         "s_waitcnt lgkmcnt(0)\n\t", "",                                                          \
         "", "ds_read_b128 %[v0], %[va] offset:" OFF_V0N "\n\t")
#define KVDMA                                                                                     \
    "global_load_lds_dwordx4 %[lo], s[40:41]\n\t"                                                 \
    "s_add_u32 s40, s40, 0x2000\n\ts_addc_u32 s41, s41, 0\n\ts_add_u32 m0, m0, 0x2000\n\ts_nop 0\n\t"
#define WOLD(R)                                                                                   \
    "global_load_dwordx4 v[" #R ":" #R "+3], %[lo], s[42:43]\n\t"                                 \
    "global_load_dwordx4 v[" #R "+4:" #R "+7], %[lo], s[42:43] offset:1024\n\t"                   \
    "s_add_u32 s42, s42, 0x4000\n\ts_addc_u32 s43, s43, 0\n\t"
#define ATTN_ASM(PY, PO)                                                                          \
    asm volatile(                                                                                 \
        "s_mov_b32 s44, m0\n\ts_mov_b32 m0, %[kd]\n\ts_mov_b64 s[40:41], %[kb]\n\ts_mov_b64 s[42:43], %[wb]\n\ts_cmp_lg_u32 %[yg], 0\n\ts_cselect_b64 vcc, -1, 0\n\t"               \
        "ds_read_b128 %[kf], %[ka]\n\t"                                                           \
        "ds_read_b128 %[v1], %[va]\n\t"                                                           \
        "ds_read_b128 %[v0], %[va]\n\t"                                                           \
        "v_mov_b32 v228, 0\n\tv_mov_b32 v229, 0\n\tv_mov_b32 v230, 0\n\tv_mov_b32 v231, 0\n\t"    \
        "s_waitcnt lgkmcnt(0)\n\t"                                                                \
        "v_mfma_f32_32x32x16_bf16 v[192:207], %[kf], %[qa], %[na]\n\t"                            \
        "s_nop 7\n\ts_nop 7\n\t"                                                                  \
        "L_loop_%=:\n\t"                                                                          \
        "s_bitcmp1_b32 %[mk], %[cnt]\n\t"                                                         \
        "s_cbranch_scc1 L_sp_%=\n\t"                                                              \
        "L_go_%=:\n\t"                                                                            \
        TILE("512", "2048", "2048", "s_setprio 0\n\t", PY)                                        \
        TILE("2560", "4096", "4096", PO, "")                                                      \
        TILE("4608", "6144", "6144", "", "")                                                      \
        HALF(R_SA, R_SB, "%[oa]", "%[ob]", "%[nb]", "%[qb]",                                      \
             "", "ds_read_b128 %[v1], %[va] offset:6656\n\t",                                     \
             "", "ds_read_b128 %[kf], %[ka] offset:8192\n\tv_add_u32 %[va], %[va], %[vs]\n\t",    \
             "s_waitcnt lgkmcnt(2)\n\t", "v_add_u32 %[ka], 0x2000, %[ka]\n\t")                    \
        HALF(R_SB, R_SA, "%[ob]", "%[oa]", "%[na]", "%[qa]",                                      \
             "s_waitcnt lgkmcnt(1)\n\t", "s_add_u32 %[cnt], %[cnt], 1\n\t",                       \
             "s_waitcnt lgkmcnt(0)\n\t", "s_and_b32 s45, %[cnt], 15\n\t",                           \
             "", "ds_read_b128 %[v0], %[va]\n\t")                                                 \
        "s_cbranch_scc1 L_loop_%=\n\t"                                                            \
        "s_branch L_end_%=\n\t"                                                                   \
        "L_sp_%=:\n\t"                                                                            \
        "s_cmp_gt_u32 %[cnt], 2\n\t"                                                              \
        "s_cbranch_scc1 L_s3_%=\n\t"                                                              \
        KVDMA KVDMA KVDMA KVDMA                                                                   \
        "s_branch L_go_%=\n\t"                                                                    \
        "L_s3_%=:\n\t"                                                                            \
        "s_cmp_gt_u32 %[cnt], 3\n\t"                                                              \
        "s_cbranch_scc1 L_s4_%=\n\t"                                                              \
        "s_waitcnt vmcnt(0)\n\t"                                                                  \
        "s_barrier\n\t"                                                                           \
        "s_branch L_go_%=\n\t"                                                                    \
        "L_s4_%=:\n\t"                                                                            \
        "s_cmp_gt_u32 %[cnt], 8\n\t"                                                              \
        "s_cbranch_scc1 L_s5_%=\n\t"                                                              \
        "v_mov_b32 v232, -1\n\tv_mov_b32 v233, -1\n\tv_mov_b32 v234, -1\n\tv_mov_b32 v235, -1\n\t" \
        "s_mov_b64 s[40:41], %[ob2]\n\t"                                                          \
        "global_store_dwordx4 %[lo], v[232:235], s[40:41] sc1\n\t"                                \
        "s_add_u32 s40, s40, 0x10000\n\ts_addc_u32 s41, s41, 0\n\t"                               \
        "global_store_dwordx4 %[lo], v[232:235], s[40:41] sc1\n\t"                                \
        WOLD(128) WOLD(136) WOLD(144) WOLD(152) WOLD(160) WOLD(168) WOLD(176) WOLD(184)           \
        "s_branch L_go_%=\n\t"                                                                    \
        "L_s5_%=:\n\t"                                                                            \
        "s_cmp_gt_u32 %[cnt], 11\n\t"                                                             \
        "s_cbranch_scc1 L_s6_%=\n\t"                                                              \
        "s_waitcnt vmcnt(0)\n\t"                                                                  \
        "s_mov_b64 exec, 1\n\t"                                                                   \
        "v_mov_b32 v232, 1\n\t"                                                                   \
        "global_atomic_add %[pf], v232, off sc1\n\t"                                              \
        "s_mov_b64 exec, -1\n\t"                                                                  \
        "global_load_dword v233, %[ot], off\n\t"                                                  \
        "s_branch L_go_%=\n\t"                                                                    \
        "L_s6_%=:\n\t"                                                                            \
        "global_load_dword %[cv], %[pf], off sc1\n\t"                                             \
        "s_branch L_go_%=\n\t"                                                                    \
        "L_end_%=:\n\t"                                                                           \
        "s_nop 1\n\t"                                                                             \
        "v_mfma_f32_32x32x16_bf16 %[ob], %[v1], v[228:231], %[ob]\n\t"                            \
        "s_waitcnt vmcnt(0) lgkmcnt(0)\n\t"                                                       \
        "s_mov_b32 m0, s44\n\t"                                                                \
        "s_nop 7\n\ts_nop 7\n\t"                                                                  \
        : [oa] "+v"(oa), [ob] "+v"(ob), [ka] "+v"(ka), [va] "+v"(va), [cnt] "+s"(cnt), [cv] "+v"(cv), \
          [kf] "=&v"(kf), [v0] "=&v"(v0), [v1] "=&v"(v1),                                         \
          "+{v[128:143]}"(w0), "+{v[144:159]}"(w1), "+{v[160:175]}"(w2), "+{v[176:191]}"(w3)      \
        : [qa] "v"(qa), [qb] "v"(qb), [na] "v"(na), [nb] "v"(nb), [vs] "v"(vstep), [yg] "s"(yg), [pf] "v"(pf), \
          [lo] "v"(lo), [kb] "s"(kb), [kd] "s"(kd), [wb] "s"(wb), [ob2] "s"(ob2), [mk] "s"(mk), [ot] "v"(ot)                                                \
        : "memory", "scc", "vcc", "s40", "s41", "s42", "s43", "s44", "s45",                                                                        \
          "v192", "v193", "v194", "v195", "v196", "v197", "v198", "v199", "v200", "v201", "v202", "v203", "v204", "v205", "v206", "v207", \
          "v208", "v209", "v210", "v211", "v212", "v213", "v214", "v215", "v216", "v217", "v218", "v219", "v220", "v221", "v222", "v223", \
          "v224", "v225", "v226", "v227", "v228", "v229", "v230", "v231", "v232", "v233", "v234", "v235")

DI void attn_main(bool young, bool first, unsigned ka, unsigned va, unsigned vstep, const bf16x8& qa, const bf16x8& qb, const f32x16& na,
                  const f32x16& nb, f32x16& oa, f32x16& ob, const char* pf, unsigned lo, const char* kb, unsigned kd,
                  const char* wb, const char* ob2, u32x16& w0, u32x16& w1, u32x16& w2, u32x16& w3, unsigned& cv, const void* ot) {
    bf16x8 kf, v0, v1;
    int cnt = __builtin_amdgcn_readfirstlane(first ? 0 : 16);
    const int yg = __builtin_amdgcn_readfirstlane(young ? 1 : 0);
    const int mk = __builtin_amdgcn_readfirstlane(young ? 0x422f : 0x448f);
    ATTN_ASM("s_cbranch_vccz L_py_%=\n\ts_setprio 1\n\tL_py_%=:\n\t", "s_setprio 1\n\t");
}

__global__ __launch_bounds__(512, 2) void attn_kernel(char* __restrict__ ws, unsigned short* __restrict__ out) {
    extern __shared__ __attribute__((aligned(16))) char smem[];
    const unsigned lds0 = (unsigned)(uintptr_t)smem;
    const int tid = threadIdx.x, lane = tid & 63;
    const int w = __builtin_amdgcn_readfirstlane(tid >> 6);
    const int r = lane & 31, h = lane >> 5;
    const int b = blockIdx.x, xcd = b & 7, slot = b >> 3;
    const int isB = (slot < 16);
    const int hd = xcd * 4 + ((slot & 15) >> 2);
    const int hh = isB ? 32 + hd : hd;
    const int kvh = isB ? hd : 32;
    const int tile = (slot & 3) * 16 + 2 * w;

    bf16x8 qa, qb;
    {
        const char* qsrc = ws + WS_Q + (size_t)hh * 65536 + tile * 1024 + lane * 16;
        asm volatile("global_load_dwordx4 %0, %2, off\n\tglobal_load_dwordx4 %1, %2, off offset:1024"
                     : "=&v"(qa), "=&v"(qb) : "v"(qsrc) : "memory");
    }
    const char* kvsrc = ws + WS_KV + (size_t)kvh * 131072 + w * 1024 + lane * 16;
#pragma unroll
    for (int i = 0; i < 4; ++i) glds16(kvsrc + i * 8192, lds0 + A_KV + i * 8192 + w * 1024);
    {
        const unsigned one = ((tid & 15) == 0 || (tid & 15) == 4) ? 0x3f803f80u : 0u;
        u32x4 c = {one, one, one, one};
        *(u32x4*)(smem + A_CONST + tid * 16) = c;
    }
    const unsigned kaddr = lds0 + A_KV + lane * 16;
    const unsigned vaddr = (r < 16) ? (lds0 + A_KV + 1024 + h * 256 + r * 16) : (lds0 + A_CONST + (r - 16) * 16);
    const unsigned vstep = (r < 16) ? 8192u : 0u;

    float ga, gb;
    {
        const float* gsrc = (const float*)(ws + WS_G) + tile * 32 + r;
        asm volatile("global_load_dword %0, %2, off\n\tglobal_load_dword %1, %2, off offset:128" : "=&v"(ga), "=&v"(gb) : "v"(gsrc) : "memory");
    }
    const char* kb = ws + WS_KV + (size_t)kvh * 131072 + 32768 + w * 1024;
    const unsigned kd = lds0 + A_KV + 32768 + w * 1024;
    const int qblk = slot & 3;
    const int mt = qblk * 8 + (hh >> 3), nt = hh & 7;
    char* obase = ws + WS_O + (size_t)tile * 65536 + hh * 1024;
    char* odst = obase + lane * 16;
    unsigned short* optr = out + (size_t)(mt * 64 + ((w >> 1) & 1) * 32 + r) * 512 + nt * 64 + (w >> 2) * 32 + (w & 1) * 16 + h * 8;
    const char* cnt = ws + WS_C + qblk * 256;
    unsigned cv = 0u;
    const char* wb = ws + WS_WO + (size_t)(w * 8) * 16384 + (nt * 2) * 1024;
    u32x16 w0, w1, w2, w3;
    asm volatile("" : "={v[128:143]}"(w0), "={v[144:159]}"(w1), "={v[160:175]}"(w2), "={v[176:191]}"(w3));
    f32x16 zero;
#pragma unroll
    for (int i = 0; i < 16; ++i) zero[i] = 0.f;
    f32x16 na, nb, oa = zero, ob = zero;
    asm volatile("" : "+v"(oa), "+v"(ob), "+s"(kb), "+s"(wb), "+s"(obase), "+v"(cnt));
    asm volatile("s_waitcnt vmcnt(2) lgkmcnt(0)\n\ts_barrier" : "+v"(qa), "+v"(qb) :: "memory");
    {
        const bf16x8 k0 = lds_b128(kaddr);
        const f32x16 sa = MFMA32(k0, qa, zero);
        const f32x16 sb = MFMA32(k0, qb, zero);
        SBAR();
        const float ma = -half_max(max16(sa)) - REFOFF, mb = -half_max(max16(sb)) - REFOFF;
#pragma unroll
        for (int i = 0; i < 16; ++i) { na[i] = ma; nb[i] = mb; }
    }
    attn_main(w >= 4, true, kaddr, vaddr, vstep, qa, qb, na, nb, oa, ob, cnt, lane * 16, kb, kd, wb, obase, w0, w1, w2, w3, cv, optr);
    asm volatile("" : "+v"(ga), "+v"(gb));

    {
        float xa = fabsf(oa[0]), xb = fabsf(ob[0]), ta = oa[0], tb = ob[0];
#pragma unroll
        for (int i = 1; i < 8; ++i) { xa = fmaxf(xa, fabsf(oa[i])); xb = fmaxf(xb, fabsf(ob[i])); ta += oa[i]; tb += ob[i]; }
        const bool bad = !(oa[8] < 1.329228e36f) || !(ob[8] < 1.329228e36f) || !(fabsf(ta) <= 3.0e38f) || !(fabsf(tb) <= 3.0e38f) ||
                         !(xa > 7.7e-34f) || !(xb > 7.7e-34f);
        if (__any(bad)) {
            float ma = -INFINITY, mb = -INFINITY;
#pragma unroll 1
            for (int t = 0; t < 64; ++t) {
                const bf16x8 kf = lds_b128(kaddr + t * 2048);
                ma = fmaxf(ma, max16(MFMA32(kf, qa, zero)));
                mb = fmaxf(mb, max16(MFMA32(kf, qb, zero)));
            }
            ma = -half_max(ma); mb = -half_max(mb);
#pragma unroll
            for (int i = 0; i < 16; ++i) { na[i] = ma; nb[i] = mb; }
            oa = zero; ob = zero;
            attn_main(false, false, kaddr, vaddr, vstep, qa, qb, na, nb, oa, ob, cnt, lane * 16, kb, kd, wb, obase, w0, w1, w2, w3, cv, optr);
        }
    }
    const float sa = (isB ? 1.f - ga : ga) * __builtin_amdgcn_rcpf(oa[8]), sb = (isB ? 1.f - gb : gb) * __builtin_amdgcn_rcpf(ob[8]);
    {
        u32x4 pa = pack8(oa, 0, sa), pb = pack8(ob, 0, sb);
#pragma unroll
        for (int i = 0; i < 4; ++i) { pa[i] = min(pa[i], 0xfffffffeu); pb[i] = min(pb[i], 0xfffffffeu); }
        gst128(odst, pa);
        gst128(odst + 65536, pb);
    }
    for (unsigned spins = 0; spins < SPIN_CAP && (unsigned)__builtin_amdgcn_readfirstlane(cv) < 512u; ++spins) {
        if ((unsigned)__builtin_amdgcn_readfirstlane(__hip_atomic_load((const unsigned*)cnt, __ATOMIC_RELAXED, __HIP_MEMORY_SCOPE_AGENT)) >= 512u) break;
        __builtin_amdgcn_s_sleep(2);
    }
    asm volatile("s_barrier" ::: "memory");
    u32x4 wf[8][2];
#define WSUB(W, k) wf[k][0] = __builtin_shufflevector(W, W, 0, 1, 2, 3); wf[k][1] = __builtin_shufflevector(W, W, 4, 5, 6, 7); \
                   wf[k + 1][0] = __builtin_shufflevector(W, W, 8, 9, 10, 11); wf[k + 1][1] = __builtin_shufflevector(W, W, 12, 13, 14, 15);
    WSUB(w0, 0) WSUB(w1, 2) WSUB(w2, 4) WSUB(w3, 6)
    f32x16 acc[4] = {zero, zero, zero, zero};
    {
        const char* osrc = ws + WS_O + (size_t)(mt * 2) * 65536 + (w * 8) * 1024;
        const unsigned lo16 = lane * 16;
        u32x4 of[8][2];
        auto valid = [&](int k) -> bool {
            const unsigned m = max(max(max(of[k][0][0], of[k][0][1]), max(of[k][0][2], of[k][0][3])),
                                   max(max(of[k][1][0], of[k][1][1]), max(of[k][1][2], of[k][1][3])));
            return !__any(m == 0xffffffffu);
        };
        auto mul = [&](int k) {
#pragma unroll
            for (int q = 0; q < 4; ++q)
                acc[q] = MFMA32(__builtin_bit_cast(bf16x8, wf[k][q >> 1]), __builtin_bit_cast(bf16x8, of[k][q & 1]), acc[q]);
        };
#pragma unroll
        for (int k = 0; k < 8; ++k)
            asm volatile("global_load_dwordx4 %0, %2, %3 offset:%5 sc1\n\tglobal_load_dwordx4 %1, %2, %4 offset:%5 sc1"
                         : "=&v"(of[k][0]), "=&v"(of[k][1])
                         : "v"(lo16), "s"(osrc + (k >> 2) * 4096), "s"(osrc + 65536 + (k >> 2) * 4096), "i"((k & 3) * 1024) : "memory");
        auto settle = [&](int k0, int k1) {
            const unsigned need = ((1u << k1) - 1u) & ~((1u << k0) - 1u);
            for (unsigned spins = 0; spins < SPIN_CAP; ++spins) {
                asm volatile("s_waitcnt vmcnt(0)"
                             : "+v"(of[0][0]), "+v"(of[0][1]), "+v"(of[1][0]), "+v"(of[1][1]), "+v"(of[2][0]), "+v"(of[2][1]), "+v"(of[3][0]), "+v"(of[3][1]),
                               "+v"(of[4][0]), "+v"(of[4][1]), "+v"(of[5][0]), "+v"(of[5][1]), "+v"(of[6][0]), "+v"(of[6][1]), "+v"(of[7][0]), "+v"(of[7][1])
                             :: "memory");
                unsigned pend = 0u;
#pragma unroll
                for (int k = k0; k < 8; ++k) if (!valid(k)) pend |= 1u << k;
                if ((pend & need) == 0u) break;
                __builtin_amdgcn_s_sleep(2);
#pragma unroll
                for (int k = k0; k < 8; ++k)
                    if (pend & (1u << k))
                        asm volatile("global_load_dwordx4 %0, %2, %3 offset:%5 sc1\n\tglobal_load_dwordx4 %1, %2, %4 offset:%5 sc1"
                                     : "+v"(of[k][0]), "+v"(of[k][1])
                                     : "v"(lo16), "s"(osrc + (k >> 2) * 4096), "s"(osrc + 65536 + (k >> 2) * 4096), "i"((k & 3) * 1024) : "memory");
            }
        };
        asm volatile("s_waitcnt vmcnt(8)" : "+v"(of[0][0]), "+v"(of[0][1]), "+v"(of[1][0]), "+v"(of[1][1]), "+v"(of[2][0]), "+v"(of[2][1]),
                                            "+v"(of[3][0]), "+v"(of[3][1]) :: "memory");
        if (!(valid(0) && valid(1) && valid(2) && valid(3))) settle(0, 4);
        mul(0); mul(1); mul(2); mul(3);
        SBAR();
        settle(4, 8);
        mul(4); mul(5); mul(6); mul(7);
    }
    float fin[8];
#pragma unroll
    for (int p = 0; p < 8; ++p) {
        if (p == w) {
#pragma unroll
            for (int i = 0; i < 8; ++i) fin[i] = acc[p >> 1][8 * (p & 1) + i];
        } else {
            char* dst = smem + (p * 7 + ((w - p - 1) & 7)) * 2048 + lane * 16;
#pragma unroll
            for (int g = 0; g < 2; ++g) {
                f32x4 v;
#pragma unroll
                for (int i = 0; i < 4; ++i) v[i] = acc[p >> 1][8 * (p & 1) + 4 * g + i];
                *(f32x4*)(dst + g * 1024) = v;
            }
        }
    }
    asm volatile("s_waitcnt lgkmcnt(0)\n\ts_barrier" ::: "memory");
#pragma unroll
    for (int p = 0; p < 7; ++p)
#pragma unroll
        for (int g = 0; g < 2; ++g) {
            const f32x4 v = *(const f32x4*)(smem + (w * 7 + p) * 2048 + g * 1024 + lane * 16);
#pragma unroll
            for (int i = 0; i < 4; ++i) fin[4 * g + i] += v[i];
        }
    const unsigned a0 = pk_bf16(fin[0], fin[1]), a1 = pk_bf16(fin[2], fin[3]);
    const unsigned b0 = pk_bf16(fin[4], fin[5]), b1 = pk_bf16(fin[6], fin[7]);
    auto r0 = __builtin_amdgcn_permlane32_swap(a0, b0, false, false);
    auto r1 = __builtin_amdgcn_permlane32_swap(a1, b1, false, false);
    u32x4 res = {r0[0], r1[0], r0[1], r1[1]};
    gst128(optr, res);
    asm volatile("pf_text_end:");
}

extern "C" void kernel_launch(void* const* d_in, const int* in_sizes, int n_in, void* d_out, int out_size, void* d_ws,
                              size_t ws_size, hipStream_t stream) {
    const unsigned short* x    = (const unsigned short*)d_in[0];
    const unsigned short* wq   = (const unsigned short*)d_in[1];
    const unsigned short* wk   = (const unsigned short*)d_in[2];
    const unsigned short* wv   = (const unsigned short*)d_in[3];
    const unsigned short* woA  = (const unsigned short*)d_in[4];
    const unsigned short* wqkv = (const unsigned short*)d_in[6];
    const unsigned short* woB  = (const unsigned short*)d_in[7];
    const unsigned short* gw   = (const unsigned short*)d_in[8];
    char* ws = (char*)d_ws;
    (void)hipFuncSetAttribute((const void*)proj_kernel, hipFuncAttributeMaxDynamicSharedMemorySize, P_LDS);
    (void)hipFuncSetAttribute((const void*)attn_kernel, hipFuncAttributeMaxDynamicSharedMemorySize, A_LDS);
    proj_kernel<<<256, 512, P_LDS, stream>>>(x, wq, wk, wv, wqkv, gw, woA, woB, ws);
    attn_kernel<<<256, 512, A_LDS, stream>>>(ws, (unsigned short*)d_out);
}
```

```cpp
#include <hip/hip_runtime.h>
#include <hip/hip_bf16.h>
#include <stdint.h>

typedef short    bf16x8 __attribute__((ext_vector_type(8)));
typedef short    s16x4  __attribute__((ext_vector_type(4)));
typedef float    f32x16 __attribute__((ext_vector_type(16)));
typedef float    f32x4  __attribute__((ext_vector_type(4)));
typedef float    f32x2  __attribute__((ext_vector_type(2)));
typedef unsigned u32x4  __attribute__((ext_vector_type(4)));
typedef unsigned u32x16 __attribute__((ext_vector_type(16)));
typedef __bf16   bf16x2v __attribute__((ext_vector_type(2)));

#define DI __device__ __forceinline__
#define MFMA32(a, b, c) __builtin_amdgcn_mfma_f32_32x32x16_bf16((a), (b), (c), 0, 0, 0)
#define LDS_AS __attribute__((address_space(3)))

constexpr size_t WS_Q  = 0;
constexpr size_t WS_KV = WS_Q + (size_t)64 * 65536;
constexpr size_t WS_O  = WS_KV + (size_t)34 * 131072;
constexpr size_t WS_WO = WS_O + (size_t)64 * 65536;
constexpr size_t WS_G  = WS_WO + (size_t)1024 * 1024;
constexpr size_t WS_C  = WS_G + 8192;

constexpr float QSCALE = 0.25f * 1.4426950408889634f;
constexpr float REFOFF = 96.f;
constexpr unsigned SPIN_CAP = 1u << 20;

DI unsigned pk_bf16(float lo, float hi) {
    f32x2 v = {lo, hi};
    bf16x2v r = __builtin_convertvector(v, bf16x2v);
    return __builtin_bit_cast(unsigned, r);
}

DI void glds16(const void* g, unsigned lds_base) {
    unsigned sv;
    asm volatile("s_mov_b32 %0, m0\n\ts_mov_b32 m0, %2\n\ts_nop 0\n\tglobal_load_lds_dwordx4 %1, off\n\ts_mov_b32 m0, %0"
                 : "=&s"(sv) : "v"(g), "s"(lds_base) : "memory");
}
#define WAIT_BAR(N) asm volatile("s_waitcnt vmcnt(" #N ") lgkmcnt(0)\n\ts_barrier" ::: "memory")

DI void gst128(void* p, u32x4 v) { asm volatile("global_store_dwordx4 %0, %1, off sc1\n\ts_nop 1" :: "v"(p), "v"(v) : "memory"); }
DI bf16x8 lds_b128(unsigned addr) { return *(const LDS_AS bf16x8*)(uintptr_t)addr; }
DI s16x4 lds_tr(unsigned addr) {
    return __builtin_bit_cast(s16x4, __builtin_amdgcn_ds_read_tr16_b64_v4i16((LDS_AS s16x4*)(uintptr_t)addr));
}
DI bf16x8 cat8(s16x4 a, s16x4 b) { return __builtin_shufflevector(a, b, 0, 1, 2, 3, 4, 5, 6, 7); }

DI u32x4 pack8(const f32x16& a, int base, float sc) {
    u32x4 r;
    r[0] = pk_bf16(a[base + 0] * sc, a[base + 1] * sc);
    r[1] = pk_bf16(a[base + 2] * sc, a[base + 3] * sc);
    r[2] = pk_bf16(a[base + 4] * sc, a[base + 5] * sc);
    r[3] = pk_bf16(a[base + 6] * sc, a[base + 7] * sc);
    return r;
}

constexpr int P_XS = 0, P_WS = 16384, P_STAGE = 32768, P_WX = 4 * P_STAGE, P_GT = P_WX + 16384, P_WO = P_GT + 9216, P_CNT = P_WO + 4096,
              P_DUMP = P_CNT + 16, P_LDS = P_DUMP + 256;
#define SBAR() __builtin_amdgcn_sched_barrier(0)

struct PSrc { const unsigned short *xs0, *xs1, *wsr; int ldw; };
DI PSrc proj_src(const unsigned short* x, const unsigned short* wsrc, int ldw, int mt, int nt, int l, int lane) {
    PSrc s;
    const int row = 32 * l + (lane >> 3);
    s.xs0 = x + (size_t)(mt * 128 + row) * 512 + ((lane & 7) ^ ((row >> 1) & 7)) * 8;
    s.xs1 = x + (size_t)(mt * 128 + row + 8) * 512 + ((lane & 7) ^ (((row + 8) >> 1) & 7)) * 8;
    const int krow = 16 * l + (lane >> 4);
    s.wsr = wsrc + (size_t)krow * ldw + ((lane & 15) ^ (((lane >> 4) & 3) << 2)) * 8;
    s.ldw = ldw;
    return s;
}
DI void proj_issue(const PSrc& s, int c, int l, unsigned lds0) {
    const unsigned st = lds0 + (c & 3) * P_STAGE + l * 4096;
    glds16(s.xs0 + c * 64, st + P_XS);
    glds16(s.xs1 + c * 64, st + P_XS + 1024);
    glds16(s.xs0 + c * 64 + 16 * 512, st + P_XS + 2048);
    glds16(s.xs1 + c * 64 + 16 * 512, st + P_XS + 3072);
#pragma unroll
    for (int i = 0; i < 4; ++i) glds16(s.wsr + (size_t)(c * 64 + 4 * i) * s.ldw, st + P_WS + i * 1024);
}
DI float wave_sum63(float v) {
#define DPP_ADD(ctrl, rm) v += __int_as_float(__builtin_amdgcn_update_dpp(0, __float_as_int(v), ctrl, rm, 0xf, false))
    DPP_ADD(0x111, 0xf); DPP_ADD(0x112, 0xf); DPP_ADD(0x114, 0xf); DPP_ADD(0x118, 0xf);
    DPP_ADD(0x142, 0xa); DPP_ADD(0x143, 0xc);
#undef DPP_ADD
    return v;
}
DI void lds_spin(const char* smem, int off, unsigned target) {
    const volatile LDS_AS unsigned* p = (const volatile LDS_AS unsigned*)(uintptr_t)((unsigned)(uintptr_t)smem + off);
    while ((unsigned)__builtin_amdgcn_readfirstlane(*p) < target) __builtin_amdgcn_s_sleep(1);
    asm volatile("" ::: "memory");
}

template <bool ISV>
DI void proj_compute(const PSrc& src, const unsigned short* __restrict__ x, const unsigned short* __restrict__ wx,
                     const unsigned short* __restrict__ gw, const unsigned short* __restrict__ wo, char* __restrict__ ws,
                     int mt, int nt, int w, unsigned lds0, char* smem) {
    const int lane = threadIdx.x & 63;
    const int wm = w >> 1, wn = w & 1;
    const int r = lane & 31, h = lane >> 5;
    unsigned xo[4][2];
#pragma unroll
    for (int tt = 0; tt < 2; ++tt) {
        const int row = wm * 64 + tt * 32 + r;
#pragma unroll
        for (int ks = 0; ks < 4; ++ks) xo[ks][tt] = lds0 + P_XS + row * 128 + (((2 * ks + h) ^ ((row >> 1) & 7)) << 4);
    }
    const int q4 = (lane & 15) >> 2, p4 = lane & 3, g1 = (lane >> 4) & 1;
    unsigned wa[2];
#pragma unroll
    for (int ct = 0; ct < 2; ++ct)
        wa[ct] = lds0 + P_WS + (8 * h + q4) * 256 + ((4 * ((2 * wn + ct) ^ q4) + 2 * g1 + (p4 >> 1)) << 4) + (p4 & 1) * 8;
    f32x16 acc[2][2];
#pragma unroll
    for (int i = 0; i < 16; ++i) { acc[0][0][i] = 0.f; acc[0][1][i] = 0.f; acc[1][0][i] = 0.f; acc[1][1][i] = 0.f; }
    bf16x8 fx[2][4][2], fw[2][4][2];
    auto mm = [&](int bf, int ks) {
#pragma unroll
        for (int ct = 0; ct < 2; ++ct)
#pragma unroll
            for (int tt = 0; tt < 2; ++tt)
                acc[ct][tt] = ISV ? MFMA32(fx[bf][ks][tt], fw[bf][ks][ct], acc[ct][tt]) : MFMA32(fw[bf][ks][ct], fx[bf][ks][tt], acc[ct][tt]);
    };
    auto rd = [&](int bf, int ks, unsigned so) {
#pragma unroll
        for (int tt = 0; tt < 2; ++tt) fx[bf][ks][tt] = lds_b128(xo[ks][tt] + so);
#pragma unroll
        for (int ct = 0; ct < 2; ++ct) fw[bf][ks][ct] = cat8(lds_tr(wa[ct] + so + ks * 4096), lds_tr(wa[ct] + so + ks * 4096 + 1024));
    };
    auto step = [&](int bf, int c) {
        __builtin_amdgcn_s_waitcnt(0xC07F);
        asm volatile("s_barrier" ::: "memory");
        SBAR();
        const unsigned so = (c & 3) * P_STAGE;
#pragma unroll
        for (int ks = 0; ks < 4; ++ks) { rd(bf, ks, so); mm(bf ^ 1, ks); }
#pragma unroll
        for (int i = 0; i < 12; ++i) {
            __builtin_amdgcn_sched_group_barrier(0x008, 1, 0);
            __builtin_amdgcn_sched_group_barrier(0x002, 1, 0);
            __builtin_amdgcn_sched_group_barrier(0x100, 2, 0);
        }
        __builtin_amdgcn_sched_group_barrier(0x008, 4, 0);
        SBAR();
    };
    asm volatile("" : "+v"(xo[0][0]), "+v"(xo[0][1]), "+v"(xo[1][0]), "+v"(xo[1][1]), "+v"(xo[2][0]), "+v"(xo[2][1]),
                      "+v"(xo[3][0]), "+v"(xo[3][1]), "+v"(wa[0]), "+v"(wa[1]),
                      "+v"(acc[0][0]), "+v"(acc[0][1]), "+v"(acc[1][0]), "+v"(acc[1][1]));
    asm volatile("s_waitcnt vmcnt(0)" ::: "memory");
    if (lane == 0) atomicAdd((unsigned*)(smem + P_CNT), 1u);
    lds_spin(smem, P_CNT, 4u);
    rd(0, 0, 0); rd(0, 1, 0); rd(0, 2, 0); rd(0, 3, 0);
    SBAR();
    {
        if (nt >= 8) {
            const unsigned short* es = wx + (size_t)((lane >> 5) * 64 + 16 * w) * 16 + (lane & 31) * 8;
#pragma unroll
            for (int i = 0; i < 4; ++i) glds16(es + i * 2048, lds0 + P_WX + w * 4096 + i * 1024);
        }
        const unsigned short* px = x + (size_t)(mt * 128 + nt * 8 + 2 * w) * 512 + lane * 8;
        glds16(px, lds0 + P_GT + w * 2048);
        glds16(px + 512, lds0 + P_GT + w * 2048 + 1024);
        glds16(gw + lane * 8, lds0 + P_GT + 8192);
        glds16(wo + (size_t)(4 * w + (lane >> 4)) * 512 + ((lane & 15) ^ (((lane >> 4) & 3) << 2)) * 8, lds0 + P_WO + w * 1024);
        const int hdt = (nt & 3) * 8 + wn * 4 + (lane & 3), tlt = mt * 4 + wm * 2;
        const char* td = (nt < 8) ? (ws + WS_Q + (size_t)((nt >> 2) * 32 + hdt) * 65536 + tlt * 1024) : (ws + WS_KV + (size_t)hdt * 131072 + tlt * 2048 + (ISV ? 1024 : 0));
        if (w == 0) {
            const int b2 = blockIdx.x, sl2 = b2 >> 3, hd2 = (b2 & 7) * 4 + ((sl2 & 15) >> 2);
            if (lane == 4) td = ws + WS_Q + (size_t)(sl2 < 16 ? 32 + hd2 : hd2) * 65536 + (sl2 & 3) * 16384;
            if (lane == 5) td = ws + WS_KV + (size_t)(sl2 < 16 ? hd2 : 32) * 131072;
        }
        unsigned sv;
        asm volatile("s_mov_b32 %0, m0\n\ts_mov_b32 m0, %2\n\ts_nop 0\n\tglobal_load_lds_dword %1, off\n\ts_mov_b32 m0, %0"
                     : "=&s"(sv) : "v"(td), "s"(lds0 + P_DUMP) : "memory");
    }

#pragma unroll 1
    for (int c = 1; c < 7; c += 2) { step(1, c); step(0, c + 1); }
    asm volatile("s_waitcnt vmcnt(0)" ::: "memory");
    step(1, 7);
#pragma unroll
    for (int ct = 0; ct < 2; ++ct)
#pragma unroll
        for (int tt = 0; tt < 2; ++tt)
#pragma unroll
            for (int ks = 0; ks < 4; ++ks)
                acc[ct][tt] = ISV ? MFMA32(fx[1][ks][tt], fw[1][ks][ct], acc[ct][tt]) : MFMA32(fw[1][ks][ct], fx[1][ks][tt], acc[ct][tt]);
#pragma unroll
    for (int ct = 0; ct < 2; ++ct)
#pragma unroll
        for (int tt = 0; tt < 2; ++tt) {
            const int hd0 = (nt & 3) * 8 + wn * 4 + ct * 2;
            const int tile = mt * 4 + wm * 2 + tt;
            if (ISV) {
                char* dst = ws + WS_KV + (size_t)(hd0 + (r >> 4)) * 131072 + tile * 2048 + 1024 + h * 256 + (r & 15) * 16;
                gst128(dst, pack8(acc[ct][tt], 0, 1.f));
                gst128(dst + 512, pack8(acc[ct][tt], 8, 1.f));
            } else {
                const bool isq = nt < 8;
                const float sc = isq ? QSCALE : 1.f;
                const size_t hs = isq ? 65536 : 131072;
                char* dst = isq ? (ws + WS_Q + (size_t)((nt >> 2) * 32 + hd0) * 65536 + tile * 1024 + lane * 16)
                                : (ws + WS_KV + (size_t)hd0 * 131072 + tile * 2048 + lane * 16);
                gst128(dst, pack8(acc[ct][tt], 0, sc));
                gst128(dst + hs, pack8(acc[ct][tt], 8, sc));
            }
        }
}

template <int XM>
DI void proj_loader(const PSrc& src, const unsigned short* __restrict__ x, const unsigned short* __restrict__ wk,
                    const unsigned short* __restrict__ wv, const unsigned short* __restrict__ gw, char* __restrict__ ws,
                    int mt, int nt, int l, unsigned lds0, char* smem) {
    const int lane = threadIdx.x & 63;
    const int r = lane & 31, h = lane >> 5;
    constexpr bool isx = XM != 0, isv = XM == 2;
    const int xtt = nt & 3;
    proj_issue(src, 2, l, lds0);
    const int q4 = (lane & 15) >> 2, p4 = lane & 3;
    const unsigned ea = lds0 + P_WX + l * 4096 + (8 * h + q4) * 32 + p4 * 8;
    const int xrow = xtt * 32 + r;
    const unsigned exa = lds0 + P_XS + xrow * 128 + (((2 * l + h) ^ ((xrow >> 1) & 7)) << 4);
    const int gtok = mt * 128 + nt * 8 + 2 * l;
    u32x4 gx0, gx1, gv;
    bf16x8 xe[8];
    if (isx) { lds_spin(smem, P_CNT, 4u); xe[0] = lds_b128(exa); }
#pragma unroll
    for (int c = 1; c < 8; ++c) {
        if (c < 7) WAIT_BAR(8); else WAIT_BAR(0);
        if (isx) xe[c] = lds_b128(exa + (c & 3) * P_STAGE);
        if (c < 6) proj_issue(src, c + 2, l, lds0);
    }
    gx0 = *(const u32x4*)(smem + P_GT + l * 2048 + lane * 16);
    gx1 = *(const u32x4*)(smem + P_GT + l * 2048 + 1024 + lane * 16);
    gv  = *(const u32x4*)(smem + P_GT + 8192 + lane * 16);
    {
        const int g1 = (lane >> 4) & 1;
        const unsigned woa = lds0 + P_WO + (4 * h + q4) * 256 + ((4 * (l ^ q4) + 2 * g1 + (p4 >> 1)) << 4) + (p4 & 1) * 8;
        const bf16x8 f = cat8(lds_tr(woa), lds_tr(woa + 2048));
        gst128(ws + WS_WO + (size_t)(blockIdx.x * 4 + l) * 1024 + lane * 16, __builtin_bit_cast(u32x4, f));
    }
    {
        float s0 = 0.f, s1 = 0.f;
#pragma unroll
        for (int i = 0; i < 4; ++i) {
            const float wl = __uint_as_float(gv[i] << 16), wh = __uint_as_float(gv[i] & 0xffff0000u);
            s0 += __uint_as_float(gx0[i] << 16) * wl + __uint_as_float(gx0[i] & 0xffff0000u) * wh;
            s1 += __uint_as_float(gx1[i] << 16) * wl + __uint_as_float(gx1[i] & 0xffff0000u) * wh;
        }
        s0 = wave_sum63(s0); s1 = wave_sum63(s1);
        if (lane == 63) {
            f32x2 g = {__builtin_amdgcn_rcpf(1.f + __expf(-s0)), __builtin_amdgcn_rcpf(1.f + __expf(-s1))};
            *(f32x2*)((float*)(ws + WS_G) + gtok) = g;
        }
    }
    f32x16 acce;
#pragma unroll
    for (int i = 0; i < 16; ++i) acce[i] = 0.f;
    if (isx) {
        __builtin_amdgcn_s_setprio(3);
#pragma unroll
        for (int c = 0; c < 8; ++c) {
            const bf16x8 we = cat8(lds_tr(ea + c * 512), lds_tr(ea + c * 512 + 128));
            acce = isv ? MFMA32(xe[c], we, acce) : MFMA32(we, xe[c], acce);
        }
    }
    if (isx) {
        f32x4* red = (f32x4*)smem;
#pragma unroll
        for (int g = 0; g < 4; ++g) {
            f32x4 v = {acce[4 * g], acce[4 * g + 1], acce[4 * g + 2], acce[4 * g + 3]};
            red[(l * 4 + g) * 64 + lane] = v;
        }
        unsigned old = 0;
        asm volatile("s_waitcnt lgkmcnt(0)" ::: "memory");
        if (lane == 0) old = atomicAdd((unsigned*)(smem + P_CNT), 1u);
        old = __builtin_amdgcn_readfirstlane(old);
        if (old == 7) {
            asm volatile("" ::: "memory");
#pragma unroll
            for (int g = 0; g < 4; ++g) {
                const f32x4 v = red[g * 64 + lane] + red[(4 + g) * 64 + lane] + red[(8 + g) * 64 + lane] + red[(12 + g) * 64 + lane];
#pragma unroll
                for (int i = 0; i < 4; ++i) acce[4 * g + i] = v[i];
            }
            char* base = ws + WS_KV + (size_t)32 * 131072 + (mt * 4 + xtt) * 2048;
            if (isv) {
                if (r < 16) {
                    gst128(base + 1024 + h * 256 + r * 16, pack8(acce, 0, 1.f));
                    gst128(base + 1536 + h * 256 + r * 16, pack8(acce, 8, 1.f));
                }
            } else {
                gst128(base + lane * 16, pack8(acce, 0, 1.f));
            }
        }
    }
}

__global__ __launch_bounds__(512, 2) void proj_kernel(const unsigned short* __restrict__ x, const unsigned short* __restrict__ wq,
                                                      const unsigned short* __restrict__ wk, const unsigned short* __restrict__ wv,
                                                      const unsigned short* __restrict__ wqkv, const unsigned short* __restrict__ gw,
                                                      const unsigned short* __restrict__ woA, const unsigned short* __restrict__ woB,
                                                      char* __restrict__ ws) {
    extern __shared__ __attribute__((aligned(16))) char smem[];
    const unsigned lds0 = (unsigned)(uintptr_t)smem;
    const int w = __builtin_amdgcn_readfirstlane(threadIdx.x >> 6);
    const int b = blockIdx.x, xcd = b & 7, slot = b >> 3;
    const int mt = (xcd & 3) * 4 + (slot & 3), nt = (xcd >> 2) * 8 + (slot >> 2);
    if (w == 7) {
        unsigned long long pc; int len;
        asm volatile("s_getpc_b64 %0\n\tpf_text_begin:\n\ts_mov_b32 %1, pf_text_end-pf_text_begin" : "=s"(pc), "=s"(len));
        int off = ((slot & 7) * 32 + (int)(threadIdx.x & 31)) * 128;
        off = off < len ? off : 0;
        unsigned sv;
        asm volatile("s_mov_b32 %0, m0\n\ts_mov_b32 m0, %3\n\ts_nop 0\n\tglobal_load_lds_dword %1, %2\n\ts_mov_b32 m0, %0"
                     : "=&s"(sv) : "v"(off), "s"(pc), "s"(lds0 + P_DUMP) : "memory");
    }
    if (threadIdx.x == 0) *(unsigned*)(smem + P_CNT) = 0u;
    const unsigned short* wsrc = (nt < 4) ? (wq + nt * 128) : (wqkv + (nt - 4) * 128);
    const PSrc src = proj_src(x, wsrc, (nt < 4) ? 512 : 1536, mt, nt, w & 3, threadIdx.x & 63);
    const unsigned short* wo = ((b < 128) ? woA : woB) + (size_t)((b >> 2) & 31) * 16 * 512 + (b & 3) * 128;
    proj_issue(src, w >> 2, w & 3, lds0);
    asm volatile("s_waitcnt lgkmcnt(0)\n\ts_barrier" ::: "memory");
    if (w >= 4) {
        if (nt >= 12)     proj_loader<2>(src, x, wk, wv, gw, ws, mt, nt, w - 4, lds0, smem);
        else if (nt >= 8) proj_loader<1>(src, x, wk, wv, gw, ws, mt, nt, w - 4, lds0, smem);
        else              proj_loader<0>(src, x, wk, wv, gw, ws, mt, nt, w - 4, lds0, smem);
        if (b == 0 && w == 4 && (threadIdx.x & 63) < 4) *(unsigned*)(ws + WS_C + (threadIdx.x & 63) * 256) = 0u;
    } else if (nt >= 12) proj_compute<true>(src, x, wv, gw, wo, ws, mt, nt, w, lds0, smem);
    else                 proj_compute<false>(src, x, wk, gw, wo, ws, mt, nt, w, lds0, smem);
}

constexpr int A_KV = 0, A_CONST = 131072, A_LDS = A_CONST + 8192;

DI float max16(const f32x16& s) {
    float a = fmaxf(fmaxf(s[0], s[1]), s[2]), b = fmaxf(fmaxf(s[3], s[4]), s[5]);
    a = fmaxf(fmaxf(a, s[6]), s[7]);   b = fmaxf(fmaxf(b, s[8]), s[9]);
    a = fmaxf(fmaxf(a, s[10]), s[11]); b = fmaxf(fmaxf(b, s[12]), s[13]);
    a = fmaxf(fmaxf(a, s[14]), s[15]);
    return fmaxf(a, b);
}
DI float half_max(float m) {
    auto rr = __builtin_amdgcn_permlane32_swap(__float_as_uint(m), __float_as_uint(m), false, false);
    return fmaxf(__uint_as_float(rr[0]), __uint_as_float(rr[1]));
}

#define R_SA "192"
#define R_SB "208"
#define EX(X, i) "v_exp_f32 v[" X "+" #i "], v[" X "+" #i "]\n\t"
#define CV(X, j, a, b) "v_cvt_pk_bf16_f32 v[224+" #j "], v[" X "+" #a "], v[" X "+" #b "]\n\t"
#define HALF(X, Y, OX, OY, NY, QY, WA, RA, WB, RB, WC, RC)                                        \
    EX(X, 0) EX(X, 1) WA                                                                          \
    "v_mfma_f32_32x32x16_bf16 " OY ", %[v1], v[228:231], " OY "\n\t" RA                           \
    EX(X, 2) EX(X, 3) CV(X, 0, 0, 1) EX(X, 4) EX(X, 5) CV(X, 1, 2, 3) WB                          \
    "v_mfma_f32_32x32x16_bf16 v[" Y ":" Y "+15], %[kf], " QY ", " NY "\n\t" RB                    \
    EX(X, 6) EX(X, 7) CV(X, 2, 4, 5) EX(X, 8) EX(X, 9) CV(X, 3, 6, 7) EX(X, 10) EX(X, 11) WC      \
    "v_mfma_f32_32x32x16_bf16 " OX ", %[v0], v[224:227], " OX "\n\t" RC                           \
    CV(X, 4, 8, 9) EX(X, 12) EX(X, 13) CV(X, 5, 10, 11) EX(X, 14) EX(X, 15) CV(X, 6, 12, 13) CV(X, 7, 14, 15)
#define TILE(OFF_V1, OFF_KN, OFF_V0N, P0, P1)                                                     \
    HALF(R_SA, R_SB, "%[oa]", "%[ob]", "%[nb]", "%[qb]",                                          \
         "", "ds_read_b128 %[v1], %[va] offset:" OFF_V1 "\n\t" P0,                                \
         "", "ds_read_b128 %[kf], %[ka] offset:" OFF_KN "\n\t",                                   \
         "s_waitcnt lgkmcnt(2)\n\t", "")                                                          \
    HALF(R_SB, R_SA, "%[ob]", "%[oa]", "%[na]", "%[qa]",                                          \
         "s_waitcnt lgkmcnt(1)\n\t", P1,                                                          \
         "s_waitcnt lgkmcnt(0)\n\t", "",                                                          \
         "", "ds_read_b128 %[v0], %[va] offset:" OFF_V0N "\n\t")
#define KVDMA                                                                                     \
    "global_load_lds_dwordx4 %[lo], s[40:41]\n\t"                                                 \
    "s_add_u32 s40, s40, 0x2000\n\ts_addc_u32 s41, s41, 0\n\ts_add_u32 m0, m0, 0x2000\n\ts_nop 0\n\t"
#define WOLD(R)                                                                                   \
    "global_load_dwordx4 v[" #R ":" #R "+3], %[lo], s[42:43]\n\t"                                 \
    "global_load_dwordx4 v[" #R "+4:" #R "+7], %[lo], s[42:43] offset:1024\n\t"                   \
    "s_add_u32 s42, s42, 0x4000\n\ts_addc_u32 s43, s43, 0\n\t"
#define ATTN_ASM(PY, PO)                                                                          \
    asm volatile(                                                                                 \
        "s_mov_b32 s44, m0\n\ts_mov_b32 m0, %[kd]\n\ts_mov_b64 s[40:41], %[kb]\n\ts_mov_b64 s[42:43], %[wb]\n\ts_cmp_lg_u32 %[yg], 0\n\ts_cselect_b64 vcc, -1, 0\n\t"               \
        "ds_read_b128 %[kf], %[ka]\n\t"                                                           \
        "ds_read_b128 %[v1], %[va]\n\t"                                                           \
        "ds_read_b128 %[v0], %[va]\n\t"                                                           \
        "v_mov_b32 v228, 0\n\tv_mov_b32 v229, 0\n\tv_mov_b32 v230, 0\n\tv_mov_b32 v231, 0\n\t"    \
        "s_waitcnt lgkmcnt(0)\n\t"                                                                \
        "v_mfma_f32_32x32x16_bf16 v[192:207], %[kf], %[qa], %[na]\n\t"                            \
        "s_nop 7\n\ts_nop 7\n\t"                                                                  \
        "L_loop_%=:\n\t"                                                                          \
        "s_bitcmp1_b32 %[mk], %[cnt]\n\t"                                                         \
        "s_cbranch_scc1 L_sp_%=\n\t"                                                              \
        "L_go_%=:\n\t"                                                                            \
        TILE("512", "2048", "2048", "s_setprio 0\n\t", PY)                                        \
        TILE("2560", "4096", "4096", PO, "")                                                      \
        TILE("4608", "6144", "6144", "", "")                                                      \
        HALF(R_SA, R_SB, "%[oa]", "%[ob]", "%[nb]", "%[qb]",                                      \
             "", "ds_read_b128 %[v1], %[va] offset:6656\n\t",                                     \
             "", "ds_read_b128 %[kf], %[ka] offset:8192\n\tv_add_u32 %[va], %[va], %[vs]\n\t",    \
             "s_waitcnt lgkmcnt(2)\n\t", "v_add_u32 %[ka], 0x2000, %[ka]\n\t")                    \
        HALF(R_SB, R_SA, "%[ob]", "%[oa]", "%[na]", "%[qa]",                                      \
             "s_waitcnt lgkmcnt(1)\n\t", "s_add_u32 %[cnt], %[cnt], 1\n\t",                       \
             "s_waitcnt lgkmcnt(0)\n\t", "s_and_b32 s45, %[cnt], 15\n\t",                           \
             "", "ds_read_b128 %[v0], %[va]\n\t")                                                 \
        "s_cbranch_scc1 L_loop_%=\n\t"                                                            \
        "s_branch L_end_%=\n\t"                                                                   \
        "L_sp_%=:\n\t"                                                                            \
        "s_cmp_gt_u32 %[cnt], 2\n\t"                                                              \
        "s_cbranch_scc1 L_s3_%=\n\t"                                                              \
        KVDMA KVDMA KVDMA KVDMA                                                                   \
        "s_branch L_go_%=\n\t"                                                                    \
        "L_s3_%=:\n\t"                                                                            \
        "s_cmp_gt_u32 %[cnt], 3\n\t"                                                              \
        "s_cbranch_scc1 L_s4_%=\n\t"                                                              \
        "s_waitcnt vmcnt(0)\n\t"                                                                  \
        "s_barrier\n\t"                                                                           \
        "s_branch L_go_%=\n\t"                                                                    \
        "L_s4_%=:\n\t"                                                                            \
        "s_cmp_gt_u32 %[cnt], 8\n\t"                                                              \
        "s_cbranch_scc1 L_s5_%=\n\t"                                                              \
        "v_mov_b32 v232, -1\n\tv_mov_b32 v233, -1\n\tv_mov_b32 v234, -1\n\tv_mov_b32 v235, -1\n\t" \
        "s_mov_b64 s[40:41], %[ob2]\n\t"                                                          \
        "global_store_dwordx4 %[lo], v[232:235], s[40:41] sc1\n\t"                                \
        "s_add_u32 s40, s40, 0x10000\n\ts_addc_u32 s41, s41, 0\n\t"                               \
        "global_store_dwordx4 %[lo], v[232:235], s[40:41] sc1\n\t"                                \
        WOLD(128) WOLD(136) WOLD(144) WOLD(152) WOLD(160) WOLD(168) WOLD(176) WOLD(184)           \
        "s_branch L_go_%=\n\t"                                                                    \
        "L_s5_%=:\n\t"                                                                            \
        "s_cmp_gt_u32 %[cnt], 11\n\t"                                                             \
        "s_cbranch_scc1 L_s6_%=\n\t"                                                              \
        "s_waitcnt vmcnt(0)\n\t"                                                                  \
        "s_mov_b64 exec, 1\n\t"                                                                   \
        "v_mov_b32 v232, 1\n\t"                                                                   \
        "global_atomic_add %[pf], v232, off sc1\n\t"                                              \
        "s_mov_b64 exec, -1\n\t"                                                                  \
        "global_load_dword v233, %[ot], off\n\t"                                                  \
        "s_branch L_go_%=\n\t"                                                                    \
        "L_s6_%=:\n\t"                                                                            \
        "global_load_dword %[cv], %[pf], off sc1\n\t"                                             \
        "s_branch L_go_%=\n\t"                                                                    \
        "L_end_%=:\n\t"                                                                           \
        "s_nop 1\n\t"                                                                             \
        "v_mfma_f32_32x32x16_bf16 %[ob], %[v1], v[228:231], %[ob]\n\t"                            \
        "s_waitcnt vmcnt(0) lgkmcnt(0)\n\t"                                                       \
        "s_mov_b32 m0, s44\n\t"                                                                \
        "s_nop 7\n\ts_nop 7\n\t"                                                                  \
        : [oa] "+v"(oa), [ob] "+v"(ob), [ka] "+v"(ka), [va] "+v"(va), [cnt] "+s"(cnt), [cv] "+v"(cv), \
          [kf] "=&v"(kf), [v0] "=&v"(v0), [v1] "=&v"(v1),                                         \
          "+{v[128:143]}"(w0), "+{v[144:159]}"(w1), "+{v[160:175]}"(w2), "+{v[176:191]}"(w3)      \
        : [qa] "v"(qa), [qb] "v"(qb), [na] "v"(na), [nb] "v"(nb), [vs] "v"(vstep), [yg] "s"(yg), [pf] "v"(pf), \
          [lo] "v"(lo), [kb] "s"(kb), [kd] "s"(kd), [wb] "s"(wb), [ob2] "s"(ob2), [mk] "s"(mk), [ot] "v"(ot)                                                \
        : "memory", "scc", "vcc", "s40", "s41", "s42", "s43", "s44", "s45",                                                                        \
          "v192", "v193", "v194", "v195", "v196", "v197", "v198", "v199", "v200", "v201", "v202", "v203", "v204", "v205", "v206", "v207", \
          "v208", "v209", "v210", "v211", "v212", "v213", "v214", "v215", "v216", "v217", "v218", "v219", "v220", "v221", "v222", "v223", \
          "v224", "v225", "v226", "v227", "v228", "v229", "v230", "v231", "v232", "v233", "v234", "v235")

DI void attn_main(bool young, bool first, unsigned ka, unsigned va, unsigned vstep, const bf16x8& qa, const bf16x8& qb, const f32x16& na,
                  const f32x16& nb, f32x16& oa, f32x16& ob, const char* pf, unsigned lo, const char* kb, unsigned kd,
                  const char* wb, const char* ob2, u32x16& w0, u32x16& w1, u32x16& w2, u32x16& w3, unsigned& cv, const void* ot) {
    bf16x8 kf, v0, v1;
    int cnt = __builtin_amdgcn_readfirstlane(first ? 0 : 16);
    const int yg = __builtin_amdgcn_readfirstlane(young ? 1 : 0);
    const int mk = __builtin_amdgcn_readfirstlane(young ? 0x422f : 0x448f);
    ATTN_ASM("s_cbranch_vccz L_py_%=\n\ts_setprio 1\n\tL_py_%=:\n\t", "s_setprio 1\n\t");
}

__global__ __launch_bounds__(512, 2) void attn_kernel(char* __restrict__ ws, unsigned short* __restrict__ out) {
    extern __shared__ __attribute__((aligned(16))) char smem[];
    const unsigned lds0 = (unsigned)(uintptr_t)smem;
    const int tid = threadIdx.x, lane = tid & 63;
    const int w = __builtin_amdgcn_readfirstlane(tid >> 6);
    const int r = lane & 31, h = lane >> 5;
    const int b = blockIdx.x, xcd = b & 7, slot = b >> 3;
    const int isB = (slot < 16);
    const int hd = xcd * 4 + ((slot & 15) >> 2);
    const int hh = isB ? 32 + hd : hd;
    const int kvh = isB ? hd : 32;
    const int tile = (slot & 3) * 16 + 2 * w;

    bf16x8 qa, qb;
    {
        const char* qsrc = ws + WS_Q + (size_t)hh * 65536 + tile * 1024 + lane * 16;
        asm volatile("global_load_dwordx4 %0, %2, off\n\tglobal_load_dwordx4 %1, %2, off offset:1024"
                     : "=&v"(qa), "=&v"(qb) : "v"(qsrc) : "memory");
    }
    const char* kvsrc = ws + WS_KV + (size_t)kvh * 131072 + w * 1024 + lane * 16;
#pragma unroll
    for (int i = 0; i < 4; ++i) glds16(kvsrc + i * 8192, lds0 + A_KV + i * 8192 + w * 1024);
    {
        const unsigned one = ((tid & 15) == 0 || (tid & 15) == 4) ? 0x3f803f80u : 0u;
        u32x4 c = {one, one, one, one};
        *(u32x4*)(smem + A_CONST + tid * 16) = c;
    }
    const unsigned kaddr = lds0 + A_KV + lane * 16;
    const unsigned vaddr = (r < 16) ? (lds0 + A_KV + 1024 + h * 256 + r * 16) : (lds0 + A_CONST + (r - 16) * 16);
    const unsigned vstep = (r < 16) ? 8192u : 0u;

    float ga, gb;
    {
        const float* gsrc = (const float*)(ws + WS_G) + tile * 32 + r;
        asm volatile("global_load_dword %0, %2, off\n\tglobal_load_dword %1, %2, off offset:128" : "=&v"(ga), "=&v"(gb) : "v"(gsrc) : "memory");
    }
    const char* kb = ws + WS_KV + (size_t)kvh * 131072 + 32768 + w * 1024;
    const unsigned kd = lds0 + A_KV + 32768 + w * 1024;
    const int qblk = slot & 3;
    const int mt = qblk * 8 + (hh >> 3), nt = hh & 7;
    char* obase = ws + WS_O + (size_t)tile * 65536 + hh * 1024;
    char* odst = obase + lane * 16;
    unsigned short* optr = out + (size_t)(mt * 64 + ((w >> 1) & 1) * 32 + r) * 512 + nt * 64 + (w >> 2) * 32 + (w & 1) * 16 + h * 8;
    const char* cnt = ws + WS_C + qblk * 256;
    unsigned cv = 0u;
    const char* wb = ws + WS_WO + (size_t)(w * 8) * 16384 + (nt * 2) * 1024;
    u32x16 w0, w1, w2, w3;
    asm volatile("" : "={v[128:143]}"(w0), "={v[144:159]}"(w1), "={v[160:175]}"(w2), "={v[176:191]}"(w3));
    f32x16 zero;
#pragma unroll
    for (int i = 0; i < 16; ++i) zero[i] = 0.f;
    f32x16 na, nb, oa = zero, ob = zero;
    asm volatile("" : "+v"(oa), "+v"(ob), "+s"(kb), "+s"(wb), "+s"(obase), "+v"(cnt));
    asm volatile("s_waitcnt vmcnt(2) lgkmcnt(0)\n\ts_barrier" : "+v"(qa), "+v"(qb) :: "memory");
    {
        const bf16x8 k0 = lds_b128(kaddr);
        const f32x16 sa = MFMA32(k0, qa, zero);
        const f32x16 sb = MFMA32(k0, qb, zero);
        SBAR();
        const float ma = -half_max(max16(sa)) - REFOFF, mb = -half_max(max16(sb)) - REFOFF;
#pragma unroll
        for (int i = 0; i < 16; ++i) { na[i] = ma; nb[i] = mb; }
    }
    attn_main(w >= 4, true, kaddr, vaddr, vstep, qa, qb, na, nb, oa, ob, cnt, lane * 16, kb, kd, wb, obase, w0, w1, w2, w3, cv, optr);
    asm volatile("" : "+v"(ga), "+v"(gb));

    {
        float xa = fabsf(oa[0]), xb = fabsf(ob[0]), ta = oa[0], tb = ob[0];
#pragma unroll
        for (int i = 1; i < 8; ++i) { xa = fmaxf(xa, fabsf(oa[i])); xb = fmaxf(xb, fabsf(ob[i])); ta += oa[i]; tb += ob[i]; }
        const bool bad = !(oa[8] < 1.329228e36f) || !(ob[8] < 1.329228e36f) || !(fabsf(ta) <= 3.0e38f) || !(fabsf(tb) <= 3.0e38f) ||
                         !(xa > 7.7e-34f) || !(xb > 7.7e-34f);
        if (__any(bad)) {
            float ma = -INFINITY, mb = -INFINITY;
#pragma unroll 1
            for (int t = 0; t < 64; ++t) {
                const bf16x8 kf = lds_b128(kaddr + t * 2048);
                ma = fmaxf(ma, max16(MFMA32(kf, qa, zero)));
                mb = fmaxf(mb, max16(MFMA32(kf, qb, zero)));
            }
            ma = -half_max(ma); mb = -half_max(mb);
#pragma unroll
            for (int i = 0; i < 16; ++i) { na[i] = ma; nb[i] = mb; }
            oa = zero; ob = zero;
            attn_main(false, false, kaddr, vaddr, vstep, qa, qb, na, nb, oa, ob, cnt, lane * 16, kb, kd, wb, obase, w0, w1, w2, w3, cv, optr);
        }
    }
    const float sa = (isB ? 1.f - ga : ga) * __builtin_amdgcn_rcpf(oa[8]), sb = (isB ? 1.f - gb : gb) * __builtin_amdgcn_rcpf(ob[8]);
    {
        u32x4 pa = pack8(oa, 0, sa), pb = pack8(ob, 0, sb);
#pragma unroll
        for (int i = 0; i < 4; ++i) { pa[i] = min(pa[i], 0xfffffffeu); pb[i] = min(pb[i], 0xfffffffeu); }
        gst128(odst, pa);
        gst128(odst + 65536, pb);
    }
    for (unsigned spins = 0; spins < SPIN_CAP && (unsigned)__builtin_amdgcn_readfirstlane(cv) < 512u; ++spins) {
        if ((unsigned)__builtin_amdgcn_readfirstlane(__hip_atomic_load((const unsigned*)cnt, __ATOMIC_RELAXED, __HIP_MEMORY_SCOPE_AGENT)) >= 512u) break;
        __builtin_amdgcn_s_sleep(2);
    }
    asm volatile("s_barrier" ::: "memory");
    u32x4 wf[8][2];
#define WSUB(W, k) wf[k][0] = __builtin_shufflevector(W, W, 0, 1, 2, 3); wf[k][1] = __builtin_shufflevector(W, W, 4, 5, 6, 7); \
                   wf[k + 1][0] = __builtin_shufflevector(W, W, 8, 9, 10, 11); wf[k + 1][1] = __builtin_shufflevector(W, W, 12, 13, 14, 15);
    WSUB(w0, 0) WSUB(w1, 2) WSUB(w2, 4) WSUB(w3, 6)
    f32x16 acc[4] = {zero, zero, zero, zero};
    {
        const char* osrc = ws + WS_O + (size_t)(mt * 2) * 65536 + (w * 8) * 1024;
        const unsigned lo16 = lane * 16;
        u32x4 of[8][2];
        auto valid = [&](int k) -> bool {
            const unsigned m = max(max(max(of[k][0][0], of[k][0][1]), max(of[k][0][2], of[k][0][3])),
                                   max(max(of[k][1][0], of[k][1][1]), max(of[k][1][2], of[k][1][3])));
            return !__any(m == 0xffffffffu);
        };
        auto mul = [&](int k) {
#pragma unroll
            for (int q = 0; q < 4; ++q)
                acc[q] = MFMA32(__builtin_bit_cast(bf16x8, wf[k][q >> 1]), __builtin_bit_cast(bf16x8, of[k][q & 1]), acc[q]);
        };
#pragma unroll
        for (int k = 0; k < 8; ++k)
            asm volatile("global_load_dwordx4 %0, %2, %3 offset:%5 sc1\n\tglobal_load_dwordx4 %1, %2, %4 offset:%5 sc1"
                         : "=&v"(of[k][0]), "=&v"(of[k][1])
                         : "v"(lo16), "s"(osrc + (k >> 2) * 4096), "s"(osrc + 65536 + (k >> 2) * 4096), "i"((k & 3) * 1024) : "memory");
        auto settle = [&](int k0, int k1) {
            const unsigned need = ((1u << k1) - 1u) & ~((1u << k0) - 1u);
            for (unsigned spins = 0; spins < SPIN_CAP; ++spins) {
                asm volatile("s_waitcnt vmcnt(0)"
                             : "+v"(of[0][0]), "+v"(of[0][1]), "+v"(of[1][0]), "+v"(of[1][1]), "+v"(of[2][0]), "+v"(of[2][1]), "+v"(of[3][0]), "+v"(of[3][1]),
                               "+v"(of[4][0]), "+v"(of[4][1]), "+v"(of[5][0]), "+v"(of[5][1]), "+v"(of[6][0]), "+v"(of[6][1]), "+v"(of[7][0]), "+v"(of[7][1])
                             :: "memory");
                unsigned pend = 0u;
#pragma unroll
                for (int k = k0; k < 8; ++k) if (!valid(k)) pend |= 1u << k;
                if ((pend & need) == 0u) break;
                __builtin_amdgcn_s_sleep(2);
#pragma unroll
                for (int k = k0; k < 8; ++k)
                    if (pend & (1u << k))
                        asm volatile("global_load_dwordx4 %0, %2, %3 offset:%5 sc1\n\tglobal_load_dwordx4 %1, %2, %4 offset:%5 sc1"
                                     : "+v"(of[k][0]), "+v"(of[k][1])
                                     : "v"(lo16), "s"(osrc + (k >> 2) * 4096), "s"(osrc + 65536 + (k >> 2) * 4096), "i"((k & 3) * 1024) : "memory");
            }
        };
        asm volatile("s_waitcnt vmcnt(8)" : "+v"(of[0][0]), "+v"(of[0][1]), "+v"(of[1][0]), "+v"(of[1][1]), "+v"(of[2][0]), "+v"(of[2][1]),
                                            "+v"(of[3][0]), "+v"(of[3][1]) :: "memory");
        if (!(valid(0) && valid(1) && valid(2) && valid(3))) settle(0, 4);
        mul(0); mul(1); mul(2); mul(3);
        SBAR();
        settle(4, 8);
        mul(4); mul(5); mul(6); mul(7);
    }
    float fin[8];
#pragma unroll
    for (int p = 0; p < 8; ++p) {
        if (p == w) {
#pragma unroll
            for (int i = 0; i < 8; ++i) fin[i] = acc[p >> 1][8 * (p & 1) + i];
        } else {
            char* dst = smem + (p * 7 + ((w - p - 1) & 7)) * 2048 + lane * 16;
#pragma unroll
            for (int g = 0; g < 2; ++g) {
                f32x4 v;
#pragma unroll
                for (int i = 0; i < 4; ++i) v[i] = acc[p >> 1][8 * (p & 1) + 4 * g + i];
                *(f32x4*)(dst + g * 1024) = v;
            }
        }
    }
    asm volatile("s_waitcnt lgkmcnt(0)\n\ts_barrier" ::: "memory");
#pragma unroll
    for (int p = 0; p < 7; ++p)
#pragma unroll
        for (int g = 0; g < 2; ++g) {
            const f32x4 v = *(const f32x4*)(smem + (w * 7 + p) * 2048 + g * 1024 + lane * 16);
#pragma unroll
            for (int i = 0; i < 4; ++i) fin[4 * g + i] += v[i];
        }
    const unsigned a0 = pk_bf16(fin[0], fin[1]), a1 = pk_bf16(fin[2], fin[3]);
    const unsigned b0 = pk_bf16(fin[4], fin[5]), b1 = pk_bf16(fin[6], fin[7]);
    auto r0 = __builtin_amdgcn_permlane32_swap(a0, b0, false, false);
    auto r1 = __builtin_amdgcn_permlane32_swap(a1, b1, false, false);
    u32x4 res = {r0[0], r1[0], r0[1], r1[1]};
    gst128(optr, res);
    asm volatile("pf_text_end:");
}

extern "C" void kernel_launch(void* const* d_in, const int* in_sizes, int n_in, void* d_out, int out_size, void* d_ws,
                              size_t ws_size, hipStream_t stream) {
    const unsigned short* x    = (const unsigned short*)d_in[0];
    const unsigned short* wq   = (const unsigned short*)d_in[1];
    const unsigned short* wk   = (const unsigned short*)d_in[2];
    const unsigned short* wv   = (const unsigned short*)d_in[3];
    const unsigned short* woA  = (const unsigned short*)d_in[4];
    const unsigned short* wqkv = (const unsigned short*)d_in[6];
    const unsigned short* woB  = (const unsigned short*)d_in[7];
    const unsigned short* gw   = (const unsigned short*)d_in[8];
    char* ws = (char*)d_ws;
    (void)hipFuncSetAttribute((const void*)proj_kernel, hipFuncAttributeMaxDynamicSharedMemorySize, P_LDS);
    (void)hipFuncSetAttribute((const void*)attn_kernel, hipFuncAttributeMaxDynamicSharedMemorySize, A_LDS);
    proj_kernel<<<256, 512, P_LDS, stream>>>(x, wq, wk, wv, wqkv, gw, woA, woB, ws);
    attn_kernel<<<256, 512, A_LDS, stream>>>(ws, (unsigned short*)d_out);
}
```

```cpp
#include <hip/hip_runtime.h>
#include <hip/hip_bf16.h>
#include <stdint.h>

typedef short    bf16x8 __attribute__((ext_vector_type(8)));
typedef short    s16x4  __attribute__((ext_vector_type(4)));
typedef float    f32x16 __attribute__((ext_vector_type(16)));
typedef float    f32x4  __attribute__((ext_vector_type(4)));
typedef float    f32x2  __attribute__((ext_vector_type(2)));
typedef unsigned u32x4  __attribute__((ext_vector_type(4)));
typedef unsigned u32x16 __attribute__((ext_vector_type(16)));
typedef __bf16   bf16x2v __attribute__((ext_vector_type(2)));

#define DI __device__ __forceinline__
#define MFMA32(a, b, c) __builtin_amdgcn_mfma_f32_32x32x16_bf16((a), (b), (c), 0, 0, 0)
#define LDS_AS __attribute__((address_space(3)))

constexpr size_t WS_Q  = 0;
constexpr size_t WS_KV = WS_Q + (size_t)64 * 65536;
constexpr size_t WS_O  = WS_KV + (size_t)34 * 131072;
constexpr size_t WS_WO = WS_O + (size_t)64 * 65536;
constexpr size_t WS_G  = WS_WO + (size_t)1024 * 1024;
constexpr size_t WS_C  = WS_G + 8192;

#define WTOUCH 6
constexpr float QSCALE = 0.25f * 1.4426950408889634f;
constexpr float REFOFF = 96.f;
constexpr unsigned SPIN_CAP = 1u << 20;

DI unsigned pk_bf16(float lo, float hi) {
    f32x2 v = {lo, hi};
    bf16x2v r = __builtin_convertvector(v, bf16x2v);
    return __builtin_bit_cast(unsigned, r);
}

DI void glds16(const void* g, unsigned lds_base) {
    unsigned sv;
    asm volatile("s_mov_b32 %0, m0\n\ts_mov_b32 m0, %2\n\ts_nop 0\n\tglobal_load_lds_dwordx4 %1, off\n\ts_mov_b32 m0, %0"
                 : "=&s"(sv) : "v"(g), "s"(lds_base) : "memory");
}
#define WAIT_BAR(N) asm volatile("s_waitcnt vmcnt(" #N ") lgkmcnt(0)\n\ts_barrier" ::: "memory")

DI void gst128(void* p, u32x4 v) { asm volatile("global_store_dwordx4 %0, %1, off sc1\n\ts_nop 1" :: "v"(p), "v"(v) : "memory"); }
DI bf16x8 lds_b128(unsigned addr) { return *(const LDS_AS bf16x8*)(uintptr_t)addr; }
DI s16x4 lds_tr(unsigned addr) {
    return __builtin_bit_cast(s16x4, __builtin_amdgcn_ds_read_tr16_b64_v4i16((LDS_AS s16x4*)(uintptr_t)addr));
}
DI bf16x8 cat8(s16x4 a, s16x4 b) { return __builtin_shufflevector(a, b, 0, 1, 2, 3, 4, 5, 6, 7); }

DI u32x4 pack8(const f32x16& a, int base, float sc) {
    u32x4 r;
    r[0] = pk_bf16(a[base + 0] * sc, a[base + 1] * sc);
    r[1] = pk_bf16(a[base + 2] * sc, a[base + 3] * sc);
    r[2] = pk_bf16(a[base + 4] * sc, a[base + 5] * sc);
    r[3] = pk_bf16(a[base + 6] * sc, a[base + 7] * sc);
    return r;
}

constexpr int P_XS = 0, P_WS = 16384, P_STAGE = 32768, P_WX = 4 * P_STAGE, P_GT = P_WX + 16384, P_WO = P_GT + 9216, P_CNT = P_WO + 4096,
              P_DUMP = P_CNT + 16, P_LDS = P_DUMP + 256;
#define SBAR() __builtin_amdgcn_sched_barrier(0)

struct PSrc { const unsigned short *xs0, *xs1, *wsr; int ldw; };
DI PSrc proj_src(const unsigned short* x, const unsigned short* wsrc, int ldw, int mt, int nt, int l, int lane) {
    PSrc s;
    const int row = 32 * l + (lane >> 3);
    s.xs0 = x + (size_t)(mt * 128 + row) * 512 + ((lane & 7) ^ ((row >> 1) & 7)) * 8;
    s.xs1 = x + (size_t)(mt * 128 + row + 8) * 512 + ((lane & 7) ^ (((row + 8) >> 1) & 7)) * 8;
    const int krow = 16 * l + (lane >> 4);
    s.wsr = wsrc + (size_t)krow * ldw + ((lane & 15) ^ (((lane >> 4) & 3) << 2)) * 8;
    s.ldw = ldw;
    return s;
}
DI void proj_issue(const PSrc& s, int c, int l, unsigned lds0) {
    const unsigned st = lds0 + (c & 3) * P_STAGE + l * 4096;
    glds16(s.xs0 + c * 64, st + P_XS);
    glds16(s.xs1 + c * 64, st + P_XS + 1024);
    glds16(s.xs0 + c * 64 + 16 * 512, st + P_XS + 2048);
    glds16(s.xs1 + c * 64 + 16 * 512, st + P_XS + 3072);
#pragma unroll
    for (int i = 0; i < 4; ++i) glds16(s.wsr + (size_t)(c * 64 + 4 * i) * s.ldw, st + P_WS + i * 1024);
}
DI float wave_sum63(float v) {
#define DPP_ADD(ctrl, rm) v += __int_as_float(__builtin_amdgcn_update_dpp(0, __float_as_int(v), ctrl, rm, 0xf, false))
    DPP_ADD(0x111, 0xf); DPP_ADD(0x112, 0xf); DPP_ADD(0x114, 0xf); DPP_ADD(0x118, 0xf);
    DPP_ADD(0x142, 0xa); DPP_ADD(0x143, 0xc);
#undef DPP_ADD
    return v;
}
DI void lds_spin(const char* smem, int off, unsigned target) {
    const volatile LDS_AS unsigned* p = (const volatile LDS_AS unsigned*)(uintptr_t)((unsigned)(uintptr_t)smem + off);
    while ((unsigned)__builtin_amdgcn_readfirstlane(*p) < target) __builtin_amdgcn_s_sleep(1);
    asm volatile("" ::: "memory");
}

template <bool ISV>
DI void proj_compute(const PSrc& src, const unsigned short* __restrict__ x, const unsigned short* __restrict__ wx,
                     const unsigned short* __restrict__ gw, const unsigned short* __restrict__ wo, char* __restrict__ ws,
                     int mt, int nt, int w, unsigned lds0, char* smem) {
    const int lane = threadIdx.x & 63;
    const int wm = w >> 1, wn = w & 1;
    const int r = lane & 31, h = lane >> 5;
    unsigned xo[4][2];
#pragma unroll
    for (int tt = 0; tt < 2; ++tt) {
        const int row = wm * 64 + tt * 32 + r;
#pragma unroll
        for (int ks = 0; ks < 4; ++ks) xo[ks][tt] = lds0 + P_XS + row * 128 + (((2 * ks + h) ^ ((row >> 1) & 7)) << 4);
    }
    const int q4 = (lane & 15) >> 2, p4 = lane & 3, g1 = (lane >> 4) & 1;
    unsigned wa[2];
#pragma unroll
    for (int ct = 0; ct < 2; ++ct)
        wa[ct] = lds0 + P_WS + (8 * h + q4) * 256 + ((4 * ((2 * wn + ct) ^ q4) + 2 * g1 + (p4 >> 1)) << 4) + (p4 & 1) * 8;
    f32x16 acc[2][2];
#pragma unroll
    for (int i = 0; i < 16; ++i) { acc[0][0][i] = 0.f; acc[0][1][i] = 0.f; acc[1][0][i] = 0.f; acc[1][1][i] = 0.f; }
    bf16x8 fx[2][4][2], fw[2][4][2];
    auto mm = [&](int bf, int ks) {
#pragma unroll
        for (int ct = 0; ct < 2; ++ct)
#pragma unroll
            for (int tt = 0; tt < 2; ++tt)
                acc[ct][tt] = ISV ? MFMA32(fx[bf][ks][tt], fw[bf][ks][ct], acc[ct][tt]) : MFMA32(fw[bf][ks][ct], fx[bf][ks][tt], acc[ct][tt]);
    };
    auto rd = [&](int bf, int ks, unsigned so) {
#pragma unroll
        for (int tt = 0; tt < 2; ++tt) fx[bf][ks][tt] = lds_b128(xo[ks][tt] + so);
#pragma unroll
        for (int ct = 0; ct < 2; ++ct) fw[bf][ks][ct] = cat8(lds_tr(wa[ct] + so + ks * 4096), lds_tr(wa[ct] + so + ks * 4096 + 1024));
    };
    auto step = [&](int bf, int c) {
        __builtin_amdgcn_s_waitcnt(0xC07F);
        asm volatile("s_barrier" ::: "memory");
        SBAR();
        const unsigned so = (c & 3) * P_STAGE;
#pragma unroll
        for (int ks = 0; ks < 4; ++ks) { rd(bf, ks, so); mm(bf ^ 1, ks); }
#pragma unroll
        for (int i = 0; i < 12; ++i) {
            __builtin_amdgcn_sched_group_barrier(0x008, 1, 0);
            __builtin_amdgcn_sched_group_barrier(0x002, 1, 0);
            __builtin_amdgcn_sched_group_barrier(0x100, 2, 0);
        }
        __builtin_amdgcn_sched_group_barrier(0x008, 4, 0);
        SBAR();
    };
    asm volatile("" : "+v"(xo[0][0]), "+v"(xo[0][1]), "+v"(xo[1][0]), "+v"(xo[1][1]), "+v"(xo[2][0]), "+v"(xo[2][1]),
                      "+v"(xo[3][0]), "+v"(xo[3][1]), "+v"(wa[0]), "+v"(wa[1]),
                      "+v"(acc[0][0]), "+v"(acc[0][1]), "+v"(acc[1][0]), "+v"(acc[1][1]));
    asm volatile("s_waitcnt vmcnt(0)" ::: "memory");
    if (lane == 0) atomicAdd((unsigned*)(smem + P_CNT), 1u);
    lds_spin(smem, P_CNT, 4u);
    rd(0, 0, 0); rd(0, 1, 0); rd(0, 2, 0); rd(0, 3, 0);
    SBAR();
    {
        if (nt >= 8) {
            const unsigned short* es = wx + (size_t)((lane >> 5) * 64 + 16 * w) * 16 + (lane & 31) * 8;
#pragma unroll
            for (int i = 0; i < 4; ++i) glds16(es + i * 2048, lds0 + P_WX + w * 4096 + i * 1024);
        }
        const unsigned short* px = x + (size_t)(mt * 128 + nt * 8 + 2 * w) * 512 + lane * 8;
        glds16(px, lds0 + P_GT + w * 2048);
        glds16(px + 512, lds0 + P_GT + w * 2048 + 1024);
        glds16(gw + lane * 8, lds0 + P_GT + 8192);
        glds16(wo + (size_t)(4 * w + (lane >> 4)) * 512 + ((lane & 15) ^ (((lane >> 4) & 3) << 2)) * 8, lds0 + P_WO + w * 1024);
        const int hdt = (nt & 3) * 8 + wn * 4 + (lane & 3), tlt = mt * 4 + wm * 2;
        const char* td = (nt < 8) ? (ws + WS_Q + (size_t)((nt >> 2) * 32 + hdt) * 65536 + tlt * 1024) : (ws + WS_KV + (size_t)hdt * 131072 + tlt * 2048 + (ISV ? 1024 : 0));
        unsigned sv;
        asm volatile("s_mov_b32 %0, m0\n\ts_mov_b32 m0, %2\n\ts_nop 0\n\tglobal_load_lds_dword %1, off\n\ts_mov_b32 m0, %0"
                     : "=&s"(sv) : "v"(td), "s"(lds0 + P_DUMP) : "memory");
    }

#pragma unroll 1
    for (int c = 1; c < 7; c += 2) { step(1, c); step(0, c + 1); }
    asm volatile("s_waitcnt vmcnt(0)" ::: "memory");
    step(1, 7);
#pragma unroll
    for (int ct = 0; ct < 2; ++ct)
#pragma unroll
        for (int tt = 0; tt < 2; ++tt)
#pragma unroll
            for (int ks = 0; ks < 4; ++ks)
                acc[ct][tt] = ISV ? MFMA32(fx[1][ks][tt], fw[1][ks][ct], acc[ct][tt]) : MFMA32(fw[1][ks][ct], fx[1][ks][tt], acc[ct][tt]);
#pragma unroll
    for (int ct = 0; ct < 2; ++ct)
#pragma unroll
        for (int tt = 0; tt < 2; ++tt) {
            const int hd0 = (nt & 3) * 8 + wn * 4 + ct * 2;
            const int tile = mt * 4 + wm * 2 + tt;
            if (ISV) {
                char* dst = ws + WS_KV + (size_t)(hd0 + (r >> 4)) * 131072 + tile * 2048 + 1024 + h * 256 + (r & 15) * 16;
                gst128(dst, pack8(acc[ct][tt], 0, 1.f));
                gst128(dst + 512, pack8(acc[ct][tt], 8, 1.f));
            } else {
                const bool isq = nt < 8;
                const float sc = isq ? QSCALE : 1.f;
                const size_t hs = isq ? 65536 : 131072;
                char* dst = isq ? (ws + WS_Q + (size_t)((nt >> 2) * 32 + hd0) * 65536 + tile * 1024 + lane * 16)
                                : (ws + WS_KV + (size_t)hd0 * 131072 + tile * 2048 + lane * 16);
                gst128(dst, pack8(acc[ct][tt], 0, sc));
                gst128(dst + hs, pack8(acc[ct][tt], 8, sc));
            }
        }
}

template <int XM>
DI void proj_loader(const PSrc& src, const unsigned short* __restrict__ x, const unsigned short* __restrict__ wk,
                    const unsigned short* __restrict__ wv, const unsigned short* __restrict__ gw, char* __restrict__ ws,
                    int mt, int nt, int l, unsigned lds0, char* smem) {
    const int lane = threadIdx.x & 63;
    const int r = lane & 31, h = lane >> 5;
    constexpr bool isx = XM != 0, isv = XM == 2;
    const int xtt = nt & 3;
    proj_issue(src, 2, l, lds0);
    const int q4 = (lane & 15) >> 2, p4 = lane & 3;
    const unsigned ea = lds0 + P_WX + l * 4096 + (8 * h + q4) * 32 + p4 * 8;
    const int xrow = xtt * 32 + r;
    const unsigned exa = lds0 + P_XS + xrow * 128 + (((2 * l + h) ^ ((xrow >> 1) & 7)) << 4);
    const int gtok = mt * 128 + nt * 8 + 2 * l;
    u32x4 gx0, gx1, gv;
    bf16x8 xe[8];
    if (isx) { lds_spin(smem, P_CNT, 4u); xe[0] = lds_b128(exa); }
#pragma unroll
    for (int c = 1; c < 8; ++c) {
        if (c < 7) WAIT_BAR(8); else WAIT_BAR(0);
        if (isx) xe[c] = lds_b128(exa + (c & 3) * P_STAGE);
        if (c < 6) proj_issue(src, c + 2, l, lds0);
    }
    gx0 = *(const u32x4*)(smem + P_GT + l * 2048 + lane * 16);
    gx1 = *(const u32x4*)(smem + P_GT + l * 2048 + 1024 + lane * 16);
    gv  = *(const u32x4*)(smem + P_GT + 8192 + lane * 16);
    {
        const int g1 = (lane >> 4) & 1;
        const unsigned woa = lds0 + P_WO + (4 * h + q4) * 256 + ((4 * (l ^ q4) + 2 * g1 + (p4 >> 1)) << 4) + (p4 & 1) * 8;
        const bf16x8 f = cat8(lds_tr(woa), lds_tr(woa + 2048));
        gst128(ws + WS_WO + (size_t)(blockIdx.x * 4 + l) * 1024 + lane * 16, __builtin_bit_cast(u32x4, f));
    }
    {
        float s0 = 0.f, s1 = 0.f;
#pragma unroll
        for (int i = 0; i < 4; ++i) {
            const float wl = __uint_as_float(gv[i] << 16), wh = __uint_as_float(gv[i] & 0xffff0000u);
            s0 += __uint_as_float(gx0[i] << 16) * wl + __uint_as_float(gx0[i] & 0xffff0000u) * wh;
            s1 += __uint_as_float(gx1[i] << 16) * wl + __uint_as_float(gx1[i] & 0xffff0000u) * wh;
        }
        s0 = wave_sum63(s0); s1 = wave_sum63(s1);
        if (lane == 63) {
            f32x2 g = {__builtin_amdgcn_rcpf(1.f + __expf(-s0)), __builtin_amdgcn_rcpf(1.f + __expf(-s1))};
            *(f32x2*)((float*)(ws + WS_G) + gtok) = g;
        }
    }
    f32x16 acce;
#pragma unroll
    for (int i = 0; i < 16; ++i) acce[i] = 0.f;
    if (isx) {
        __builtin_amdgcn_s_setprio(3);
#pragma unroll
        for (int c = 0; c < 8; ++c) {
            const bf16x8 we = cat8(lds_tr(ea + c * 512), lds_tr(ea + c * 512 + 128));
            acce = isv ? MFMA32(xe[c], we, acce) : MFMA32(we, xe[c], acce);
        }
    }
    if (isx) {
        f32x4* red = (f32x4*)smem;
#pragma unroll
        for (int g = 0; g < 4; ++g) {
            f32x4 v = {acce[4 * g], acce[4 * g + 1], acce[4 * g + 2], acce[4 * g + 3]};
            red[(l * 4 + g) * 64 + lane] = v;
        }
        unsigned old = 0;
        asm volatile("s_waitcnt lgkmcnt(0)" ::: "memory");
        if (lane == 0) old = atomicAdd((unsigned*)(smem + P_CNT), 1u);
        old = __builtin_amdgcn_readfirstlane(old);
        if (old == 7) {
            asm volatile("" ::: "memory");
#pragma unroll
            for (int g = 0; g < 4; ++g) {
                const f32x4 v = red[g * 64 + lane] + red[(4 + g) * 64 + lane] + red[(8 + g) * 64 + lane] + red[(12 + g) * 64 + lane];
#pragma unroll
                for (int i = 0; i < 4; ++i) acce[4 * g + i] = v[i];
            }
            char* base = ws + WS_KV + (size_t)32 * 131072 + (mt * 4 + xtt) * 2048;
            if (isv) {
                if (r < 16) {
                    gst128(base + 1024 + h * 256 + r * 16, pack8(acce, 0, 1.f));
                    gst128(base + 1536 + h * 256 + r * 16, pack8(acce, 8, 1.f));
                }
            } else {
                gst128(base + lane * 16, pack8(acce, 0, 1.f));
            }
        }
    }
}

__global__ __launch_bounds__(512, 2) void proj_kernel(const unsigned short* __restrict__ x, const unsigned short* __restrict__ wq,
                                                      const unsigned short* __restrict__ wk, const unsigned short* __restrict__ wv,
                                                      const unsigned short* __restrict__ wqkv, const unsigned short* __restrict__ gw,
                                                      const unsigned short* __restrict__ woA, const unsigned short* __restrict__ woB,
                                                      char* __restrict__ ws) {
    extern __shared__ __attribute__((aligned(16))) char smem[];
    const unsigned lds0 = (unsigned)(uintptr_t)smem;
    const int w = __builtin_amdgcn_readfirstlane(threadIdx.x >> 6);
    const int b = blockIdx.x, xcd = b & 7, slot = b >> 3;
    const int mt = (xcd & 3) * 4 + (slot & 3), nt = (xcd >> 2) * 8 + (slot >> 2);
    if (w == 7) {
        unsigned long long pc; int len;
        asm volatile("s_getpc_b64 %0\n\tpf_text_begin:\n\ts_mov_b32 %1, pf_text_end-pf_text_begin" : "=s"(pc), "=s"(len));
        int off = ((slot & 7) * 32 + (int)(threadIdx.x & 31)) * 128;
        off = off < len ? off : 0;
        unsigned sv;
        asm volatile("s_mov_b32 %0, m0\n\ts_mov_b32 m0, %3\n\ts_nop 0\n\tglobal_load_lds_dword %1, %2\n\ts_mov_b32 m0, %0"
                     : "=&s"(sv) : "v"(off), "s"(pc), "s"(lds0 + P_DUMP) : "memory");
    }
    if (threadIdx.x == 0) *(unsigned*)(smem + P_CNT) = 0u;
    const unsigned short* wsrc = (nt < 4) ? (wq + nt * 128) : (wqkv + (nt - 4) * 128);
    const PSrc src = proj_src(x, wsrc, (nt < 4) ? 512 : 1536, mt, nt, w & 3, threadIdx.x & 63);
    const unsigned short* wo = ((b < 128) ? woA : woB) + (size_t)((b >> 2) & 31) * 16 * 512 + (b & 3) * 128;
    proj_issue(src, w >> 2, w & 3, lds0);
    if (w == WTOUCH) {
        const unsigned short* tp = wsrc + (size_t)((threadIdx.x & 63) * 8) * ((nt < 4) ? 512 : 1536);
        unsigned sv;
        asm volatile("s_mov_b32 %0, m0\n\ts_mov_b32 m0, %2\n\ts_nop 0\n\tglobal_load_lds_dword %1, off\n\ts_mov_b32 m0, %0"
                     : "=&s"(sv) : "v"(tp), "s"(lds0 + P_DUMP) : "memory");
    }
    asm volatile("s_waitcnt lgkmcnt(0)\n\ts_barrier" ::: "memory");
    if (w >= 4) {
        if (nt >= 12)     proj_loader<2>(src, x, wk, wv, gw, ws, mt, nt, w - 4, lds0, smem);
        else if (nt >= 8) proj_loader<1>(src, x, wk, wv, gw, ws, mt, nt, w - 4, lds0, smem);
        else              proj_loader<0>(src, x, wk, wv, gw, ws, mt, nt, w - 4, lds0, smem);
        if (b == 0 && w == 4 && (threadIdx.x & 63) < 4) *(unsigned*)(ws + WS_C + (threadIdx.x & 63) * 256) = 0u;
    } else if (nt >= 12) proj_compute<true>(src, x, wv, gw, wo, ws, mt, nt, w, lds0, smem);
    else                 proj_compute<false>(src, x, wk, gw, wo, ws, mt, nt, w, lds0, smem);
}

constexpr int A_KV = 0, A_CONST = 131072, A_LDS = A_CONST + 8192;

DI float max16(const f32x16& s) {
    float a = fmaxf(fmaxf(s[0], s[1]), s[2]), b = fmaxf(fmaxf(s[3], s[4]), s[5]);
    a = fmaxf(fmaxf(a, s[6]), s[7]);   b = fmaxf(fmaxf(b, s[8]), s[9]);
    a = fmaxf(fmaxf(a, s[10]), s[11]); b = fmaxf(fmaxf(b, s[12]), s[13]);
    a = fmaxf(fmaxf(a, s[14]), s[15]);
    return fmaxf(a, b);
}
DI float half_max(float m) {
    auto rr = __builtin_amdgcn_permlane32_swap(__float_as_uint(m), __float_as_uint(m), false, false);
    return fmaxf(__uint_as_float(rr[0]), __uint_as_float(rr[1]));
}

#define R_SA "192"
#define R_SB "208"
#define EX(X, i) "v_exp_f32 v[" X "+" #i "], v[" X "+" #i "]\n\t"
#define CV(X, j, a, b) "v_cvt_pk_bf16_f32 v[224+" #j "], v[" X "+" #a "], v[" X "+" #b "]\n\t"
#define HALF(X, Y, OX, OY, NY, QY, WA, RA, WB, RB, WC, RC)                                        \
    EX(X, 0) EX(X, 1) WA                                                                          \
    "v_mfma_f32_32x32x16_bf16 " OY ", %[v1], v[228:231], " OY "\n\t" RA                           \
    EX(X, 2) EX(X, 3) CV(X, 0, 0, 1) EX(X, 4) EX(X, 5) CV(X, 1, 2, 3) WB                          \
    "v_mfma_f32_32x32x16_bf16 v[" Y ":" Y "+15], %[kf], " QY ", " NY "\n\t" RB                    \
    EX(X, 6) EX(X, 7) CV(X, 2, 4, 5) EX(X, 8) EX(X, 9) CV(X, 3, 6, 7) EX(X, 10) EX(X, 11) WC      \
    "v_mfma_f32_32x32x16_bf16 " OX ", %[v0], v[224:227], " OX "\n\t" RC                           \
    CV(X, 4, 8, 9) EX(X, 12) EX(X, 13) CV(X, 5, 10, 11) EX(X, 14) EX(X, 15) CV(X, 6, 12, 13) CV(X, 7, 14, 15)
#define TILE(OFF_V1, OFF_KN, OFF_V0N, P0, P1)                                                     \
    HALF(R_SA, R_SB, "%[oa]", "%[ob]", "%[nb]", "%[qb]",                                          \
         "", "ds_read_b128 %[v1], %[va] offset:" OFF_V1 "\n\t" P0,                                \
         "", "ds_read_b128 %[kf], %[ka] offset:" OFF_KN "\n\t",                                   \
         "s_waitcnt lgkmcnt(2)\n\t", "")                                                          \
    HALF(R_SB, R_SA, "%[ob]", "%[oa]", "%[na]", "%[qa]",                                          \
         "s_waitcnt lgkmcnt(1)\n\t", P1,                                                          \
         "s_waitcnt lgkmcnt(0)\n\t", "",                                                          \
         "", "ds_read_b128 %[v0], %[va] offset:" OFF_V0N "\n\t")
#define KVDMA                                                                                     \
    "global_load_lds_dwordx4 %[lo], s[40:41]\n\t"                                                 \
    "s_add_u32 s40, s40, 0x2000\n\ts_addc_u32 s41, s41, 0\n\ts_add_u32 m0, m0, 0x2000\n\ts_nop 0\n\t"
#define WOLD(R)                                                                                   \
    "global_load_dwordx4 v[" #R ":" #R "+3], %[lo], s[42:43]\n\t"                                 \
    "global_load_dwordx4 v[" #R "+4:" #R "+7], %[lo], s[42:43] offset:1024\n\t"                   \
    "s_add_u32 s42, s42, 0x4000\n\ts_addc_u32 s43, s43, 0\n\t"
#define ATTN_ASM(PY, PO)                                                                          \
    asm volatile(                                                                                 \
        "s_mov_b32 s44, m0\n\ts_mov_b32 m0, %[kd]\n\ts_mov_b64 s[40:41], %[kb]\n\ts_mov_b64 s[42:43], %[wb]\n\ts_cmp_lg_u32 %[yg], 0\n\ts_cselect_b64 vcc, -1, 0\n\t"               \
        "ds_read_b128 %[kf], %[ka]\n\t"                                                           \
        "ds_read_b128 %[v1], %[va]\n\t"                                                           \
        "ds_read_b128 %[v0], %[va]\n\t"                                                           \
        "v_mov_b32 v228, 0\n\tv_mov_b32 v229, 0\n\tv_mov_b32 v230, 0\n\tv_mov_b32 v231, 0\n\t"    \
        "s_waitcnt lgkmcnt(0)\n\t"                                                                \
        "v_mfma_f32_32x32x16_bf16 v[192:207], %[kf], %[qa], %[na]\n\t"                            \
        "s_nop 7\n\ts_nop 7\n\t"                                                                  \
        "L_loop_%=:\n\t"                                                                          \
        "s_bitcmp1_b32 %[mk], %[cnt]\n\t"                                                         \
        "s_cbranch_scc1 L_sp_%=\n\t"                                                              \
        "L_go_%=:\n\t"                                                                            \
        TILE("512", "2048", "2048", "s_setprio 0\n\t", PY)                                        \
        TILE("2560", "4096", "4096", PO, "")                                                      \
        TILE("4608", "6144", "6144", "", "")                                                      \
        HALF(R_SA, R_SB, "%[oa]", "%[ob]", "%[nb]", "%[qb]",                                      \
             "", "ds_read_b128 %[v1], %[va] offset:6656\n\t",                                     \
             "", "ds_read_b128 %[kf], %[ka] offset:8192\n\tv_add_u32 %[va], %[va], %[vs]\n\t",    \
             "s_waitcnt lgkmcnt(2)\n\t", "v_add_u32 %[ka], 0x2000, %[ka]\n\t")                    \
        HALF(R_SB, R_SA, "%[ob]", "%[oa]", "%[na]", "%[qa]",                                      \
             "s_waitcnt lgkmcnt(1)\n\t", "s_add_u32 %[cnt], %[cnt], 1\n\t",                       \
             "s_waitcnt lgkmcnt(0)\n\t", "s_and_b32 s45, %[cnt], 15\n\t",                           \
             "", "ds_read_b128 %[v0], %[va]\n\t")                                                 \
        "s_cbranch_scc1 L_loop_%=\n\t"                                                            \
        "s_branch L_end_%=\n\t"                                                                   \
        "L_sp_%=:\n\t"                                                                            \
        "s_cmp_gt_u32 %[cnt], 2\n\t"                                                              \
        "s_cbranch_scc1 L_s3_%=\n\t"                                                              \
        KVDMA KVDMA KVDMA KVDMA                                                                   \
        "s_branch L_go_%=\n\t"                                                                    \
        "L_s3_%=:\n\t"                                                                            \
        "s_cmp_gt_u32 %[cnt], 3\n\t"                                                              \
        "s_cbranch_scc1 L_s4_%=\n\t"                                                              \
        "s_waitcnt vmcnt(0)\n\t"                                                                  \
        "s_barrier\n\t"                                                                           \
        "s_branch L_go_%=\n\t"                                                                    \
        "L_s4_%=:\n\t"                                                                            \
        "s_cmp_gt_u32 %[cnt], 8\n\t"                                                              \
        "s_cbranch_scc1 L_s5_%=\n\t"                                                              \
        "v_mov_b32 v232, -1\n\tv_mov_b32 v233, -1\n\tv_mov_b32 v234, -1\n\tv_mov_b32 v235, -1\n\t" \
        "s_mov_b64 s[40:41], %[ob2]\n\t"                                                          \
        "global_store_dwordx4 %[lo], v[232:235], s[40:41] sc1\n\t"                                \
        "s_add_u32 s40, s40, 0x10000\n\ts_addc_u32 s41, s41, 0\n\t"                               \
        "global_store_dwordx4 %[lo], v[232:235], s[40:41] sc1\n\t"                                \
        WOLD(128) WOLD(136) WOLD(144) WOLD(152) WOLD(160) WOLD(168) WOLD(176) WOLD(184)           \
        "s_branch L_go_%=\n\t"                                                                    \
        "L_s5_%=:\n\t"                                                                            \
        "s_cmp_gt_u32 %[cnt], 11\n\t"                                                             \
        "s_cbranch_scc1 L_s6_%=\n\t"                                                              \
        "s_waitcnt vmcnt(0)\n\t"                                                                  \
        "s_mov_b64 exec, 1\n\t"                                                                   \
        "v_mov_b32 v232, 1\n\t"                                                                   \
        "global_atomic_add %[pf], v232, off sc1\n\t"                                              \
        "s_mov_b64 exec, -1\n\t"                                                                  \
        "global_load_dword v233, %[ot], off\n\t"                                                  \
        "s_branch L_go_%=\n\t"                                                                    \
        "L_s6_%=:\n\t"                                                                            \
        "global_load_dword %[cv], %[pf], off sc1\n\t"                                             \
        "s_branch L_go_%=\n\t"                                                                    \
        "L_end_%=:\n\t"                                                                           \
        "s_nop 1\n\t"                                                                             \
        "v_mfma_f32_32x32x16_bf16 %[ob], %[v1], v[228:231], %[ob]\n\t"                            \
        "s_waitcnt vmcnt(0) lgkmcnt(0)\n\t"                                                       \
        "s_mov_b32 m0, s44\n\t"                                                                \
        "s_nop 7\n\ts_nop 7\n\t"                                                                  \
        : [oa] "+v"(oa), [ob] "+v"(ob), [ka] "+v"(ka), [va] "+v"(va), [cnt] "+s"(cnt), [cv] "+v"(cv), \
          [kf] "=&v"(kf), [v0] "=&v"(v0), [v1] "=&v"(v1),                                         \
          "+{v[128:143]}"(w0), "+{v[144:159]}"(w1), "+{v[160:175]}"(w2), "+{v[176:191]}"(w3)      \
        : [qa] "v"(qa), [qb] "v"(qb), [na] "v"(na), [nb] "v"(nb), [vs] "v"(vstep), [yg] "s"(yg), [pf] "v"(pf), \
          [lo] "v"(lo), [kb] "s"(kb), [kd] "s"(kd), [wb] "s"(wb), [ob2] "s"(ob2), [mk] "s"(mk), [ot] "v"(ot)                                                \
        : "memory", "scc", "vcc", "s40", "s41", "s42", "s43", "s44", "s45",                                                                        \
          "v192", "v193", "v194", "v195", "v196", "v197", "v198", "v199", "v200", "v201", "v202", "v203", "v204", "v205", "v206", "v207", \
          "v208", "v209", "v210", "v211", "v212", "v213", "v214", "v215", "v216", "v217", "v218", "v219", "v220", "v221", "v222", "v223", \
          "v224", "v225", "v226", "v227", "v228", "v229", "v230", "v231", "v232", "v233", "v234", "v235")

DI void attn_main(bool young, bool first, unsigned ka, unsigned va, unsigned vstep, const bf16x8& qa, const bf16x8& qb, const f32x16& na,
                  const f32x16& nb, f32x16& oa, f32x16& ob, const char* pf, unsigned lo, const char* kb, unsigned kd,
                  const char* wb, const char* ob2, u32x16& w0, u32x16& w1, u32x16& w2, u32x16& w3, unsigned& cv, const void* ot) {
    bf16x8 kf, v0, v1;
    int cnt = __builtin_amdgcn_readfirstlane(first ? 0 : 16);
    const int yg = __builtin_amdgcn_readfirstlane(young ? 1 : 0);
    const int mk = __builtin_amdgcn_readfirstlane(young ? 0x422f : 0x448f);
    ATTN_ASM("s_cbranch_vccz L_py_%=\n\ts_setprio 1\n\tL_py_%=:\n\t", "s_setprio 1\n\t");
}

__global__ __launch_bounds__(512, 2) void attn_kernel(char* __restrict__ ws, unsigned short* __restrict__ out) {
    extern __shared__ __attribute__((aligned(16))) char smem[];
    const unsigned lds0 = (unsigned)(uintptr_t)smem;
    const int tid = threadIdx.x, lane = tid & 63;
    const int w = __builtin_amdgcn_readfirstlane(tid >> 6);
    const int r = lane & 31, h = lane >> 5;
    const int b = blockIdx.x, xcd = b & 7, slot = b >> 3;
    const int isB = (slot < 16);
    const int hd = xcd * 4 + ((slot & 15) >> 2);
    const int hh = isB ? 32 + hd : hd;
    const int kvh = isB ? hd : 32;
    const int tile = (slot & 3) * 16 + 2 * w;

    bf16x8 qa, qb;
    {
        const char* qsrc = ws + WS_Q + (size_t)hh * 65536 + tile * 1024 + lane * 16;
        asm volatile("global_load_dwordx4 %0, %2, off\n\tglobal_load_dwordx4 %1, %2, off offset:1024"
                     : "=&v"(qa), "=&v"(qb) : "v"(qsrc) : "memory");
    }
    const char* kvsrc = ws + WS_KV + (size_t)kvh * 131072 + w * 1024 + lane * 16;
#pragma unroll
    for (int i = 0; i < 4; ++i) glds16(kvsrc + i * 8192, lds0 + A_KV + i * 8192 + w * 1024);
    {
        const unsigned one = ((tid & 15) == 0 || (tid & 15) == 4) ? 0x3f803f80u : 0u;
        u32x4 c = {one, one, one, one};
        *(u32x4*)(smem + A_CONST + tid * 16) = c;
    }
    const unsigned kaddr = lds0 + A_KV + lane * 16;
    const unsigned vaddr = (r < 16) ? (lds0 + A_KV + 1024 + h * 256 + r * 16) : (lds0 + A_CONST + (r - 16) * 16);
    const unsigned vstep = (r < 16) ? 8192u : 0u;

    float ga, gb;
    {
        const float* gsrc = (const float*)(ws + WS_G) + tile * 32 + r;
        asm volatile("global_load_dword %0, %2, off\n\tglobal_load_dword %1, %2, off offset:128" : "=&v"(ga), "=&v"(gb) : "v"(gsrc) : "memory");
    }
    const char* kb = ws + WS_KV + (size_t)kvh * 131072 + 32768 + w * 1024;
    const unsigned kd = lds0 + A_KV + 32768 + w * 1024;
    const int qblk = slot & 3;
    const int mt = qblk * 8 + (hh >> 3), nt = hh & 7;
    char* obase = ws + WS_O + (size_t)tile * 65536 + hh * 1024;
    char* odst = obase + lane * 16;
    unsigned short* optr = out + (size_t)(mt * 64 + ((w >> 1) & 1) * 32 + r) * 512 + nt * 64 + (w >> 2) * 32 + (w & 1) * 16 + h * 8;
    const char* cnt = ws + WS_C + qblk * 256;
    unsigned cv = 0u;
    const char* wb = ws + WS_WO + (size_t)(w * 8) * 16384 + (nt * 2) * 1024;
    u32x16 w0, w1, w2, w3;
    asm volatile("" : "={v[128:143]}"(w0), "={v[144:159]}"(w1), "={v[160:175]}"(w2), "={v[176:191]}"(w3));
    f32x16 zero;
#pragma unroll
    for (int i = 0; i < 16; ++i) zero[i] = 0.f;
    f32x16 na, nb, oa = zero, ob = zero;
    asm volatile("" : "+v"(oa), "+v"(ob), "+s"(kb), "+s"(wb), "+s"(obase), "+v"(cnt));
    asm volatile("s_waitcnt vmcnt(2) lgkmcnt(0)\n\ts_barrier" : "+v"(qa), "+v"(qb) :: "memory");
    {
        const bf16x8 k0 = lds_b128(kaddr);
        const f32x16 sa = MFMA32(k0, qa, zero);
        const f32x16 sb = MFMA32(k0, qb, zero);
        SBAR();
        const float ma = -half_max(max16(sa)) - REFOFF, mb = -half_max(max16(sb)) - REFOFF;
#pragma unroll
        for (int i = 0; i < 16; ++i) { na[i] = ma; nb[i] = mb; }
    }
    attn_main(w >= 4, true, kaddr, vaddr, vstep, qa, qb, na, nb, oa, ob, cnt, lane * 16, kb, kd, wb, obase, w0, w1, w2, w3, cv, optr);
    asm volatile("" : "+v"(ga), "+v"(gb));

    {
        float xa = fabsf(oa[0]), xb = fabsf(ob[0]), ta = oa[0], tb = ob[0];
#pragma unroll
        for (int i = 1; i < 8; ++i) { xa = fmaxf(xa, fabsf(oa[i])); xb = fmaxf(xb, fabsf(ob[i])); ta += oa[i]; tb += ob[i]; }
        const bool bad = !(oa[8] < 1.329228e36f) || !(ob[8] < 1.329228e36f) || !(fabsf(ta) <= 3.0e38f) || !(fabsf(tb) <= 3.0e38f) ||
                         !(xa > 7.7e-34f) || !(xb > 7.7e-34f);
        if (__any(bad)) {
            float ma = -INFINITY, mb = -INFINITY;
#pragma unroll 1
            for (int t = 0; t < 64; ++t) {
                const bf16x8 kf = lds_b128(kaddr + t * 2048);
                ma = fmaxf(ma, max16(MFMA32(kf, qa, zero)));
                mb = fmaxf(mb, max16(MFMA32(kf, qb, zero)));
            }
            ma = -half_max(ma); mb = -half_max(mb);
#pragma unroll
            for (int i = 0; i < 16; ++i) { na[i] = ma; nb[i] = mb; }
            oa = zero; ob = zero;
            attn_main(false, false, kaddr, vaddr, vstep, qa, qb, na, nb, oa, ob, cnt, lane * 16, kb, kd, wb, obase, w0, w1, w2, w3, cv, optr);
        }
    }
    const float sa = (isB ? 1.f - ga : ga) * __builtin_amdgcn_rcpf(oa[8]), sb = (isB ? 1.f - gb : gb) * __builtin_amdgcn_rcpf(ob[8]);
    {
        u32x4 pa = pack8(oa, 0, sa), pb = pack8(ob, 0, sb);
#pragma unroll
        for (int i = 0; i < 4; ++i) { pa[i] = min(pa[i], 0xfffffffeu); pb[i] = min(pb[i], 0xfffffffeu); }
        gst128(odst, pa);
        gst128(odst + 65536, pb);
    }
    for (unsigned spins = 0; spins < SPIN_CAP && (unsigned)__builtin_amdgcn_readfirstlane(cv) < 512u; ++spins) {
        if ((unsigned)__builtin_amdgcn_readfirstlane(__hip_atomic_load((const unsigned*)cnt, __ATOMIC_RELAXED, __HIP_MEMORY_SCOPE_AGENT)) >= 512u) break;
        __builtin_amdgcn_s_sleep(2);
    }
    asm volatile("s_barrier" ::: "memory");
    u32x4 wf[8][2];
#define WSUB(W, k) wf[k][0] = __builtin_shufflevector(W, W, 0, 1, 2, 3); wf[k][1] = __builtin_shufflevector(W, W, 4, 5, 6, 7); \
                   wf[k + 1][0] = __builtin_shufflevector(W, W, 8, 9, 10, 11); wf[k + 1][1] = __builtin_shufflevector(W, W, 12, 13, 14, 15);
    WSUB(w0, 0) WSUB(w1, 2) WSUB(w2, 4) WSUB(w3, 6)
    f32x16 acc[4] = {zero, zero, zero, zero};
    {
        const char* osrc = ws + WS_O + (size_t)(mt * 2) * 65536 + (w * 8) * 1024;
        const unsigned lo16 = lane * 16;
        u32x4 of[8][2];
        auto valid = [&](int k) -> bool {
            const unsigned m = max(max(max(of[k][0][0], of[k][0][1]), max(of[k][0][2], of[k][0][3])),
                                   max(max(of[k][1][0], of[k][1][1]), max(of[k][1][2], of[k][1][3])));
            return !__any(m == 0xffffffffu);
        };
        auto mul = [&](int k) {
#pragma unroll
            for (int q = 0; q < 4; ++q)
                acc[q] = MFMA32(__builtin_bit_cast(bf16x8, wf[k][q >> 1]), __builtin_bit_cast(bf16x8, of[k][q & 1]), acc[q]);
        };
#pragma unroll
        for (int k = 0; k < 8; ++k)
            asm volatile("global_load_dwordx4 %0, %2, %3 offset:%5 sc1\n\tglobal_load_dwordx4 %1, %2, %4 offset:%5 sc1"
                         : "=&v"(of[k][0]), "=&v"(of[k][1])
                         : "v"(lo16), "s"(osrc + (k >> 2) * 4096), "s"(osrc + 65536 + (k >> 2) * 4096), "i"((k & 3) * 1024) : "memory");
        auto settle = [&](int k0, int k1) {
            const unsigned need = ((1u << k1) - 1u) & ~((1u << k0) - 1u);
            for (unsigned spins = 0; spins < SPIN_CAP; ++spins) {
                asm volatile("s_waitcnt vmcnt(0)"
                             : "+v"(of[0][0]), "+v"(of[0][1]), "+v"(of[1][0]), "+v"(of[1][1]), "+v"(of[2][0]), "+v"(of[2][1]), "+v"(of[3][0]), "+v"(of[3][1]),
                               "+v"(of[4][0]), "+v"(of[4][1]), "+v"(of[5][0]), "+v"(of[5][1]), "+v"(of[6][0]), "+v"(of[6][1]), "+v"(of[7][0]), "+v"(of[7][1])
                             :: "memory");
                unsigned pend = 0u;
#pragma unroll
                for (int k = k0; k < 8; ++k) if (!valid(k)) pend |= 1u << k;
                if ((pend & need) == 0u) break;
                __builtin_amdgcn_s_sleep(2);
#pragma unroll
                for (int k = k0; k < 8; ++k)
                    if (pend & (1u << k))
                        asm volatile("global_load_dwordx4 %0, %2, %3 offset:%5 sc1\n\tglobal_load_dwordx4 %1, %2, %4 offset:%5 sc1"
                                     : "+v"(of[k][0]), "+v"(of[k][1])
                                     : "v"(lo16), "s"(osrc + (k >> 2) * 4096), "s"(osrc + 65536 + (k >> 2) * 4096), "i"((k & 3) * 1024) : "memory");
            }
        };
        asm volatile("s_waitcnt vmcnt(8)" : "+v"(of[0][0]), "+v"(of[0][1]), "+v"(of[1][0]), "+v"(of[1][1]), "+v"(of[2][0]), "+v"(of[2][1]),
                                            "+v"(of[3][0]), "+v"(of[3][1]) :: "memory");
        if (!(valid(0) && valid(1) && valid(2) && valid(3))) settle(0, 4);
        mul(0); mul(1); mul(2); mul(3);
        SBAR();
        settle(4, 8);
        mul(4); mul(5); mul(6); mul(7);
    }
    float fin[8];
#pragma unroll
    for (int p = 0; p < 8; ++p) {
        if (p == w) {
#pragma unroll
            for (int i = 0; i < 8; ++i) fin[i] = acc[p >> 1][8 * (p & 1) + i];
        } else {
            char* dst = smem + (p * 7 + ((w - p - 1) & 7)) * 2048 + lane * 16;
#pragma unroll
            for (int g = 0; g < 2; ++g) {
                f32x4 v;
#pragma unroll
                for (int i = 0; i < 4; ++i) v[i] = acc[p >> 1][8 * (p & 1) + 4 * g + i];
                *(f32x4*)(dst + g * 1024) = v;
            }
        }
    }
    asm volatile("s_waitcnt lgkmcnt(0)\n\ts_barrier" ::: "memory");
#pragma unroll
    for (int p = 0; p < 7; ++p)
#pragma unroll
        for (int g = 0; g < 2; ++g) {
            const f32x4 v = *(const f32x4*)(smem + (w * 7 + p) * 2048 + g * 1024 + lane * 16);
#pragma unroll
            for (int i = 0; i < 4; ++i) fin[4 * g + i] += v[i];
        }
    const unsigned a0 = pk_bf16(fin[0], fin[1]), a1 = pk_bf16(fin[2], fin[3]);
    const unsigned b0 = pk_bf16(fin[4], fin[5]), b1 = pk_bf16(fin[6], fin[7]);
    auto r0 = __builtin_amdgcn_permlane32_swap(a0, b0, false, false);
    auto r1 = __builtin_amdgcn_permlane32_swap(a1, b1, false, false);
    u32x4 res = {r0[0], r1[0], r0[1], r1[1]};
    gst128(optr, res);
    asm volatile("pf_text_end:");
}

extern "C" void kernel_launch(void* const* d_in, const int* in_sizes, int n_in, void* d_out, int out_size, void* d_ws,
                              size_t ws_size, hipStream_t stream) {
    const unsigned short* x    = (const unsigned short*)d_in[0];
    const unsigned short* wq   = (const unsigned short*)d_in[1];
    const unsigned short* wk   = (const unsigned short*)d_in[2];
    const unsigned short* wv   = (const unsigned short*)d_in[3];
    const unsigned short* woA  = (const unsigned short*)d_in[4];
    const unsigned short* wqkv = (const unsigned short*)d_in[6];
    const unsigned short* woB  = (const unsigned short*)d_in[7];
    const unsigned short* gw   = (const unsigned short*)d_in[8];
    char* ws = (char*)d_ws;
    (void)hipFuncSetAttribute((const void*)proj_kernel, hipFuncAttributeMaxDynamicSharedMemorySize, P_LDS);
    (void)hipFuncSetAttribute((const void*)attn_kernel, hipFuncAttributeMaxDynamicSharedMemorySize, A_LDS);
    proj_kernel<<<256, 512, P_LDS, stream>>>(x, wq, wk, wv, wqkv, gw, woA, woB, ws);
    attn_kernel<<<256, 512, A_LDS, stream>>>(ws, (unsigned short*)d_out);
}
```

```cpp
#include <hip/hip_runtime.h>
#include <hip/hip_bf16.h>
#include <stdint.h>

typedef short    bf16x8 __attribute__((ext_vector_type(8)));
typedef short    s16x4  __attribute__((ext_vector_type(4)));
typedef float    f32x16 __attribute__((ext_vector_type(16)));
typedef float    f32x4  __attribute__((ext_vector_type(4)));
typedef float    f32x2  __attribute__((ext_vector_type(2)));
typedef unsigned u32x4  __attribute__((ext_vector_type(4)));
typedef unsigned u32x16 __attribute__((ext_vector_type(16)));
typedef __bf16   bf16x2v __attribute__((ext_vector_type(2)));

#define DI __device__ __forceinline__
#define MFMA32(a, b, c) __builtin_amdgcn_mfma_f32_32x32x16_bf16((a), (b), (c), 0, 0, 0)
#define LDS_AS __attribute__((address_space(3)))

constexpr size_t WS_Q  = 0;
constexpr size_t WS_KV = WS_Q + (size_t)64 * 65536;
constexpr size_t WS_O  = WS_KV + (size_t)34 * 131072;
constexpr size_t WS_WO = WS_O + (size_t)64 * 65536;
constexpr size_t WS_G  = WS_WO + (size_t)1024 * 1024;
constexpr size_t WS_C  = WS_G + 8192;

constexpr float QSCALE = 0.25f * 1.4426950408889634f;
constexpr float REFOFF = 96.f;
constexpr unsigned SPIN_CAP = 1u << 16;

DI unsigned pk_bf16(float lo, float hi) {
    f32x2 v = {lo, hi};
    bf16x2v r = __builtin_convertvector(v, bf16x2v);
    return __builtin_bit_cast(unsigned, r);
}

DI void glds16(const void* g, unsigned lds_base) {
    unsigned sv;
    asm volatile("s_mov_b32 %0, m0\n\ts_mov_b32 m0, %2\n\ts_nop 0\n\tglobal_load_lds_dwordx4 %1, off\n\ts_mov_b32 m0, %0"
                 : "=&s"(sv) : "v"(g), "s"(lds_base) : "memory");
}
#define WAIT_BAR(N) asm volatile("s_waitcnt vmcnt(" #N ") lgkmcnt(0)\n\ts_barrier" ::: "memory")

DI void gst128(void* p, u32x4 v) { asm volatile("global_store_dwordx4 %0, %1, off sc1\n\ts_nop 1" :: "v"(p), "v"(v) : "memory"); }
DI bf16x8 lds_b128(unsigned addr) { return *(const LDS_AS bf16x8*)(uintptr_t)addr; }
DI s16x4 lds_tr(unsigned addr) {
    return __builtin_bit_cast(s16x4, __builtin_amdgcn_ds_read_tr16_b64_v4i16((LDS_AS s16x4*)(uintptr_t)addr));
}
DI bf16x8 cat8(s16x4 a, s16x4 b) { return __builtin_shufflevector(a, b, 0, 1, 2, 3, 4, 5, 6, 7); }

DI u32x4 pack8(const f32x16& a, int base, float sc) {
    u32x4 r;
    r[0] = pk_bf16(a[base + 0] * sc, a[base + 1] * sc);
    r[1] = pk_bf16(a[base + 2] * sc, a[base + 3] * sc);
    r[2] = pk_bf16(a[base + 4] * sc, a[base + 5] * sc);
    r[3] = pk_bf16(a[base + 6] * sc, a[base + 7] * sc);
    return r;
}

constexpr int P_XS = 0, P_WS = 16384, P_STAGE = 32768, P_WX = 4 * P_STAGE, P_GT = P_WX + 16384, P_WO = P_GT + 9216, P_CNT = P_WO + 4096,
              P_DUMP = P_CNT + 16, P_LDS = P_DUMP + 256;
#define SBAR() __builtin_amdgcn_sched_barrier(0)

struct PSrc { const unsigned short *xs0, *xs1, *wsr; int ldw; };
DI PSrc proj_src(const unsigned short* x, const unsigned short* wsrc, int ldw, int mt, int nt, int l, int lane) {
    PSrc s;
    const int row = 32 * l + (lane >> 3);
    s.xs0 = x + (size_t)(mt * 128 + row) * 512 + ((lane & 7) ^ ((row >> 1) & 7)) * 8;
    s.xs1 = x + (size_t)(mt * 128 + row + 8) * 512 + ((lane & 7) ^ (((row + 8) >> 1) & 7)) * 8;
    const int krow = 16 * l + (lane >> 4);
    s.wsr = wsrc + (size_t)krow * ldw + ((lane & 15) ^ (((lane >> 4) & 3) << 2)) * 8;
    s.ldw = ldw;
    return s;
}
DI void proj_issue(const PSrc& s, int c, int l, unsigned lds0) {
    const unsigned st = lds0 + (c & 3) * P_STAGE + l * 4096;
    glds16(s.xs0 + c * 64, st + P_XS);
    glds16(s.xs1 + c * 64, st + P_XS + 1024);
    glds16(s.xs0 + c * 64 + 16 * 512, st + P_XS + 2048);
    glds16(s.xs1 + c * 64 + 16 * 512, st + P_XS + 3072);
#pragma unroll
    for (int i = 0; i < 4; ++i) glds16(s.wsr + (size_t)(c * 64 + 4 * i) * s.ldw, st + P_WS + i * 1024);
}
DI float wave_sum63(float v) {
#define DPP_ADD(ctrl, rm) v += __int_as_float(__builtin_amdgcn_update_dpp(0, __float_as_int(v), ctrl, rm, 0xf, false))
    DPP_ADD(0x111, 0xf); DPP_ADD(0x112, 0xf); DPP_ADD(0x114, 0xf); DPP_ADD(0x118, 0xf);
    DPP_ADD(0x142, 0xa); DPP_ADD(0x143, 0xc);
#undef DPP_ADD
    return v;
}
DI void lds_spin(const char* smem, int off, unsigned target) {
    const volatile LDS_AS unsigned* p = (const volatile LDS_AS unsigned*)(uintptr_t)((unsigned)(uintptr_t)smem + off);
    while ((unsigned)__builtin_amdgcn_readfirstlane(*p) < target) __builtin_amdgcn_s_sleep(1);
    asm volatile("" ::: "memory");
}

template <bool ISV>
DI void proj_compute(const PSrc& src, const unsigned short* __restrict__ x, const unsigned short* __restrict__ wx,
                     const unsigned short* __restrict__ gw, const unsigned short* __restrict__ wo, char* __restrict__ ws,
                     int mt, int nt, int w, unsigned lds0, char* smem) {
    const int lane = threadIdx.x & 63;
    const int wm = w >> 1, wn = w & 1;
    const int r = lane & 31, h = lane >> 5;
    unsigned xo[4][2];
#pragma unroll
    for (int tt = 0; tt < 2; ++tt) {
        const int row = wm * 64 + tt * 32 + r;
#pragma unroll
        for (int ks = 0; ks < 4; ++ks) xo[ks][tt] = lds0 + P_XS + row * 128 + (((2 * ks + h) ^ ((row >> 1) & 7)) << 4);
    }
    const int q4 = (lane & 15) >> 2, p4 = lane & 3, g1 = (lane >> 4) & 1;
    unsigned wa[2];
#pragma unroll
    for (int ct = 0; ct < 2; ++ct)
        wa[ct] = lds0 + P_WS + (8 * h + q4) * 256 + ((4 * ((2 * wn + ct) ^ q4) + 2 * g1 + (p4 >> 1)) << 4) + (p4 & 1) * 8;
    f32x16 acc[2][2];
#pragma unroll
    for (int i = 0; i < 16; ++i) { acc[0][0][i] = 0.f; acc[0][1][i] = 0.f; acc[1][0][i] = 0.f; acc[1][1][i] = 0.f; }
    bf16x8 fx[2][4][2], fw[2][4][2];
    auto mm = [&](int bf, int ks) {
#pragma unroll
        for (int ct = 0; ct < 2; ++ct)
#pragma unroll
            for (int tt = 0; tt < 2; ++tt)
                acc[ct][tt] = ISV ? MFMA32(fx[bf][ks][tt], fw[bf][ks][ct], acc[ct][tt]) : MFMA32(fw[bf][ks][ct], fx[bf][ks][tt], acc[ct][tt]);
    };
    auto rd = [&](int bf, int ks, unsigned so) {
#pragma unroll
        for (int tt = 0; tt < 2; ++tt) fx[bf][ks][tt] = lds_b128(xo[ks][tt] + so);
#pragma unroll
        for (int ct = 0; ct < 2; ++ct) fw[bf][ks][ct] = cat8(lds_tr(wa[ct] + so + ks * 4096), lds_tr(wa[ct] + so + ks * 4096 + 1024));
    };
    auto step = [&](int bf, int c) {
        __builtin_amdgcn_s_waitcnt(0xC07F);
        asm volatile("s_barrier" ::: "memory");
        SBAR();
        const unsigned so = (c & 3) * P_STAGE;
#pragma unroll
        for (int ks = 0; ks < 4; ++ks) { rd(bf, ks, so); mm(bf ^ 1, ks); }
#pragma unroll
        for (int i = 0; i < 12; ++i) {
            __builtin_amdgcn_sched_group_barrier(0x008, 1, 0);
            __builtin_amdgcn_sched_group_barrier(0x002, 1, 0);
            __builtin_amdgcn_sched_group_barrier(0x100, 2, 0);
        }
        __builtin_amdgcn_sched_group_barrier(0x008, 4, 0);
        SBAR();
    };
    asm volatile("" : "+v"(xo[0][0]), "+v"(xo[0][1]), "+v"(xo[1][0]), "+v"(xo[1][1]), "+v"(xo[2][0]), "+v"(xo[2][1]),
                      "+v"(xo[3][0]), "+v"(xo[3][1]), "+v"(wa[0]), "+v"(wa[1]),
                      "+v"(acc[0][0]), "+v"(acc[0][1]), "+v"(acc[1][0]), "+v"(acc[1][1]));
    asm volatile("s_waitcnt vmcnt(0)" ::: "memory");
    if (lane == 0) atomicAdd((unsigned*)(smem + P_CNT), 1u);
    lds_spin(smem, P_CNT, 4u);
    rd(0, 0, 0); rd(0, 1, 0); rd(0, 2, 0); rd(0, 3, 0);
    SBAR();
    {
        if (nt >= 8) {
            const unsigned short* es = wx + (size_t)((lane >> 5) * 64 + 16 * w) * 16 + (lane & 31) * 8;
#pragma unroll
            for (int i = 0; i < 4; ++i) glds16(es + i * 2048, lds0 + P_WX + w * 4096 + i * 1024);
        }
        const unsigned short* px = x + (size_t)(mt * 128 + nt * 8 + 2 * w) * 512 + lane * 8;
        glds16(px, lds0 + P_GT + w * 2048);
        glds16(px + 512, lds0 + P_GT + w * 2048 + 1024);
        glds16(gw + lane * 8, lds0 + P_GT + 8192);
        glds16(wo + (size_t)(4 * w + (lane >> 4)) * 512 + ((lane & 15) ^ (((lane >> 4) & 3) << 2)) * 8, lds0 + P_WO + w * 1024);
        const int hdt = (nt & 3) * 8 + wn * 4 + (lane & 3), tlt = mt * 4 + wm * 2;
        const char* td = (nt < 8) ? (ws + WS_Q + (size_t)((nt >> 2) * 32 + hdt) * 65536 + tlt * 1024) : (ws + WS_KV + (size_t)hdt * 131072 + tlt * 2048 + (ISV ? 1024 : 0));
        unsigned sv;
        asm volatile("s_mov_b32 %0, m0\n\ts_mov_b32 m0, %2\n\ts_nop 0\n\tglobal_load_lds_dword %1, off\n\ts_mov_b32 m0, %0"
                     : "=&s"(sv) : "v"(td), "s"(lds0 + P_DUMP) : "memory");
    }

#pragma unroll 1
    for (int c = 1; c < 7; c += 2) { step(1, c); step(0, c + 1); }
    asm volatile("s_waitcnt vmcnt(0)" ::: "memory");
    step(1, 7);
#pragma unroll
    for (int ct = 0; ct < 2; ++ct)
#pragma unroll
        for (int tt = 0; tt < 2; ++tt)
#pragma unroll
            for (int ks = 0; ks < 4; ++ks)
                acc[ct][tt] = ISV ? MFMA32(fx[1][ks][tt], fw[1][ks][ct], acc[ct][tt]) : MFMA32(fw[1][ks][ct], fx[1][ks][tt], acc[ct][tt]);
#pragma unroll
    for (int ct = 0; ct < 2; ++ct)
#pragma unroll
        for (int tt = 0; tt < 2; ++tt) {
            const int hd0 = (nt & 3) * 8 + wn * 4 + ct * 2;
            const int tile = mt * 4 + wm * 2 + tt;
            if (ISV) {
                char* dst = ws + WS_KV + (size_t)(hd0 + (r >> 4)) * 131072 + tile * 2048 + 1024 + h * 256 + (r & 15) * 16;
                gst128(dst, pack8(acc[ct][tt], 0, 1.f));
                gst128(dst + 512, pack8(acc[ct][tt], 8, 1.f));
            } else {
                const bool isq = nt < 8;
                const float sc = isq ? QSCALE : 1.f;
                const size_t hs = isq ? 65536 : 131072;
                char* dst = isq ? (ws + WS_Q + (size_t)((nt >> 2) * 32 + hd0) * 65536 + tile * 1024 + lane * 16)
                                : (ws + WS_KV + (size_t)hd0 * 131072 + tile * 2048 + lane * 16);
                gst128(dst, pack8(acc[ct][tt], 0, sc));
                gst128(dst + hs, pack8(acc[ct][tt], 8, sc));
            }
        }
}

template <int XM>
DI void proj_loader(const PSrc& src, const unsigned short* __restrict__ x, const unsigned short* __restrict__ wk,
                    const unsigned short* __restrict__ wv, const unsigned short* __restrict__ gw, char* __restrict__ ws,
                    int mt, int nt, int l, unsigned lds0, char* smem) {
    const int lane = threadIdx.x & 63;
    const int r = lane & 31, h = lane >> 5;
    constexpr bool isx = XM != 0, isv = XM == 2;
    const int xtt = nt & 3;
    proj_issue(src, 2, l, lds0);
    const int q4 = (lane & 15) >> 2, p4 = lane & 3;
    const unsigned ea = lds0 + P_WX + l * 4096 + (8 * h + q4) * 32 + p4 * 8;
    const int xrow = xtt * 32 + r;
    const unsigned exa = lds0 + P_XS + xrow * 128 + (((2 * l + h) ^ ((xrow >> 1) & 7)) << 4);
    const int gtok = mt * 128 + nt * 8 + 2 * l;
    u32x4 gx0, gx1, gv;
    bf16x8 xe[8];
    if (isx) { lds_spin(smem, P_CNT, 4u); xe[0] = lds_b128(exa); }
#pragma unroll
    for (int c = 1; c < 8; ++c) {
        if (c < 7) WAIT_BAR(8); else WAIT_BAR(0);
        if (isx) xe[c] = lds_b128(exa + (c & 3) * P_STAGE);
        if (c < 6) proj_issue(src, c + 2, l, lds0);
    }
    gx0 = *(const u32x4*)(smem + P_GT + l * 2048 + lane * 16);
    gx1 = *(const u32x4*)(smem + P_GT + l * 2048 + 1024 + lane * 16);
    gv  = *(const u32x4*)(smem + P_GT + 8192 + lane * 16);
    {
        const int g1 = (lane >> 4) & 1;
        const unsigned woa = lds0 + P_WO + (4 * h + q4) * 256 + ((4 * (l ^ q4) + 2 * g1 + (p4 >> 1)) << 4) + (p4 & 1) * 8;
        const bf16x8 f = cat8(lds_tr(woa), lds_tr(woa + 2048));
        gst128(ws + WS_WO + (size_t)(blockIdx.x * 4 + l) * 1024 + lane * 16, __builtin_bit_cast(u32x4, f));
    }
    {
        float s0 = 0.f, s1 = 0.f;
#pragma unroll
        for (int i = 0; i < 4; ++i) {
            const float wl = __uint_as_float(gv[i] << 16), wh = __uint_as_float(gv[i] & 0xffff0000u);
            s0 += __uint_as_float(gx0[i] << 16) * wl + __uint_as_float(gx0[i] & 0xffff0000u) * wh;
            s1 += __uint_as_float(gx1[i] << 16) * wl + __uint_as_float(gx1[i] & 0xffff0000u) * wh;
        }
        s0 = wave_sum63(s0); s1 = wave_sum63(s1);
        if (lane == 63) {
            f32x2 g = {__builtin_amdgcn_rcpf(1.f + __expf(-s0)), __builtin_amdgcn_rcpf(1.f + __expf(-s1))};
            *(f32x2*)((float*)(ws + WS_G) + gtok) = g;
        }
    }
    f32x16 acce;
#pragma unroll
    for (int i = 0; i < 16; ++i) acce[i] = 0.f;
    if (isx) {
        __builtin_amdgcn_s_setprio(3);
#pragma unroll
        for (int c = 0; c < 8; ++c) {
            const bf16x8 we = cat8(lds_tr(ea + c * 512), lds_tr(ea + c * 512 + 128));
            acce = isv ? MFMA32(xe[c], we, acce) : MFMA32(we, xe[c], acce);
        }
    }
    if (isx) {
        f32x4* red = (f32x4*)smem;
#pragma unroll
        for (int g = 0; g < 4; ++g) {
            f32x4 v = {acce[4 * g], acce[4 * g + 1], acce[4 * g + 2], acce[4 * g + 3]};
            red[(l * 4 + g) * 64 + lane] = v;
        }
        unsigned old = 0;
        asm volatile("s_waitcnt lgkmcnt(0)" ::: "memory");
        if (lane == 0) old = atomicAdd((unsigned*)(smem + P_CNT), 1u);
        old = __builtin_amdgcn_readfirstlane(old);
        if (old == 7) {
            asm volatile("" ::: "memory");
#pragma unroll
            for (int g = 0; g < 4; ++g) {
                const f32x4 v = red[g * 64 + lane] + red[(4 + g) * 64 + lane] + red[(8 + g) * 64 + lane] + red[(12 + g) * 64 + lane];
#pragma unroll
                for (int i = 0; i < 4; ++i) acce[4 * g + i] = v[i];
            }
            char* base = ws + WS_KV + (size_t)32 * 131072 + (mt * 4 + xtt) * 2048;
            if (isv) {
                if (r < 16) {
                    gst128(base + 1024 + h * 256 + r * 16, pack8(acce, 0, 1.f));
                    gst128(base + 1536 + h * 256 + r * 16, pack8(acce, 8, 1.f));
                }
            } else {
                gst128(base + lane * 16, pack8(acce, 0, 1.f));
            }
        }
    }
}

__global__ __launch_bounds__(512, 2) void proj_kernel(const unsigned short* __restrict__ x, const unsigned short* __restrict__ wq,
                                                      const unsigned short* __restrict__ wk, const unsigned short* __restrict__ wv,
                                                      const unsigned short* __restrict__ wqkv, const unsigned short* __restrict__ gw,
                                                      const unsigned short* __restrict__ woA, const unsigned short* __restrict__ woB,
                                                      char* __restrict__ ws) {
    extern __shared__ __attribute__((aligned(16))) char smem[];
    const unsigned lds0 = (unsigned)(uintptr_t)smem;
    const int w = __builtin_amdgcn_readfirstlane(threadIdx.x >> 6);
    const int b = blockIdx.x, xcd = b & 7, slot = b >> 3;
    const int mt = (xcd & 3) * 4 + (slot & 3), nt = (xcd >> 2) * 8 + (slot >> 2);
    if (w == 7) {
        unsigned long long pc; int len;
        asm volatile("s_getpc_b64 %0\n\tpf_text_begin:\n\ts_mov_b32 %1, pf_text_end-pf_text_begin" : "=s"(pc), "=s"(len));
        int off = ((slot & 7) * 32 + (int)(threadIdx.x & 31)) * 128;
        off = off < len ? off : 0;
        unsigned sv;
        asm volatile("s_mov_b32 %0, m0\n\ts_mov_b32 m0, %3\n\ts_nop 0\n\tglobal_load_lds_dword %1, %2\n\ts_mov_b32 m0, %0"
                     : "=&s"(sv) : "v"(off), "s"(pc), "s"(lds0 + P_DUMP) : "memory");
    }
    if (threadIdx.x == 0) *(unsigned*)(smem + P_CNT) = 0u;
    const unsigned short* wsrc = (nt < 4) ? (wq + nt * 128) : (wqkv + (nt - 4) * 128);
    const PSrc src = proj_src(x, wsrc, (nt < 4) ? 512 : 1536, mt, nt, w & 3, threadIdx.x & 63);
    const unsigned short* wo = ((b < 128) ? woA : woB) + (size_t)((b >> 2) & 31) * 16 * 512 + (b & 3) * 128;
    proj_issue(src, w >> 2, w & 3, lds0);
    asm volatile("s_waitcnt lgkmcnt(0)\n\ts_barrier" ::: "memory");
    if (w >= 4) {
        if (nt >= 12)     proj_loader<2>(src, x, wk, wv, gw, ws, mt, nt, w - 4, lds0, smem);
        else if (nt >= 8) proj_loader<1>(src, x, wk, wv, gw, ws, mt, nt, w - 4, lds0, smem);
        else              proj_loader<0>(src, x, wk, wv, gw, ws, mt, nt, w - 4, lds0, smem);
        if (b == 0 && w == 4 && (threadIdx.x & 63) < 4) *(unsigned*)(ws + WS_C + (threadIdx.x & 63) * 256) = 0u;
    } else if (nt >= 12) proj_compute<true>(src, x, wv, gw, wo, ws, mt, nt, w, lds0, smem);
    else                 proj_compute<false>(src, x, wk, gw, wo, ws, mt, nt, w, lds0, smem);
}

constexpr int A_KV = 0, A_CONST = 131072, A_LDS = A_CONST + 8192;

DI float max16(const f32x16& s) {
    float a = fmaxf(fmaxf(s[0], s[1]), s[2]), b = fmaxf(fmaxf(s[3], s[4]), s[5]);
    a = fmaxf(fmaxf(a, s[6]), s[7]);   b = fmaxf(fmaxf(b, s[8]), s[9]);
    a = fmaxf(fmaxf(a, s[10]), s[11]); b = fmaxf(fmaxf(b, s[12]), s[13]);
    a = fmaxf(fmaxf(a, s[14]), s[15]);
    return fmaxf(a, b);
}
DI float half_max(float m) {
    auto rr = __builtin_amdgcn_permlane32_swap(__float_as_uint(m), __float_as_uint(m), false, false);
    return fmaxf(__uint_as_float(rr[0]), __uint_as_float(rr[1]));
}

#define R_SA "192"
#define R_SB "208"
#define EX(X, i) "v_exp_f32 v[" X "+" #i "], v[" X "+" #i "]\n\t"
#define CV(X, j, a, b) "v_cvt_pk_bf16_f32 v[224+" #j "], v[" X "+" #a "], v[" X "+" #b "]\n\t"
#define HALF(X, Y, OX, OY, NY, QY, WA, RA, WB, RB, WC, RC)                                        \
    EX(X, 0) EX(X, 1) WA                                                                          \
    "v_mfma_f32_32x32x16_bf16 " OY ", %[v1], v[228:231], " OY "\n\t" RA                           \
    EX(X, 2) EX(X, 3) CV(X, 0, 0, 1) EX(X, 4) EX(X, 5) CV(X, 1, 2, 3) WB                          \
    "v_mfma_f32_32x32x16_bf16 v[" Y ":" Y "+15], %[kf], " QY ", " NY "\n\t" RB                    \
    EX(X, 6) EX(X, 7) CV(X, 2, 4, 5) EX(X, 8) EX(X, 9) CV(X, 3, 6, 7) EX(X, 10) EX(X, 11) WC      \
    "v_mfma_f32_32x32x16_bf16 " OX ", %[v0], v[224:227], " OX "\n\t" RC                           \
    CV(X, 4, 8, 9) EX(X, 12) EX(X, 13) CV(X, 5, 10, 11) EX(X, 14) EX(X, 15) CV(X, 6, 12, 13) CV(X, 7, 14, 15)
#define TILE(OFF_V1, OFF_KN, OFF_V0N, P0, P1)                                                     \
    HALF(R_SA, R_SB, "%[oa]", "%[ob]", "%[nb]", "%[qb]",                                          \
         "", "ds_read_b128 %[v1], %[va] offset:" OFF_V1 "\n\t" P0,                                \
         "", "ds_read_b128 %[kf], %[ka] offset:" OFF_KN "\n\t",                                   \
         "s_waitcnt lgkmcnt(2)\n\t", "")                                                          \
    HALF(R_SB, R_SA, "%[ob]", "%[oa]", "%[na]", "%[qa]",                                          \
         "s_waitcnt lgkmcnt(1)\n\t", P1,                                                          \
         "s_waitcnt lgkmcnt(0)\n\t", "",                                                          \
         "", "ds_read_b128 %[v0], %[va] offset:" OFF_V0N "\n\t")
#define KVDMA                                                                                     \
    "global_load_lds_dwordx4 %[lo], s[40:41]\n\t"                                                 \
    "s_add_u32 s40, s40, 0x2000\n\ts_addc_u32 s41, s41, 0\n\ts_add_u32 m0, m0, 0x2000\n\ts_nop 0\n\t"
#define WOLD(R)                                                                                   \
    "global_load_dwordx4 v[" #R ":" #R "+3], %[lo], s[42:43]\n\t"                                 \
    "global_load_dwordx4 v[" #R "+4:" #R "+7], %[lo], s[42:43] offset:1024\n\t"                   \
    "s_add_u32 s42, s42, 0x4000\n\ts_addc_u32 s43, s43, 0\n\t"
#define ATTN_ASM(PY, PO)                                                                          \
    asm volatile(                                                                                 \
        "s_mov_b32 s44, m0\n\ts_mov_b32 m0, %[kd]\n\ts_mov_b64 s[40:41], %[kb]\n\ts_mov_b64 s[42:43], %[wb]\n\ts_cmp_lg_u32 %[yg], 0\n\ts_cselect_b64 vcc, -1, 0\n\t"               \
        "ds_read_b128 %[kf], %[ka]\n\t"                                                           \
        "ds_read_b128 %[v1], %[va]\n\t"                                                           \
        "ds_read_b128 %[v0], %[va]\n\t"                                                           \
        "v_mov_b32 v228, 0\n\tv_mov_b32 v229, 0\n\tv_mov_b32 v230, 0\n\tv_mov_b32 v231, 0\n\t"    \
        "s_waitcnt lgkmcnt(0)\n\t"                                                                \
        "v_mfma_f32_32x32x16_bf16 v[192:207], %[kf], %[qa], %[na]\n\t"                            \
        "s_nop 7\n\ts_nop 7\n\t"                                                                  \
        "L_loop_%=:\n\t"                                                                          \
        "s_bitcmp1_b32 %[mk], %[cnt]\n\t"                                                         \
        "s_cbranch_scc1 L_sp_%=\n\t"                                                              \
        "L_go_%=:\n\t"                                                                            \
        TILE("512", "2048", "2048", "s_setprio 0\n\t", PY)                                        \
        TILE("2560", "4096", "4096", PO, "")                                                      \
        TILE("4608", "6144", "6144", "", "")                                                      \
        HALF(R_SA, R_SB, "%[oa]", "%[ob]", "%[nb]", "%[qb]",                                      \
             "", "ds_read_b128 %[v1], %[va] offset:6656\n\t",                                     \
             "", "ds_read_b128 %[kf], %[ka] offset:8192\n\tv_add_u32 %[va], %[va], %[vs]\n\t",    \
             "s_waitcnt lgkmcnt(2)\n\t", "v_add_u32 %[ka], 0x2000, %[ka]\n\t")                    \
        HALF(R_SB, R_SA, "%[ob]", "%[oa]", "%[na]", "%[qa]",                                      \
             "s_waitcnt lgkmcnt(1)\n\t", "s_add_u32 %[cnt], %[cnt], 1\n\t",                       \
             "s_waitcnt lgkmcnt(0)\n\t", "s_and_b32 s45, %[cnt], 15\n\t",                           \
             "", "ds_read_b128 %[v0], %[va]\n\t")                                                 \
        "s_cbranch_scc1 L_loop_%=\n\t"                                                            \
        "s_branch L_end_%=\n\t"                                                                   \
        "L_sp_%=:\n\t"                                                                            \
        "s_cmp_gt_u32 %[cnt], 2\n\t"                                                              \
        "s_cbranch_scc1 L_s3_%=\n\t"                                                              \
        KVDMA KVDMA KVDMA KVDMA                                                                   \
        "s_branch L_go_%=\n\t"                                                                    \
        "L_s3_%=:\n\t"                                                                            \
        "s_cmp_gt_u32 %[cnt], 3\n\t"                                                              \
        "s_cbranch_scc1 L_s4_%=\n\t"                                                              \
        "s_waitcnt vmcnt(0)\n\t"                                                                  \
        "s_barrier\n\t"                                                                           \
        "s_branch L_go_%=\n\t"                                                                    \
        "L_s4_%=:\n\t"                                                                            \
        "s_cmp_gt_u32 %[cnt], 8\n\t"                                                              \
        "s_cbranch_scc1 L_s5_%=\n\t"                                                              \
        "v_mov_b32 v232, -1\n\tv_mov_b32 v233, -1\n\tv_mov_b32 v234, -1\n\tv_mov_b32 v235, -1\n\t" \
        "s_mov_b64 s[40:41], %[ob2]\n\t"                                                          \
        "global_store_dwordx4 %[lo], v[232:235], s[40:41] sc1\n\t"                                \
        "s_add_u32 s40, s40, 0x10000\n\ts_addc_u32 s41, s41, 0\n\t"                               \
        "global_store_dwordx4 %[lo], v[232:235], s[40:41] sc1\n\t"                                \
        WOLD(128) WOLD(136) WOLD(144) WOLD(152) WOLD(160) WOLD(168) WOLD(176) WOLD(184)           \
        "s_branch L_go_%=\n\t"                                                                    \
        "L_s5_%=:\n\t"                                                                            \
        "s_cmp_gt_u32 %[cnt], 11\n\t"                                                             \
        "s_cbranch_scc1 L_s6_%=\n\t"                                                              \
        "s_waitcnt vmcnt(0)\n\t"                                                                  \
        "s_mov_b64 exec, 1\n\t"                                                                   \
        "v_mov_b32 v232, 1\n\t"                                                                   \
        "global_atomic_add %[pf], v232, off sc1\n\t"                                              \
        "s_mov_b64 exec, -1\n\t"                                                                  \
        "global_load_dword v233, %[ot], off\n\t"                                                  \
        "s_branch L_go_%=\n\t"                                                                    \
        "L_s6_%=:\n\t"                                                                            \
        "global_load_dword %[cv], %[pf], off sc1\n\t"                                             \
        "s_branch L_go_%=\n\t"                                                                    \
        "L_end_%=:\n\t"                                                                           \
        "s_nop 1\n\t"                                                                             \
        "v_mfma_f32_32x32x16_bf16 %[ob], %[v1], v[228:231], %[ob]\n\t"                            \
        "s_waitcnt vmcnt(0) lgkmcnt(0)\n\t"                                                       \
        "s_mov_b32 m0, s44\n\t"                                                                \
        "s_nop 7\n\ts_nop 7\n\t"                                                                  \
        : [oa] "+v"(oa), [ob] "+v"(ob), [ka] "+v"(ka), [va] "+v"(va), [cnt] "+s"(cnt), [cv] "+v"(cv), \
          [kf] "=&v"(kf), [v0] "=&v"(v0), [v1] "=&v"(v1),                                         \
          "+{v[128:143]}"(w0), "+{v[144:159]}"(w1), "+{v[160:175]}"(w2), "+{v[176:191]}"(w3)      \
        : [qa] "v"(qa), [qb] "v"(qb), [na] "v"(na), [nb] "v"(nb), [vs] "v"(vstep), [yg] "s"(yg), [pf] "v"(pf), \
          [lo] "v"(lo), [kb] "s"(kb), [kd] "s"(kd), [wb] "s"(wb), [ob2] "s"(ob2), [mk] "s"(mk), [ot] "v"(ot)                                                \
        : "memory", "scc", "vcc", "s40", "s41", "s42", "s43", "s44", "s45",                                                                        \
          "v192", "v193", "v194", "v195", "v196", "v197", "v198", "v199", "v200", "v201", "v202", "v203", "v204", "v205", "v206", "v207", \
          "v208", "v209", "v210", "v211", "v212", "v213", "v214", "v215", "v216", "v217", "v218", "v219", "v220", "v221", "v222", "v223", \
          "v224", "v225", "v226", "v227", "v228", "v229", "v230", "v231", "v232", "v233", "v234", "v235")

DI void attn_main(bool young, bool first, unsigned ka, unsigned va, unsigned vstep, const bf16x8& qa, const bf16x8& qb, const f32x16& na,
                  const f32x16& nb, f32x16& oa, f32x16& ob, const char* pf, unsigned lo, const char* kb, unsigned kd,
                  const char* wb, const char* ob2, u32x16& w0, u32x16& w1, u32x16& w2, u32x16& w3, unsigned& cv, const void* ot) {
    bf16x8 kf, v0, v1;
    int cnt = __builtin_amdgcn_readfirstlane(first ? 0 : 16);
    const int yg = __builtin_amdgcn_readfirstlane(young ? 1 : 0);
    const int mk = __builtin_amdgcn_readfirstlane(young ? 0x422f : 0x448f);
    ATTN_ASM("s_cbranch_vccz L_py_%=\n\ts_setprio 1\n\tL_py_%=:\n\t", "s_setprio 1\n\t");
}

__global__ __launch_bounds__(512, 2) void attn_kernel(char* __restrict__ ws, unsigned short* __restrict__ out) {
    extern __shared__ __attribute__((aligned(16))) char smem[];
    const unsigned lds0 = (unsigned)(uintptr_t)smem;
    const int tid = threadIdx.x, lane = tid & 63;
    const int w = __builtin_amdgcn_readfirstlane(tid >> 6);
    const int r = lane & 31, h = lane >> 5;
    const int b = blockIdx.x, xcd = b & 7, slot = b >> 3;
    const int isB = (slot < 16);
    const int hd = xcd * 4 + ((slot & 15) >> 2);
    const int hh = isB ? 32 + hd : hd;
    const int kvh = isB ? hd : 32;
    const int tile = (slot & 3) * 16 + 2 * w;

    bf16x8 qa, qb;
    {
        const char* qsrc = ws + WS_Q + (size_t)hh * 65536 + tile * 1024 + lane * 16;
        asm volatile("global_load_dwordx4 %0, %2, off\n\tglobal_load_dwordx4 %1, %2, off offset:1024"
                     : "=&v"(qa), "=&v"(qb) : "v"(qsrc) : "memory");
    }
    const char* kvsrc = ws + WS_KV + (size_t)kvh * 131072 + w * 1024 + lane * 16;
#pragma unroll
    for (int i = 0; i < 4; ++i) glds16(kvsrc + i * 8192, lds0 + A_KV + i * 8192 + w * 1024);
    {
        const unsigned one = ((tid & 15) == 0 || (tid & 15) == 4) ? 0x3f803f80u : 0u;
        u32x4 c = {one, one, one, one};
        *(u32x4*)(smem + A_CONST + tid * 16) = c;
    }
    const unsigned kaddr = lds0 + A_KV + lane * 16;
    const unsigned vaddr = (r < 16) ? (lds0 + A_KV + 1024 + h * 256 + r * 16) : (lds0 + A_CONST + (r - 16) * 16);
    const unsigned vstep = (r < 16) ? 8192u : 0u;

    float ga, gb;
    {
        const float* gsrc = (const float*)(ws + WS_G) + tile * 32 + r;
        asm volatile("global_load_dword %0, %2, off\n\tglobal_load_dword %1, %2, off offset:128" : "=&v"(ga), "=&v"(gb) : "v"(gsrc) : "memory");
    }
    const char* kb = ws + WS_KV + (size_t)kvh * 131072 + 32768 + w * 1024;
    const unsigned kd = lds0 + A_KV + 32768 + w * 1024;
    const int qblk = slot & 3;
    const int mt = qblk * 8 + (hh >> 3), nt = hh & 7;
    char* obase = ws + WS_O + (size_t)tile * 65536 + hh * 1024;
    char* odst = obase + lane * 16;
    unsigned short* optr = out + (size_t)(mt * 64 + ((w >> 1) & 1) * 32 + r) * 512 + nt * 64 + (w >> 2) * 32 + (w & 1) * 16 + h * 8;
    const char* cnt = ws + WS_C + qblk * 256;
    unsigned cv = 0u;
    const char* wb = ws + WS_WO + (size_t)(w * 8) * 16384 + (nt * 2) * 1024;
    u32x16 w0, w1, w2, w3;
    asm volatile("" : "={v[128:143]}"(w0), "={v[144:159]}"(w1), "={v[160:175]}"(w2), "={v[176:191]}"(w3));
    f32x16 zero;
#pragma unroll
    for (int i = 0; i < 16; ++i) zero[i] = 0.f;
    f32x16 na, nb, oa = zero, ob = zero;
    asm volatile("" : "+v"(oa), "+v"(ob), "+s"(kb), "+s"(wb), "+s"(obase), "+v"(cnt));
    asm volatile("s_waitcnt vmcnt(2) lgkmcnt(0)\n\ts_barrier" : "+v"(qa), "+v"(qb) :: "memory");
    {
        const bf16x8 k0 = lds_b128(kaddr);
        const f32x16 sa = MFMA32(k0, qa, zero);
        const f32x16 sb = MFMA32(k0, qb, zero);
        SBAR();
        const float ma = -half_max(max16(sa)) - REFOFF, mb = -half_max(max16(sb)) - REFOFF;
#pragma unroll
        for (int i = 0; i < 16; ++i) { na[i] = ma; nb[i] = mb; }
    }
    attn_main(w >= 4, true, kaddr, vaddr, vstep, qa, qb, na, nb, oa, ob, cnt, lane * 16, kb, kd, wb, obase, w0, w1, w2, w3, cv, optr);
    asm volatile("" : "+v"(ga), "+v"(gb));

    {
        float xa = fabsf(oa[0]), xb = fabsf(ob[0]), ta = oa[0], tb = ob[0];
#pragma unroll
        for (int i = 1; i < 8; ++i) { xa = fmaxf(xa, fabsf(oa[i])); xb = fmaxf(xb, fabsf(ob[i])); ta += oa[i]; tb += ob[i]; }
        const bool bad = !(oa[8] < 1.329228e36f) || !(ob[8] < 1.329228e36f) || !(fabsf(ta) <= 3.0e38f) || !(fabsf(tb) <= 3.0e38f) ||
                         !(xa > 7.7e-34f) || !(xb > 7.7e-34f);
        if (__any(bad)) {
            float ma = -INFINITY, mb = -INFINITY;
#pragma unroll 1
            for (int t = 0; t < 64; ++t) {
                const bf16x8 kf = lds_b128(kaddr + t * 2048);
                ma = fmaxf(ma, max16(MFMA32(kf, qa, zero)));
                mb = fmaxf(mb, max16(MFMA32(kf, qb, zero)));
            }
            ma = -half_max(ma); mb = -half_max(mb);
#pragma unroll
            for (int i = 0; i < 16; ++i) { na[i] = ma; nb[i] = mb; }
            oa = zero; ob = zero;
            attn_main(false, false, kaddr, vaddr, vstep, qa, qb, na, nb, oa, ob, cnt, lane * 16, kb, kd, wb, obase, w0, w1, w2, w3, cv, optr);
        }
    }
    const float sa = (isB ? 1.f - ga : ga) * __builtin_amdgcn_rcpf(oa[8]), sb = (isB ? 1.f - gb : gb) * __builtin_amdgcn_rcpf(ob[8]);
    {
        u32x4 pa = pack8(oa, 0, sa), pb = pack8(ob, 0, sb);
#pragma unroll
        for (int i = 0; i < 4; ++i) { pa[i] = min(pa[i], 0xfffffffeu); pb[i] = min(pb[i], 0xfffffffeu); }
        gst128(odst, pa);
        gst128(odst + 65536, pb);
    }
    for (unsigned spins = 0; spins < SPIN_CAP && (unsigned)__builtin_amdgcn_readfirstlane(cv) < 512u; ++spins) {
        if ((unsigned)__builtin_amdgcn_readfirstlane(__hip_atomic_load((const unsigned*)cnt, __ATOMIC_RELAXED, __HIP_MEMORY_SCOPE_AGENT)) >= 512u) break;
        __builtin_amdgcn_s_sleep(2);
    }
    asm volatile("s_barrier" ::: "memory");
    u32x4 wf[8][2];
#define WSUB(W, k) wf[k][0] = __builtin_shufflevector(W, W, 0, 1, 2, 3); wf[k][1] = __builtin_shufflevector(W, W, 4, 5, 6, 7); \
                   wf[k + 1][0] = __builtin_shufflevector(W, W, 8, 9, 10, 11); wf[k + 1][1] = __builtin_shufflevector(W, W, 12, 13, 14, 15);
    WSUB(w0, 0) WSUB(w1, 2) WSUB(w2, 4) WSUB(w3, 6)
    f32x16 acc[4] = {zero, zero, zero, zero};
    {
        const char* osrc = ws + WS_O + (size_t)(mt * 2) * 65536 + (w * 8) * 1024;
        const unsigned lo16 = lane * 16;
        u32x4 of[8][2];
        auto valid = [&](int k) -> bool {
            const unsigned m = max(max(max(of[k][0][0], of[k][0][1]), max(of[k][0][2], of[k][0][3])),
                                   max(max(of[k][1][0], of[k][1][1]), max(of[k][1][2], of[k][1][3])));
            return !__any(m == 0xffffffffu);
        };
        auto mul = [&](int k) {
#pragma unroll
            for (int q = 0; q < 4; ++q)
                acc[q] = MFMA32(__builtin_bit_cast(bf16x8, wf[k][q >> 1]), __builtin_bit_cast(bf16x8, of[k][q & 1]), acc[q]);
        };
#pragma unroll
        for (int k = 0; k < 8; ++k)
            asm volatile("global_load_dwordx4 %0, %2, %3 offset:%5 sc1\n\tglobal_load_dwordx4 %1, %2, %4 offset:%5 sc1"
                         : "=&v"(of[k][0]), "=&v"(of[k][1])
                         : "v"(lo16), "s"(osrc + (k >> 2) * 4096), "s"(osrc + 65536 + (k >> 2) * 4096), "i"((k & 3) * 1024) : "memory");
        auto settle = [&](int k0, int k1) {
            const unsigned need = ((1u << k1) - 1u) & ~((1u << k0) - 1u);
            for (unsigned spins = 0; spins < SPIN_CAP; ++spins) {
                asm volatile("s_waitcnt vmcnt(0)"
                             : "+v"(of[0][0]), "+v"(of[0][1]), "+v"(of[1][0]), "+v"(of[1][1]), "+v"(of[2][0]), "+v"(of[2][1]), "+v"(of[3][0]), "+v"(of[3][1]),
                               "+v"(of[4][0]), "+v"(of[4][1]), "+v"(of[5][0]), "+v"(of[5][1]), "+v"(of[6][0]), "+v"(of[6][1]), "+v"(of[7][0]), "+v"(of[7][1])
                             :: "memory");
                unsigned pend = 0u;
#pragma unroll
                for (int k = k0; k < 8; ++k) if (!valid(k)) pend |= 1u << k;
                if ((pend & need) == 0u) break;
                __builtin_amdgcn_s_sleep(2);
#pragma unroll
                for (int k = k0; k < 8; ++k)
                    if (pend & (1u << k))
                        asm volatile("global_load_dwordx4 %0, %2, %3 offset:%5 sc1\n\tglobal_load_dwordx4 %1, %2, %4 offset:%5 sc1"
                                     : "+v"(of[k][0]), "+v"(of[k][1])
                                     : "v"(lo16), "s"(osrc + (k >> 2) * 4096), "s"(osrc + 65536 + (k >> 2) * 4096), "i"((k & 3) * 1024) : "memory");
            }
        };
        asm volatile("s_waitcnt vmcnt(8)" : "+v"(of[0][0]), "+v"(of[0][1]), "+v"(of[1][0]), "+v"(of[1][1]), "+v"(of[2][0]), "+v"(of[2][1]),
                                            "+v"(of[3][0]), "+v"(of[3][1]) :: "memory");
        if (!(valid(0) && valid(1) && valid(2) && valid(3))) settle(0, 4);
        mul(0); mul(1); mul(2); mul(3);
        SBAR();
        settle(4, 8);
        mul(4); mul(5); mul(6); mul(7);
    }
    float fin[8];
#pragma unroll
    for (int p = 0; p < 8; ++p) {
        if (p == w) {
#pragma unroll
            for (int i = 0; i < 8; ++i) fin[i] = acc[p >> 1][8 * (p & 1) + i];
        } else {
            char* dst = smem + (p * 7 + ((w - p - 1) & 7)) * 2048 + lane * 16;
#pragma unroll
            for (int g = 0; g < 2; ++g) {
                f32x4 v;
#pragma unroll
                for (int i = 0; i < 4; ++i) v[i] = acc[p >> 1][8 * (p & 1) + 4 * g + i];
                *(f32x4*)(dst + g * 1024) = v;
            }
        }
    }
    asm volatile("s_waitcnt lgkmcnt(0)\n\ts_barrier" ::: "memory");
#pragma unroll
    for (int p = 0; p < 7; ++p)
#pragma unroll
        for (int g = 0; g < 2; ++g) {
            const f32x4 v = *(const f32x4*)(smem + (w * 7 + p) * 2048 + g * 1024 + lane * 16);
#pragma unroll
            for (int i = 0; i < 4; ++i) fin[4 * g + i] += v[i];
        }
    const unsigned a0 = pk_bf16(fin[0], fin[1]), a1 = pk_bf16(fin[2], fin[3]);
    const unsigned b0 = pk_bf16(fin[4], fin[5]), b1 = pk_bf16(fin[6], fin[7]);
    auto r0 = __builtin_amdgcn_permlane32_swap(a0, b0, false, false);
    auto r1 = __builtin_amdgcn_permlane32_swap(a1, b1, false, false);
    u32x4 res = {r0[0], r1[0], r0[1], r1[1]};
    gst128(optr, res);
    asm volatile("pf_text_end:");
}

extern "C" void kernel_launch(void* const* d_in, const int* in_sizes, int n_in, void* d_out, int out_size, void* d_ws,
                              size_t ws_size, hipStream_t stream) {
    const unsigned short* x    = (const unsigned short*)d_in[0];
    const unsigned short* wq   = (const unsigned short*)d_in[1];
    const unsigned short* wk   = (const unsigned short*)d_in[2];
    const unsigned short* wv   = (const unsigned short*)d_in[3];
    const unsigned short* woA  = (const unsigned short*)d_in[4];
    const unsigned short* wqkv = (const unsigned short*)d_in[6];
    const unsigned short* woB  = (const unsigned short*)d_in[7];
    const unsigned short* gw   = (const unsigned short*)d_in[8];
    char* ws = (char*)d_ws;
    (void)hipFuncSetAttribute((const void*)proj_kernel, hipFuncAttributeMaxDynamicSharedMemorySize, P_LDS);
    (void)hipFuncSetAttribute((const void*)attn_kernel, hipFuncAttributeMaxDynamicSharedMemorySize, A_LDS);
    proj_kernel<<<256, 512, P_LDS, stream>>>(x, wq, wk, wv, wqkv, gw, woA, woB, ws);
    attn_kernel<<<256, 512, A_LDS, stream>>>(ws, (unsigned short*)d_out);
}
```
